```python
import torch

def _softmax(x, axis=-1):
    return torch.softmax(x, dim=axis)
def _gelu(x):
    return torch.nn.functional.gelu(x, approximate="tanh")
def _silu(x):
    return torch.nn.functional.silu(x)
def _sigmoid(x):
    return torch.sigmoid(x)
def _layernorm(x, axis=-1, eps=1e-5):
    m = x.mean(dim=axis, keepdim=True)
    v = x.var(dim=axis, keepdim=True, unbiased=False)
    return (x - m) / torch.sqrt(v + eps)
def _scatter_add(tgt, idx, src, axis=0):
    return tgt.clone().index_add_(axis, idx, src)
def _segment_sum(src, seg, n):
    out = torch.zeros((n,) + src.shape[1:], dtype=src.dtype)
    return out.index_add_(0, seg, src)
def _pad(x, pw):
    flat = tuple(v for pair in reversed(pw) for v in pair)
    return torch.nn.functional.pad(x, flat)

kernel_name = 'interleave_layers__ssm_conv_s4d__mixer_channel_mlp__prompt'

def setup_inputs():

    _w = (lambda a: (lambda t: t.to(torch.bfloat16) if t.is_floating_point() else t)(torch.as_tensor(a)))
    def _A_setup():
        Bsz, L, D, N = 1, 2048, 512, 128
        return {
            "u": _w((lambda *s: torch.randn(*s, dtype=torch.bfloat16) * 0.05)(Bsz, L, D)),
            "log_A_real": _w((lambda *s: torch.randn(*s, dtype=torch.bfloat16) * 3.2)(D, N) * 0.5),
            "A_imag": _w((lambda *s: torch.randn(*s, dtype=torch.bfloat16) * 0.05)(D, N) * 3.14),
            "C_real": _w((lambda *s: torch.randn(*s, dtype=torch.bfloat16) * 0.05)(D, N) / N ** 0.5),
            "C_imag": _w((lambda *s: torch.randn(*s, dtype=torch.bfloat16) * 0.05)(D, N) / N ** 0.5),
            "log_dt": _w((lambda *s: torch.randn(*s, dtype=torch.bfloat16) * 0.05)(D) * 0.5 - 4.0),
            "D_skip": _w((lambda *s: torch.randn(*s, dtype=torch.bfloat16) * 0.05)(D)),
        }
    _A_in = _A_setup()

    def _B_setup():
        B, S, D, F = 1, 2048, 512, 1792
        return {
            "x": _w((lambda *s: torch.randn(*s, dtype=torch.bfloat16) * 0.05)(B, S, D)),
            "w1": _w((lambda *s: torch.randn(*s, dtype=torch.bfloat16) * 1.6)(D, F) * (D ** -0.5)),
            "b1": _w((lambda *s: torch.randn(*s, dtype=torch.bfloat16) * 0.4)(F)),
            "w2": _w((lambda *s: torch.randn(*s, dtype=torch.bfloat16) * 0.1)(F, D) * (F ** -0.5)),
            "b2": _w((lambda *s: torch.randn(*s, dtype=torch.bfloat16) * 0.1)(D)),
        }
    _B_in = _B_setup()

    return {
        "A_u": _A_in["u"],
        "A_log_A_real": _A_in["log_A_real"],
        "A_A_imag": _A_in["A_imag"],
        "A_C_real": _A_in["C_real"],
        "A_C_imag": _A_in["C_imag"],
        "A_log_dt": _A_in["log_dt"],
        "A_D_skip": _A_in["D_skip"],
        "B_x": _B_in["x"],
        "B_w1": _B_in["w1"],
        "B_b1": _B_in["b1"],
        "B_w2": _B_in["w2"],
        "B_b2": _B_in["b2"],
    }

def reference(A_u, A_log_A_real, A_A_imag, A_C_real, A_C_imag, A_log_dt, A_D_skip, B_x, B_w1, B_b1, B_w2, B_b2):
    def _A_ref(u, log_A_real, A_imag, C_real, C_imag, log_dt, D_skip):
        L = u.shape[1]
        dt = torch.exp(log_dt)
        A = -torch.exp(log_A_real) + 1j * A_imag
        C = (C_real + 1j * C_imag) * dt[:, None]
        pos = (lambda x, dt: x.to(dt))(torch.arange(L), log_dt.dtype)
        P = torch.exp((A * dt[:, None])[:, :, None] * pos[None, None, :])
        K = 2.0 * torch.einsum("dn,dnl->dl", C, P).real
        L2 = 2 * L
        u_t = (lambda x, *d: x.permute(*d))(u, 0, 2, 1)
        Kf = (lambda x, *a, **k: torch.fft.rfft(x.float(), *a, **k))(K, L2)
        Uf = (lambda x, *a, **k: torch.fft.rfft(x.float(), *a, **k))(u_t, L2)
        y = (lambda x, *a, **k: torch.fft.irfft(x, *a, **k).to(torch.bfloat16))(Uf * Kf[None, :, :], L2)[:, :, :L]
        y = (lambda x, *d: x.permute(*d))(y, 0, 2, 1)
        return y + u * D_skip[None, None, :]

    def _B_ref(x, w1, b1, w2, b2):
        y = _layernorm(x, axis=-1)
        h = _gelu(torch.einsum("bsd,df->bsf", y, w1) + b1)
        return torch.einsum("bsf,fd->bsd", h, w2) + b2

    x = A_u
    x = x + _A_ref(_layernorm(x, axis=-1), A_log_A_real, A_A_imag, A_C_real, A_C_imag, A_log_dt, A_D_skip)
    x = x + _B_ref(_layernorm(x, axis=-1), B_w1, B_b1, B_w2, B_b2)
    x = x + _A_ref(_layernorm(x, axis=-1), A_log_A_real, A_A_imag, A_C_real, A_C_imag, A_log_dt, A_D_skip)
    x = x + _B_ref(_layernorm(x, axis=-1), B_w1, B_b1, B_w2, B_b2)
    return x
```

```cpp
#include <hip/hip_runtime.h>
#include <hip/hip_bf16.h>

typedef unsigned int u32;
typedef unsigned short u16;
typedef __attribute__((ext_vector_type(4))) u32 v4u;
typedef __attribute__((ext_vector_type(2))) u32 v2u;
typedef __attribute__((ext_vector_type(4))) float f32x4;
typedef __attribute__((ext_vector_type(2))) float f32x2;
typedef __attribute__((ext_vector_type(16))) float f32x16;
typedef __attribute__((ext_vector_type(8))) __bf16 bf16x8;
typedef __attribute__((ext_vector_type(2))) __bf16 bf16x2;
typedef __attribute__((ext_vector_type(8))) _Float16 f16x8;
typedef __attribute__((ext_vector_type(2))) _Float16 f16x2;
typedef __attribute__((ext_vector_type(4))) short s16x4;
#define LAS __attribute__((address_space(3)))
#define DI __device__ __forceinline__

constexpr int SL = 2048, DM = 512, NS = 128, FF = 1792;
constexpr int KROW = 2064;
constexpr size_t XT0_OFF = 0, XT1_OFF = 2u << 20, XT2_OFF = 4u << 20, XT3_OFF = 6u << 20;
constexpr size_t KREV_OFF = 8u << 20;
constexpr size_t KSC_OFF = KREV_OFF + (size_t)DM * KROW * 2;
constexpr size_t ST0_OFF = KSC_OFF + 4096;
constexpr size_t ST2_OFF = ST0_OFF + SL * 8;
constexpr size_t W1F_OFF = 11u << 20;
constexpr size_t W2F_OFF = 13u << 20;
constexpr size_t H_OFF = 16u << 20;

DI float bf2f(u32 h) { return __builtin_bit_cast(float, h << 16); }
DI float bflo(u32 p) { return __builtin_bit_cast(float, p << 16); }
DI float bfhi(u32 p) { return __builtin_bit_cast(float, p & 0xffff0000u); }
DI u32 pack_bf16(float a, float b) { bf16x2 v = {(__bf16)a, (__bf16)b}; return __builtin_bit_cast(u32, v); }
DI float bfr(float a) { return bflo(pack_bf16(a, a)); }
DI u32 pack_f16(float a, float b) { f16x2 v = {(_Float16)a, (_Float16)b}; return __builtin_bit_cast(u32, v); }
DI float dot2bf(u32 a, u32 b, float c) {
  return __builtin_amdgcn_fdot2_f32_bf16(__builtin_bit_cast(bf16x2, a), __builtin_bit_cast(bf16x2, b), c, false);
}
DI f32x16 mfma32(v4u a, v4u b, f32x16 c) {
  return __builtin_amdgcn_mfma_f32_32x32x16_bf16(__builtin_bit_cast(bf16x8, a), __builtin_bit_cast(bf16x8, b), c, 0, 0, 0);
}
DI f32x4 mfma16h(v4u a, v4u b, f32x4 c) {
  return __builtin_amdgcn_mfma_f32_16x16x32_f16(__builtin_bit_cast(f16x8, a), __builtin_bit_cast(f16x8, b), c, 0, 0, 0);
}
DI v2u tr_read(const LAS char* p) {
  s16x4 t = __builtin_amdgcn_ds_read_tr16_b64_v4i16((LAS s16x4*)p);
  return __builtin_bit_cast(v2u, t);
}
DI float wave_sum(float v) {
#pragma unroll
  for (int m = 32; m >= 1; m >>= 1) v += __shfl_xor(v, m);
  return v;
}
DI void cexp_p(float arl, float ai, float p, float& re, float& im) {
  const float e = __builtin_amdgcn_exp2f(arl * p);
  const float th = ai * p;
  const float k = __builtin_rintf(th * 0.15915494309189535f);
  float r = __builtin_fmaf(th, 0.15915494309189535f, -k);
  r = __builtin_fmaf(th, 6.4206382e-09f, r);
  re = e * __builtin_amdgcn_cosf(r);
  im = e * __builtin_amdgcn_sinf(r);
}

__global__ __launch_bounds__(512, 2) void k_prep(const u16* __restrict__ x0, const u16* __restrict__ logA,
    const u16* __restrict__ Aim, const u16* __restrict__ Cre, const u16* __restrict__ Cim,
    const u16* __restrict__ logdt, const u16* __restrict__ w1, const u16* __restrict__ w2, char* __restrict__ ws) {
  __shared__ float part[8][768];
  __shared__ float ktab[2][768];
  __shared__ float bnd[8][4];
  __shared__ u32 trt[64 * 33];
  const int b = blockIdx.x, tid = threadIdx.x, l = tid & 63, w = __builtin_amdgcn_readfirstlane(tid >> 6);
  const int ch = 2 * b + (w >> 2), nq = w & 3, kk = l >> 4, i16 = l & 15;
  const int n0 = 32 * nq + 8 * kk;
  const v4u vLA = *(const v4u*)(logA + ch * NS + n0);
  const v4u vAI = *(const v4u*)(Aim + ch * NS + n0);
  const v4u vCR = *(const v4u*)(Cre + ch * NS + n0);
  const v4u vCI = *(const v4u*)(Cim + ch * NS + n0);
  const float ldt = bf2f(logdt[ch]);
  const v4u xr = *(const v4u*)(x0 + (8 * b + w) * DM + 8 * l);
  const v4u xt = *(const v4u*)(x0 + (64 * (b >> 3) + (tid >> 3)) * DM + 64 * (b & 7) + 8 * (tid & 7));
  u32 a1[8], a2[8];
  const int fr = b * 7 + w, h = l >> 5, c32 = l & 31;
  if (w < 7) {
    const int ft = fr >> 5, ks = fr & 31;
#pragma unroll
    for (int j = 0; j < 8; ++j) a1[j] = w1[(16 * ks + 8 * h + j) * FF + 32 * ft + c32];
    const int ct = fr & 1, r1 = fr >> 1, ks2 = r1 % 14, r2 = r1 / 14, wk = r2 & 7, s = r2 >> 3;
#pragma unroll
    for (int j = 0; j < 8; ++j)
      a2[j] = w2[(224 * wk + 16 * ks2 + 8 * (j >> 2) + 4 * h + (j & 3)) * DM + 64 * s + 32 * ct + c32];
  }
  const float dt = bfr(expf(ldt));
  const float pB = (float)(8 * i16);
  const float pA0 = (float)(128 * (i16 >> 3) + (i16 & 7));
  const float pA1 = (float)(i16 < 8 ? 256 + 128 * (i16 >> 2) + 2 * (i16 & 3) : 512 + 128 * ((i16 - 8) >> 1) + 4 * (i16 & 1));
  const float pA2 = (float)(i16 < 8 ? 1024 + 128 * i16 : 2048);
  f32x4 acc0 = {0.f, 0.f, 0.f, 0.f}, acc1 = acc0, acc2 = acc0;
  float bsum = 0.f;
#pragma unroll
  for (int g = 0; g < 8; ++g) {
    const u32 pla = vLA[g >> 1], pai = vAI[g >> 1], pcr = vCR[g >> 1], pci = vCI[g >> 1];
    const float la = (g & 1) ? bfhi(pla) : bflo(pla);
    const float ai = ((g & 1) ? bfhi(pai) : bflo(pai)) * dt;
    const float cr = ((g & 1) ? bfhi(pcr) : bflo(pcr)) * dt * 2.0f;
    const float ci = ((g & 1) ? bfhi(pci) : bflo(pci)) * dt * 2.0f;
    const float arl = -bfr(expf(la)) * dt * 1.4426950408889634f;
    bsum += fabsf(cr) + fabsf(ci);
    float br, bi, er, ei;
    cexp_p(arl, ai, pB, br, bi);
    cexp_p(arl, ai, pA0, er, ei);
    acc0 = __builtin_amdgcn_mfma_f32_16x16x4f32(cr * er - ci * ei, br, acc0, 0, 0, 0);
    acc0 = __builtin_amdgcn_mfma_f32_16x16x4f32(-(cr * ei + ci * er), bi, acc0, 0, 0, 0);
    cexp_p(arl, ai, pA1, er, ei);
    acc1 = __builtin_amdgcn_mfma_f32_16x16x4f32(cr * er - ci * ei, br, acc1, 0, 0, 0);
    acc1 = __builtin_amdgcn_mfma_f32_16x16x4f32(-(cr * ei + ci * er), bi, acc1, 0, 0, 0);
    cexp_p(arl, ai, pA2, er, ei);
    acc2 = __builtin_amdgcn_mfma_f32_16x16x4f32(cr * er - ci * ei, br, acc2, 0, 0, 0);
    acc2 = __builtin_amdgcn_mfma_f32_16x16x4f32(-(cr * ei + ci * er), bi, acc2, 0, 0, 0);
  }
#pragma unroll
  for (int r = 0; r < 4; ++r) {
    part[w][(4 * kk + r) * 16 + i16] = acc0[r];
    part[w][256 + (4 * kk + r) * 16 + i16] = acc1[r];
    part[w][512 + (4 * kk + r) * 16 + i16] = acc2[r];
  }
  if (i16 == 0) bnd[w][kk] = bsum;
#pragma unroll
  for (int j = 0; j < 4; ++j) trt[(tid >> 3) * 33 + 4 * (tid & 7) + j] = xt[j];
  {
    float s = 0.f, q = 0.f;
#pragma unroll
    for (int j = 0; j < 4; ++j) { const u32 p = xr[j]; s = dot2bf(p, 0x3f803f80u, s); q = dot2bf(p, p, q); }
    s = wave_sum(s); q = wave_sum(q);
    if (l == 0) *(f32x2*)(ws + ST0_OFF + (size_t)(8 * b + w) * 8) = f32x2{s, q};
  }
  if (w < 7) {
    v4u o1, o2;
#pragma unroll
    for (int j = 0; j < 4; ++j) { o1[j] = a1[2 * j] | (a1[2 * j + 1] << 16); o2[j] = a2[2 * j] | (a2[2 * j + 1] << 16); }
    *(v4u*)(ws + W1F_OFF + ((size_t)fr * 64 + l) * 16) = o1;
    *(v4u*)(ws + W2F_OFF + ((size_t)fr * 64 + l) * 16) = o2;
  }
  __syncthreads();
#pragma unroll
  for (int i = 0; i < 3; ++i) {
    const int e = tid + 512 * i, c = e >= 768 ? 1 : 0, x = e - 768 * c;
    ktab[c][x] = (part[4 * c][x] + part[4 * c + 1][x]) + (part[4 * c + 2][x] + part[4 * c + 3][x]);
  }
  {
    const int cc = tid >> 3, oct = tid & 7;
    const u16* t16 = (const u16*)trt;
    v4u o;
#pragma unroll
    for (int j = 0; j < 4; ++j)
      o[j] = (u32)t16[(8 * oct + 2 * j) * 66 + cc] | ((u32)t16[(8 * oct + 2 * j + 1) * 66 + cc] << 16);
    *(v4u*)(ws + XT0_OFF + ((size_t)(64 * (b & 7) + cc) * SL + 64 * (b >> 3) + 8 * oct) * 2) = o;
  }
  __syncthreads();
  {
    const int c = tid >> 8, un = tid & 255;
    float bs = 0.f;
#pragma unroll
    for (int i = 0; i < 4; ++i) bs += (bnd[4 * c + i][0] + bnd[4 * c + i][1]) + (bnd[4 * c + i][2] + bnd[4 * c + i][3]);
    int ex = (int)((__builtin_bit_cast(u32, bs) >> 23) & 0xff) - 126;
    int e = 15 - ex; e = e > 100 ? 100 : (e < -100 ? -100 : e);
    const float sc = __builtin_bit_cast(float, (u32)(127 + e) << 23);
    float kv[8];
#pragma unroll
    for (int j = 0; j < 8; ++j) {
      const int lag = 2047 - 8 * un - j;
      const int p = (int)bfr((float)lag);
      const int q = p & 127, col = q >> 3, i = p >> 7;
      int idx;
      if (p < 256) idx = (8 * i + (q & 7)) * 16 + col;
      else if (p < 512) idx = 256 + (4 * (i - 2) + ((q & 7) >> 1)) * 16 + col;
      else if (p < 1024) idx = 256 + (8 + 2 * (i - 4) + ((q & 7) >> 2)) * 16 + col;
      else idx = 512 + (i - 8) * 16 + col;
      kv[j] = ktab[c][idx] * sc;
    }
    v4u o;
#pragma unroll
    for (int j = 0; j < 4; ++j) o[j] = pack_f16(kv[2 * j], kv[2 * j + 1]);
    char* krow = ws + KREV_OFF + (size_t)(2 * b + c) * KROW * 2;
    *(v4u*)(krow + un * 16) = o;
    if (un < 2) *(v4u*)(krow + 4096 + un * 16) = v4u{0u, 0u, 0u, 0u};
    if (un == 0) *(float*)(ws + KSC_OFF + (size_t)(2 * b + c) * 4) = __builtin_bit_cast(float, (u32)(127 - e) << 23);
  }
}

constexpr int KC_CH = 8 * 260 * 16;
constexpr int CV_U = 2 * KC_CH;
constexpr int CV_P = CV_U + 2 * 4608;
constexpr int CV_LDS = CV_P + 4 * 8192;
template <int NP>
__global__ __launch_bounds__(256, 1) void k_conv(const u16* __restrict__ xin, const f32x4* __restrict__ st,
    const u16* __restrict__ krev, const float* __restrict__ ksc, const u16* __restrict__ dskip, u16* __restrict__ xout) {
  extern __shared__ __attribute__((aligned(16))) char sm[];
  const int b = blockIdx.x, tid = threadIdx.x, l = tid & 63, w = __builtin_amdgcn_readfirstlane(tid >> 6);
  v4u xv[2], k0[2], k1[2];
#pragma unroll
  for (int c = 0; c < 2; ++c) {
    k0[c] = *(const v4u*)(krev + (2 * b + c) * KROW + 8 * (255 - tid));
    k1[c] = *(const v4u*)(krev + (2 * b + c) * KROW + 8 * (256 - tid));
  }
#pragma unroll
  for (int c = 0; c < 2; ++c) xv[c] = *(const v4u*)(xin + (2 * b + c) * SL + 8 * tid);
  f32x4 sv[NP][4];
#pragma unroll
  for (int p = 0; p < NP; ++p)
#pragma unroll
    for (int j = 0; j < 4; ++j) sv[p][j] = st[p * (SL / 2) + 4 * tid + j];
  const u32 dsk = *(const u32*)(dskip + 2 * b);
  const f32x2 ks2 = *(const f32x2*)(ksc + 2 * b);
#pragma unroll
  for (int c = 0; c < 2; ++c) {
    u32 wd[8];
#pragma unroll
    for (int j = 0; j < 4; ++j) { wd[j] = k0[c][j]; wd[4 + j] = k1[c][j]; }
#pragma unroll
    for (int b0 = 0; b0 < 8; ++b0) {
      const int sg = 7 - b0;
      v4u o;
#pragma unroll
      for (int k = 0; k < 4; ++k)
        o[k] = (sg & 1) ? __builtin_amdgcn_alignbit(wd[(sg + 1) / 2 + k], wd[(sg - 1) / 2 + k], 16) : wd[sg / 2 + k];
      *(v4u*)(sm + c * KC_CH + b0 * 4160 + (tid + 1) * 16) = o;
    }
  }
  if (tid < 16) *(v4u*)(sm + (tid >> 3) * KC_CH + (tid & 7) * 4160) = v4u{0u, 0u, 0u, 0u};
  if (tid >= 64 && tid < 128) *(v4u*)(sm + CV_U + ((tid >> 5) & 1) * 4608 + (tid & 31) * 16) = v4u{0u, 0u, 0u, 0u};
  float mb[8], rs[8];
#pragma unroll
  for (int j = 0; j < 4; ++j) {
    f32x4 a = sv[0][j];
#pragma unroll
    for (int p = 1; p < NP; ++p) a += sv[p][j];
#pragma unroll
    for (int e = 0; e < 2; ++e) {
      const float m = a[2 * e] * (1.0f / DM);
      const float v = fmaxf(a[2 * e + 1] * (1.0f / DM) - m * m, 0.f);
      mb[2 * j + e] = bfr(m);
      rs[2 * j + e] = 1.0f / bfr(sqrtf(bfr(bfr(v) + 1e-5f)));
    }
  }
  u32 ub[2][4];
#pragma unroll
  for (int c = 0; c < 2; ++c) {
    v4u o;
#pragma unroll
    for (int j = 0; j < 4; ++j) {
      const u32 p = xv[c][j];
      const u32 d = pack_bf16(bflo(p) - mb[2 * j], bfhi(p) - mb[2 * j + 1]);
      const u32 u = pack_bf16(bflo(d) * rs[2 * j], bfhi(d) * rs[2 * j + 1]);
      ub[c][j] = u;
      o[j] = pack_f16(bflo(u), bfhi(u));
    }
    *(v4u*)(sm + CV_U + c * 4608 + 512 + tid * 16) = o;
  }
  __syncthreads();
  {
    const int c = w >> 1, hf = w & 1, b16 = l & 15, g = l >> 4;
    const char* pa = sm + c * KC_CH + (b16 & 7) * 4160 + (3 + (b16 >> 3) - g) * 16 + hf * 256;
    const char* pb = sm + CV_U + c * 4608 + (30 + 2 * b16 + g) * 16 - hf * 256;
    f32x4 acc[8];
#pragma unroll
    for (int i = 0; i < 8; ++i) acc[i] = f32x4{0.f, 0.f, 0.f, 0.f};
#pragma unroll
    for (int tau = 0; tau < 4; ++tau) {
      v4u fa[8], fb[8];
#pragma unroll
      for (int i = 0; i < 8; ++i) {
        fa[i] = *(const v4u*)(pa + i * 512 + tau * 64);
        fb[i] = *(const v4u*)(pb + i * 512 - tau * 64);
      }
#pragma unroll
      for (int s = 0; s < 8; ++s)
#pragma unroll
        for (int e = 0; e + s < 8; ++e) acc[s + e] = mfma16h(fa[s], fb[e], acc[s + e]);
    }
    const int sl = (4 * b16 + g) ^ ((b16 >> 1) & 3);
#pragma unroll
    for (int i = 0; i < 8; ++i) *(f32x4*)(sm + CV_P + w * 8192 + i * 1024 + sl * 16) = acc[i];
  }
  __syncthreads();
#pragma unroll
  for (int c = 0; c < 2; ++c) {
    const int s0 = (2 * tid) ^ ((tid >> 2) & 3), s1 = (2 * tid + 1) ^ ((tid >> 2) & 3);
    const f32x4 y0 = *(const f32x4*)(sm + CV_P + (2 * c) * 8192 + s0 * 16) + *(const f32x4*)(sm + CV_P + (2 * c + 1) * 8192 + s0 * 16);
    const f32x4 y1 = *(const f32x4*)(sm + CV_P + (2 * c) * 8192 + s1 * 16) + *(const f32x4*)(sm + CV_P + (2 * c + 1) * 8192 + s1 * 16);
    const float sc = ks2[c], dk = c ? bfhi(dsk) : bflo(dsk);
    v4u o;
#pragma unroll
    for (int j = 0; j < 4; ++j) {
      const float ya = (j < 2 ? y0[2 * j] : y1[2 * j - 4]) * sc, yb = (j < 2 ? y0[2 * j + 1] : y1[2 * j - 3]) * sc;
      const u32 yp = pack_bf16(ya, yb);
      const u32 tp = pack_bf16(bflo(ub[c][j]) * dk, bfhi(ub[c][j]) * dk);
      const u32 sp = pack_bf16(bflo(yp) + bflo(tp), bfhi(yp) + bfhi(tp));
      const u32 xp = xv[c][j];
      o[j] = pack_bf16(bflo(xp) + bflo(sp), bfhi(xp) + bfhi(sp));
    }
    *(v4u*)(xout + (2 * b + c) * SL + 8 * tid) = o;
  }
}

DI float gelu_t(float x) {
  const float t = x * __builtin_fmaf(x * x, -0.10294324f, -2.3022082f);
  return x * __builtin_amdgcn_rcpf(1.0f + __builtin_amdgcn_exp2f(t));
}
constexpr int G1_AB = 65536, G1_CS = G1_AB + 512, G1_LDS = G1_CS + 7 * 128;
__global__ __launch_bounds__(512, 2) void k_gemm1(const u16* __restrict__ xT, const v4u* __restrict__ w1f,
    const u16* __restrict__ b1, v4u* __restrict__ H) {
  extern __shared__ __attribute__((aligned(16))) char sm[];
  const int b = blockIdx.x, tid = threadIdx.x, l = tid & 63, w = __builtin_amdgcn_readfirstlane(tid >> 6);
  const int t = 4 * (b & 7) + (b >> 6), s = (b >> 3) & 7;
  const int h = l >> 5;
  v4u xs[8];
#pragma unroll
  for (int i = 0; i < 8; ++i) xs[i] = *(const v4u*)(xT + (64 * i + (tid >> 3)) * SL + 64 * t + 8 * (tid & 7));
  v4u wf[32];
  const int ft = 7 * s + (w < 7 ? w : 0);
  if (w < 7) {
#pragma unroll
    for (int ks = 0; ks < 32; ++ks) wf[ks] = w1f[(ft * 32 + ks) * 64 + l];
  }
  v2u bb[4];
#pragma unroll
  for (int q = 0; q < 4; ++q) bb[q] = *(const v2u*)(b1 + 32 * ft + 8 * q + 4 * h);
  __builtin_amdgcn_sched_barrier(0);
  {
    const int row = tid >> 3, chn = (tid & 7) ^ (((row >> 1) & 1) << 2);
#pragma unroll
    for (int i = 0; i < 8; ++i) *(v4u*)(sm + (64 * i + row) * 128 + chn * 16) = xs[i];
  }
  __syncthreads();
  const int q4 = (l & 15) >> 2;
  const LAS char* xb0 = (const LAS char*)sm + (8 * h + q4) * 128 + ((32 * ((l >> 4) & 1) + 8 * (l & 3)) ^ (((q4 >> 1) & 1) << 6));
  const LAS char* xb1 = (const LAS char*)sm + (8 * h + q4) * 128 + ((64 + 32 * ((l >> 4) & 1) + 8 * (l & 3)) ^ (((q4 >> 1) & 1) << 6));
  f32x16 acc0 = {}, acc1 = {};
  if (w < 7) {
    float cs = 0.f;
#pragma unroll
    for (int ks = 0; ks < 32; ++ks) {
      const v2u a0 = tr_read(xb0 + ks * 2048), a1 = tr_read(xb0 + ks * 2048 + 512);
      const v2u c0 = tr_read(xb1 + ks * 2048), c1 = tr_read(xb1 + ks * 2048 + 512);
      acc0 = mfma32(wf[ks], v4u{a0.x, a0.y, a1.x, a1.y}, acc0);
      acc1 = mfma32(wf[ks], v4u{c0.x, c0.y, c1.x, c1.y}, acc1);
#pragma unroll
      for (int j = 0; j < 4; ++j) { const u32 p = wf[ks][j]; cs = dot2bf(p, 0x3f803f80u, cs); }
    }
    cs += __shfl_xor(cs, 32);
    if (l < 32) *(float*)(sm + G1_CS + w * 128 + l * 4) = cs;
  } else {
    float s0 = 0.f, q0 = 0.f, s1 = 0.f, q1 = 0.f;
#pragma unroll 8
    for (int ks = 0; ks < 32; ++ks) {
      const v2u a0 = tr_read(xb0 + ks * 2048), a1 = tr_read(xb0 + ks * 2048 + 512);
      const v2u c0 = tr_read(xb1 + ks * 2048), c1 = tr_read(xb1 + ks * 2048 + 512);
      const u32 e0[4] = {a0.x, a0.y, a1.x, a1.y}, e1[4] = {c0.x, c0.y, c1.x, c1.y};
#pragma unroll
      for (int j = 0; j < 4; ++j) {
        s0 = dot2bf(e0[j], 0x3f803f80u, s0); q0 = dot2bf(e0[j], e0[j], q0);
        s1 = dot2bf(e1[j], 0x3f803f80u, s1); q1 = dot2bf(e1[j], e1[j], q1);
      }
    }
    s0 += __shfl_xor(s0, 32); q0 += __shfl_xor(q0, 32); s1 += __shfl_xor(s1, 32); q1 += __shfl_xor(q1, 32);
    const float sS = h ? s1 : s0, sQ = h ? q1 : q0;
    const float m = sS * (1.0f / DM);
    const float v = fmaxf(sQ * (1.0f / DM) - m * m, 0.f);
    const float mbf = bfr(m), sb = bfr(sqrtf(bfr(bfr(v) + 1e-5f)));
    const float r1 = 1.0f / sb;
    const float m2 = bfr((m - mbf) * r1), v2 = bfr(v * r1 * r1);
    const float r2 = 1.0f / bfr(sqrtf(bfr(v2 + 1e-5f)));
    *(f32x2*)(sm + G1_AB + l * 8) = f32x2{r1 * r2, (mbf * r1 + m2) * r2};
  }
  __syncthreads();
  if (w < 7) {
    f32x4 cs4[4];
#pragma unroll
    for (int q = 0; q < 4; ++q) cs4[q] = *(const f32x4*)(sm + G1_CS + w * 128 + (8 * q + 4 * h) * 4);
#pragma unroll
    for (int tt = 0; tt < 2; ++tt) {
      const f32x2 ab = *(const f32x2*)(sm + G1_AB + (32 * tt + (l & 31)) * 8);
#pragma unroll
      for (int sp = 0; sp < 2; ++sp) {
        v4u o;
#pragma unroll
        for (int j = 0; j < 4; ++j) {
          const int r = 8 * sp + 2 * j, q = r >> 2;
          const float x0 = (tt ? acc1[r] : acc0[r]) * ab.x - ab.y * cs4[q][r & 3];
          const float x1 = (tt ? acc1[r + 1] : acc0[r + 1]) * ab.x - ab.y * cs4[q][(r + 1) & 3];
          const u32 gp = pack_bf16(x0, x1);
          const u32 bp = bb[q][(r >> 1) & 1];
          const u32 g2 = pack_bf16(bflo(gp) + bflo(bp), bfhi(gp) + bfhi(bp));
          o[j] = pack_bf16(gelu_t(bflo(g2)), gelu_t(bfhi(g2)));
        }
        H[(((t * 8 + s) * 14 + 2 * w + sp) * 2 + tt) * 64 + l] = o;
      }
    }
  }
}

constexpr int G2_ROW = 272, G2_LDS = 8 * 64 * G2_ROW;
template <bool LAST>
__global__ __launch_bounds__(512, 2) void k_gemm2(const v4u* __restrict__ H, const v4u* __restrict__ w2f,
    const u16* __restrict__ b2, const u16* __restrict__ xT, u16* __restrict__ xout, float* __restrict__ stp) {
  extern __shared__ __attribute__((aligned(16))) char sm[];
  const int b = blockIdx.x, tid = threadIdx.x, l = tid & 63, w = __builtin_amdgcn_readfirstlane(tid >> 6);
  const int t = 4 * (b & 7) + (b >> 6), s = (b >> 3) & 7;
  const int h = l >> 5;
  constexpr int WIN = 10;
  const v4u* hp = H + ((t * 8 + w) * 28) * 64 + l;
  const v4u* wp = w2f + ((s * 8 + w) * 28) * 64 + l;
  v4u fh[WIN][2], fw[WIN][2];
#pragma unroll
  for (int k = 0; k < WIN; ++k) {
    fh[k][0] = hp[(2 * k) * 64]; fh[k][1] = hp[(2 * k + 1) * 64];
    fw[k][0] = wp[(2 * k) * 64]; fw[k][1] = wp[(2 * k + 1) * 64];
  }
  const v4u xo = *(const v4u*)(xT + (64 * s + l) * SL + 64 * t + 8 * w);
  const float bias = bf2f(b2[64 * s + l]);
  __builtin_amdgcn_sched_barrier(0);
  f32x16 acc[2][2] = {};
#pragma unroll
  for (int k = 0; k < 14; ++k) {
    const v4u h0 = fh[k % WIN][0], h1 = fh[k % WIN][1], w0 = fw[k % WIN][0], w1 = fw[k % WIN][1];
    if (k + WIN < 14) {
      fh[k % WIN][0] = hp[(2 * (k + WIN)) * 64]; fh[k % WIN][1] = hp[(2 * (k + WIN) + 1) * 64];
      fw[k % WIN][0] = wp[(2 * (k + WIN)) * 64]; fw[k % WIN][1] = wp[(2 * (k + WIN) + 1) * 64];
    }
    acc[0][0] = mfma32(h0, w0, acc[0][0]);
    acc[0][1] = mfma32(h0, w1, acc[0][1]);
    acc[1][0] = mfma32(h1, w0, acc[1][0]);
    acc[1][1] = mfma32(h1, w1, acc[1][1]);
    __builtin_amdgcn_sched_barrier(0);
  }
#pragma unroll
  for (int tt = 0; tt < 2; ++tt)
#pragma unroll
    for (int ct = 0; ct < 2; ++ct)
#pragma unroll
      for (int q = 0; q < 4; ++q)
        *(f32x4*)(sm + (w * 64 + 32 * ct + (l & 31)) * G2_ROW + (32 * tt + 8 * q + 4 * h) * 4) =
            f32x4{acc[tt][ct][4 * q], acc[tt][ct][4 * q + 1], acc[tt][ct][4 * q + 2], acc[tt][ct][4 * q + 3]};
  __syncthreads();
  f32x4 o0 = {0.f, 0.f, 0.f, 0.f}, o1 = o0;
#pragma unroll
  for (int ww = 0; ww < 8; ++ww) {
    o0 += *(const f32x4*)(sm + (ww * 64 + l) * G2_ROW + w * 32);
    o1 += *(const f32x4*)(sm + (ww * 64 + l) * G2_ROW + w * 32 + 16);
  }
  v4u o;
#pragma unroll
  for (int j = 0; j < 4; ++j) {
    const u32 op = pack_bf16(j < 2 ? o0[2 * j] : o1[2 * j - 4], j < 2 ? o0[2 * j + 1] : o1[2 * j - 3]);
    const u32 o2 = pack_bf16(bflo(op) + bias, bfhi(op) + bias);
    const u32 xp = xo[j];
    o[j] = pack_bf16(bflo(xp) + bflo(o2), bfhi(xp) + bfhi(o2));
  }
  if (LAST) {
#pragma unroll
    for (int j = 0; j < 4; ++j) {
      const u32 p = o[j];
      xout[(64 * t + 8 * w + 2 * j) * DM + 64 * s + l] = (u16)(p & 0xffffu);
      xout[(64 * t + 8 * w + 2 * j + 1) * DM + 64 * s + l] = (u16)(p >> 16);
    }
  } else {
    *(v4u*)(xout + (64 * s + l) * SL + 64 * t + 8 * w) = o;
    float v[16];
#pragma unroll
    for (int j = 0; j < 4; ++j) {
      const u32 p = o[j];
      const float a = bflo(p), c = bfhi(p);
      v[4 * j] = a; v[4 * j + 1] = a * a; v[4 * j + 2] = c; v[4 * j + 3] = c * c;
    }
#pragma unroll
    for (int st = 0; st < 4; ++st) {
      const int n = 8 >> st, m = 32 >> st;
      const bool up = (l & m) != 0;
#pragma unroll
      for (int i = 0; i < n; ++i) {
        const float keep = up ? v[i + n] : v[i], give = up ? v[i] : v[i + n];
        v[i] = keep + __shfl_xor(give, m);
      }
    }
    v[0] += __shfl_xor(v[0], 2);
    v[0] += __shfl_xor(v[0], 1);
    if ((l & 3) == 0) stp[(size_t)s * (2 * SL) + (64 * t + 8 * w) * 2 + (l >> 2)] = v[0];
  }
}

extern "C" void kernel_launch(void* const* d_in, const int* in_sizes, int n_in, void* d_out, int out_size, void* d_ws,
                              size_t ws_size, hipStream_t stream) {
  const u16* x0 = (const u16*)d_in[0];
  const u16* logA = (const u16*)d_in[1];
  const u16* Aim = (const u16*)d_in[2];
  const u16* Cre = (const u16*)d_in[3];
  const u16* Cim = (const u16*)d_in[4];
  const u16* logdt = (const u16*)d_in[5];
  const u16* dskip = (const u16*)d_in[6];
  const u16* w1 = (const u16*)d_in[8];
  const u16* b1 = (const u16*)d_in[9];
  const u16* w2 = (const u16*)d_in[10];
  const u16* b2 = (const u16*)d_in[11];
  char* ws = (char*)d_ws;
  (void)hipFuncSetAttribute((const void*)k_conv<1>, hipFuncAttributeMaxDynamicSharedMemorySize, CV_LDS);
  (void)hipFuncSetAttribute((const void*)k_conv<8>, hipFuncAttributeMaxDynamicSharedMemorySize, CV_LDS);
  (void)hipFuncSetAttribute((const void*)k_gemm1, hipFuncAttributeMaxDynamicSharedMemorySize, G1_LDS);
  (void)hipFuncSetAttribute((const void*)k_gemm2<false>, hipFuncAttributeMaxDynamicSharedMemorySize, G2_LDS);
  (void)hipFuncSetAttribute((const void*)k_gemm2<true>, hipFuncAttributeMaxDynamicSharedMemorySize, G2_LDS);
  u16* xt0 = (u16*)(ws + XT0_OFF); u16* xt1 = (u16*)(ws + XT1_OFF); u16* xt2 = (u16*)(ws + XT2_OFF); u16* xt3 = (u16*)(ws + XT3_OFF);
  const u16* krev = (const u16*)(ws + KREV_OFF);
  const float* ksc = (const float*)(ws + KSC_OFF);
  const v4u* w1f = (const v4u*)(ws + W1F_OFF);
  const v4u* w2f = (const v4u*)(ws + W2F_OFF);
  v4u* H = (v4u*)(ws + H_OFF);
  k_prep<<<256, 512, 0, stream>>>(x0, logA, Aim, Cre, Cim, logdt, w1, w2, ws);
  k_conv<1><<<256, 256, CV_LDS, stream>>>(xt0, (const f32x4*)(ws + ST0_OFF), krev, ksc, dskip, xt1);
  k_gemm1<<<256, 512, G1_LDS, stream>>>(xt1, w1f, b1, H);
  k_gemm2<false><<<256, 512, G2_LDS, stream>>>(H, w2f, b2, xt1, xt2, (float*)(ws + ST2_OFF));
  k_conv<8><<<256, 256, CV_LDS, stream>>>(xt2, (const f32x4*)(ws + ST2_OFF), krev, ksc, dskip, xt3);
  k_gemm1<<<256, 512, G1_LDS, stream>>>(xt3, w1f, b1, H);
  k_gemm2<true><<<256, 512, G2_LDS, stream>>>(H, w2f, b2, xt3, (u16*)d_out, nullptr);
}
```

```cpp
#include <hip/hip_runtime.h>
#include <hip/hip_bf16.h>

typedef unsigned int u32;
typedef unsigned short u16;
typedef __attribute__((ext_vector_type(4))) u32 v4u;
typedef __attribute__((ext_vector_type(2))) u32 v2u;
typedef __attribute__((ext_vector_type(4))) float f32x4;
typedef __attribute__((ext_vector_type(2))) float f32x2;
typedef __attribute__((ext_vector_type(16))) float f32x16;
typedef __attribute__((ext_vector_type(8))) __bf16 bf16x8;
typedef __attribute__((ext_vector_type(2))) __bf16 bf16x2;
typedef __attribute__((ext_vector_type(8))) _Float16 f16x8;
typedef __attribute__((ext_vector_type(2))) _Float16 f16x2;
typedef __attribute__((ext_vector_type(4))) short s16x4;
#define LAS __attribute__((address_space(3)))
#define DI __device__ __forceinline__
#define STAMP(i)
#define STAMP_FLUSH(kid)

constexpr int SL = 2048, DM = 512, NS = 128, FF = 1792;
constexpr int KROW = 2064;
constexpr int XQ_OFF = 0;
constexpr int XM_OFF = 4 << 20;
constexpr int KREV_OFF = 8 << 20;
constexpr int KSC_OFF = KREV_OFF + DM * KROW * 2;
constexpr int W1F_OFF = 11 << 20;
constexpr int W2F_OFF = 13 << 20;
constexpr int FST_OFF = 15 << 20;
constexpr int CTL_OFF = FST_OFF + 16384;
constexpr int CNT_OFF = CTL_OFF;
constexpr int HF_OFF = CNT_OFF + 40960;
constexpr int PST_OFF = HF_OFF + 16384;
constexpr int CTL_BYTES = 40960 + 16384 + 262144;
constexpr int H_OFF = 16 << 20;
constexpr int H_LAYER = 32 * 8 * 2 * 14 * 1024;
constexpr int WS_BYTES = 32 << 20;
constexpr int SC1 = 16;
constexpr int SPIN_CAP = 1 << 16;

DI float bf2f(u32 h) { return __builtin_bit_cast(float, h << 16); }
DI float bflo(u32 p) { return __builtin_bit_cast(float, p << 16); }
DI float bfhi(u32 p) { return __builtin_bit_cast(float, p & 0xffff0000u); }
DI u32 pack_bf16(float a, float b) { bf16x2 v = {(__bf16)a, (__bf16)b}; return __builtin_bit_cast(u32, v); }
DI float bfr(float a) { return bflo(pack_bf16(a, a)); }
DI u32 pack_f16(float a, float b) { f16x2 v = {(_Float16)a, (_Float16)b}; return __builtin_bit_cast(u32, v); }
DI float dot2bf(u32 a, u32 b, float c) {
  return __builtin_amdgcn_fdot2_f32_bf16(__builtin_bit_cast(bf16x2, a), __builtin_bit_cast(bf16x2, b), c, false);
}
DI f32x16 mfma32(v4u a, v4u b, f32x16 c) {
  return __builtin_amdgcn_mfma_f32_32x32x16_bf16(__builtin_bit_cast(bf16x8, a), __builtin_bit_cast(bf16x8, b), c, 0, 0, 0);
}
DI f32x4 mfma16h(v4u a, v4u b, f32x4 c) {
  return __builtin_amdgcn_mfma_f32_16x16x32_f16(__builtin_bit_cast(f16x8, a), __builtin_bit_cast(f16x8, b), c, 0, 0, 0);
}
DI v4u tr_frag(const LAS char* p) {
  const s16x4 a = __builtin_amdgcn_ds_read_tr16_b64_v4i16((LAS s16x4*)p);
  const s16x4 c = __builtin_amdgcn_ds_read_tr16_b64_v4i16((LAS s16x4*)(p + 256));
  return __builtin_bit_cast(v4u, __builtin_shufflevector(a, c, 0, 1, 2, 3, 4, 5, 6, 7));
}
template <int CTRL> DI float dpp_add(float v) {
  const int o = __builtin_amdgcn_update_dpp(0, __builtin_bit_cast(int, v), CTRL, 0xf, 0xf, false);
  return v + __builtin_bit_cast(float, o);
}
DI float row_sum16(float v) { v = dpp_add<0x128>(v); v = dpp_add<0x124>(v); v = dpp_add<0x122>(v); return dpp_add<0x121>(v); }
DI float swap32_add(float a, float c) {
  const auto r = __builtin_amdgcn_permlane32_swap(__builtin_bit_cast(u32, a), __builtin_bit_cast(u32, c), false, false);
  const u32 r0 = r[0], r1 = r[1];
  return __builtin_bit_cast(float, r0) + __builtin_bit_cast(float, r1);
}
DI float swap16_add(float a, float c) {
  const auto r = __builtin_amdgcn_permlane16_swap(__builtin_bit_cast(u32, a), __builtin_bit_cast(u32, c), false, false);
  const u32 r0 = r[0], r1 = r[1];
  return __builtin_bit_cast(float, r0) + __builtin_bit_cast(float, r1);
}
DI float wave_sum(float v) { v = row_sum16(v); v = swap16_add(v, v); return swap32_add(v, v); }
DI u32 ln_pack(float s, float q) {
  const float m = s * (1.0f / DM);
  const float v = fmaxf(q * (1.0f / DM) - m * m, 0.f);
  return min(pack_bf16(m, __builtin_sqrtf(bfr(bfr(v) + 1e-5f))), 0xfffffffeu);
}
#define LD128(vo, so, aux) __builtin_amdgcn_raw_buffer_load_b128(rs, (vo), (so), (aux))
#define LD64(vo, so, aux) __builtin_amdgcn_raw_buffer_load_b64(rs, (vo), (so), (aux))
#define LD32(vo, so, aux) __builtin_amdgcn_raw_buffer_load_b32(rs, (vo), (so), (aux))
#define ST128(v, vo, so, aux) __builtin_amdgcn_raw_buffer_store_b128((v), rs, (vo), (so), (aux))
#define ST64(v, vo, so, aux) __builtin_amdgcn_raw_buffer_store_b64((v), rs, (vo), (so), (aux))
#define ST32(v, vo, so, aux) __builtin_amdgcn_raw_buffer_store_b32((v), rs, (vo), (so), (aux))
#define DRAIN() asm volatile("s_waitcnt vmcnt(0)" ::: "memory")
#define CBAR() asm volatile("" ::: "memory")
#define SBAR() __builtin_amdgcn_sched_barrier(0)
DI int opaque_v(int x) { asm volatile("" : "+v"(x)); return x; }
template <typename T, int N> DI void undef_regs(T (&a)[N]) {
#pragma unroll
  for (int i = 0; i < N; ++i) asm volatile("" : "=v"(a[i]));
}
DI int opaque_s(int x) { asm volatile("" : "+s"(x)); return x; }

DI void cexp_p(float arl, float rh, float rl, float p, float& re, float& im) {
  const float e = __builtin_amdgcn_exp2f(arl * p);
  const float r = __builtin_fmaf(rl, p, __builtin_amdgcn_fractf(rh * p));
  re = e * __builtin_amdgcn_cosf(r);
  im = e * __builtin_amdgcn_sinf(r);
}
DI float k_scale(float bs, int sign) {
  const int ex = (int)((__builtin_bit_cast(u32, bs) >> 23) & 0xff) - 126;
  int e = 15 - ex; e = e > 100 ? 100 : (e < -100 ? -100 : e);
  return __builtin_bit_cast(float, (u32)(127 + sign * e) << 23);
}
__global__ __launch_bounds__(512, 2) void k_prep(const u16* __restrict__ x0, const u16* __restrict__ logA,
    const u16* __restrict__ Aim, const u16* __restrict__ Cre, const u16* __restrict__ Cim,
    const u16* __restrict__ logdt, const u16* __restrict__ w1, const u16* __restrict__ w2, char* __restrict__ ws) {
  __shared__ __attribute__((aligned(16))) float part[8][768];
  __shared__ __attribute__((aligned(16))) float prm[256][8];
  __shared__ float ktab[2][644];
  __shared__ float bnd[4];
  __shared__ u32 trt[8 * 257];
  const int b = blockIdx.x, tid = threadIdx.x, l = tid & 63, w = __builtin_amdgcn_readfirstlane(tid >> 6);
  STAMP(0);
  u32 rla = 0, rai = 0, rcr = 0, rci = 0, rdt = 0;
  if (w < 4) {
    const int o = 2 * b * NS + tid;
    rla = logA[o]; rai = Aim[o]; rcr = Cre[o]; rci = Cim[o]; rdt = logdt[2 * b + (w >> 1)];
  }
  const v4u xr = *(const v4u*)(x0 + (8 * b + w) * DM + 8 * l);
  u32 a1[8], a2[8];
  const int fr = b * 7 + w, h = l >> 5, c32 = l & 31;
  if (w < 7) {
    const int ft = fr >> 5, ks = fr & 31;
#pragma unroll
    for (int j = 0; j < 8; ++j) a1[j] = w1[(16 * ks + 8 * h + j) * FF + 32 * ft + c32];
    const int ct = fr & 1, r1 = fr >> 1, ks2 = r1 % 14, r2 = r1 / 14, wk = r2 & 7, s = r2 >> 3;
#pragma unroll
    for (int j = 0; j < 8; ++j)
      a2[j] = w2[(224 * wk + 16 * ks2 + 8 * (j >> 2) + 4 * h + (j & 3)) * DM + 64 * s + 32 * ct + c32];
  }
  *(v4u*)(ws + XM_OFF + (b * 512 + tid) * 16) = v4u{~0u, ~0u, ~0u, ~0u};
  if (tid >= 128 && tid < 130) *(v4u*)(ws + FST_OFF + 8192 + (2 * b + tid - 128) * 16) = v4u{~0u, ~0u, ~0u, ~0u};
  if (tid < CTL_BYTES / 16 / 256) *(v4u*)(ws + CTL_OFF + (b * (CTL_BYTES / 16 / 256) + tid) * 16) = v4u{0u, 0u, 0u, 0u};
  STAMP(1);
  if (w < 4) {
    const float dt = bfr(expf(bf2f(rdt)));
    const float arl = -bfr(expf(bf2f(rla))) * dt * 1.4426950408889634f;
    double t = (double)(bf2f(rai) * dt) * 0.15915494309189533577;
    t -= __builtin_rint(t);
    const float rh = __builtin_bit_cast(float, __builtin_bit_cast(u32, (float)t) & 0xfffff000u);
    const float rl = (float)(t - (double)rh);
    const float cr = bf2f(rcr) * dt * 2.0f, ci = bf2f(rci) * dt * 2.0f;
    *(f32x4*)&prm[tid][0] = f32x4{arl, rh, rl, cr};
    prm[tid][4] = ci;
    const float bs = wave_sum(fabsf(cr) + fabsf(ci));
    if (l == 0) bnd[w] = bs;
  }
  STAMP(2);
  __syncthreads();
  STAMP(3);
  const int kk = l >> 4, i16 = l & 15;
  {
    const float sc = k_scale(bnd[2 * (w >> 2)] + bnd[2 * (w >> 2) + 1], 1);
    const float pB = (float)(8 * i16);
    const float pA0 = (float)(128 * (i16 >> 3) + (i16 & 7));
    const float pA1 = (float)(i16 < 8 ? 256 + 128 * (i16 >> 2) + 2 * (i16 & 3) : 512 + 128 * ((i16 - 8) >> 1) + 4 * (i16 & 1));
    const float pA2 = (float)(i16 < 8 ? 1024 + 128 * i16 : 2048);
    f32x4 acc0 = {0.f, 0.f, 0.f, 0.f}, acc1 = acc0, acc2 = acc0;
#pragma unroll
    for (int g = 0; g < 2; ++g) {
      v4u fa0, fa1, fa2, fbh, fbl;
#pragma unroll
      for (int j = 0; j < 4; ++j) {
        const f32x4 pv = *(const f32x4*)&prm[32 * w + 16 * g + 4 * kk + j][0];
        const float ci = prm[32 * w + 16 * g + 4 * kk + j][4] * sc;
        const float arl = pv[0], rh = pv[1], rl = pv[2], cr = pv[3] * sc;
        float br, bi, er, ei;
        cexp_p(arl, rh, rl, pB, br, bi);
        bi = -bi;
        const u32 hi = pack_f16(br, bi);
        const f16x2 hv = __builtin_bit_cast(f16x2, hi);
        fbh[j] = hi;
        fbl[j] = pack_f16(br - (float)hv.x, bi - (float)hv.y);
        cexp_p(arl, rh, rl, pA0, er, ei);
        fa0[j] = pack_f16(cr * er - ci * ei, cr * ei + ci * er);
        cexp_p(arl, rh, rl, pA1, er, ei);
        fa1[j] = pack_f16(cr * er - ci * ei, cr * ei + ci * er);
        cexp_p(arl, rh, rl, pA2, er, ei);
        fa2[j] = pack_f16(cr * er - ci * ei, cr * ei + ci * er);
      }
      acc0 = mfma16h(fa0, fbh, acc0); acc1 = mfma16h(fa1, fbh, acc1); acc2 = mfma16h(fa2, fbh, acc2);
      acc0 = mfma16h(fa0, fbl, acc0); acc1 = mfma16h(fa1, fbl, acc1); acc2 = mfma16h(fa2, fbl, acc2);
    }
    *(f32x4*)&part[w][i16 * 16 + 4 * kk] = acc0;
    *(f32x4*)&part[w][256 + i16 * 16 + 4 * kk] = acc1;
    *(f32x4*)&part[w][512 + i16 * 16 + 4 * kk] = acc2;
  }
  STAMP(4);
  {
    float s = 0.f, q = 0.f;
#pragma unroll
    for (int j = 0; j < 4; ++j) {
      const u32 p = min(xr[j], 0xfffffffeu);
      s = dot2bf(p, 0x3f803f80u, s); q = dot2bf(p, p, q);
      trt[w * 257 + 4 * l + j] = p;
    }
    s = wave_sum(s); q = wave_sum(q);
    if (l == 0) *(u32*)(ws + FST_OFF + (8 * b + w) * 4) = ln_pack(s, q);
  }
  if (w < 7) {
    v4u o1, o2;
#pragma unroll
    for (int j = 0; j < 4; ++j) { o1[j] = a1[2 * j] | (a1[2 * j + 1] << 16); o2[j] = a2[2 * j] | (a2[2 * j + 1] << 16); }
    *(v4u*)(ws + W1F_OFF + (fr * 64 + l) * 16) = o1;
    *(v4u*)(ws + W2F_OFF + (fr * 64 + l) * 16) = o2;
  }
  STAMP(5);
  __syncthreads();
  STAMP(6);
  {
    const int c = tid >> 8, x = tid & 255, col = x >> 4, row = x & 15;
    const int p0 = 128 * (row >> 3) + (row & 7) + 8 * col;
    const int p1 = (row < 8 ? 256 + 128 * (row >> 2) + 2 * (row & 3) : 512 + 128 * ((row - 8) >> 1) + 4 * (row & 1)) + 8 * col;
    const int c1 = row < 8 ? 128 + (p1 >> 1) : 256 + (p1 >> 2);
    const int c2 = row < 8 ? 512 + 16 * row + col : (x == 8 ? 640 : 641);
    ktab[c][p0] = (part[4 * c][x] + part[4 * c + 1][x]) + (part[4 * c + 2][x] + part[4 * c + 3][x]);
    ktab[c][c1] = (part[4 * c][256 + x] + part[4 * c + 1][256 + x]) + (part[4 * c + 2][256 + x] + part[4 * c + 3][256 + x]);
    ktab[c][c2] = (part[4 * c][512 + x] + part[4 * c + 1][512 + x]) + (part[4 * c + 2][512 + x] + part[4 * c + 3][512 + x]);
    const int q = tid >> 8, dp = tid & 255;
    const u32 t0 = trt[(4 * q) * 257 + dp], t1 = trt[(4 * q + 1) * 257 + dp], t2 = trt[(4 * q + 2) * 257 + dp], t3 = trt[(4 * q + 3) * 257 + dp];
    v4u o;
    o[0] = (t0 & 0xffffu) | (t1 << 16); o[1] = (t2 & 0xffffu) | (t3 << 16);
    o[2] = (t0 >> 16) | (t1 & 0xffff0000u); o[3] = (t2 >> 16) | (t3 & 0xffff0000u);
#pragma unroll
    for (int j = 0; j < 4; ++j) o[j] = min(o[j], 0xfffffffeu);
    *(v4u*)(ws + XQ_OFF + ((2 * b + q) * 512 + 2 * dp) * 8) = o;
  }
  __syncthreads();
  STAMP(7);
  {
    const int c = tid >> 8, un = tid & 255;
    v4u o;
#pragma unroll
    for (int j = 0; j < 4; ++j) {
      const int la = 2047 - 8 * un - 2 * j, lb = la - 1;
      const u32 hb = pack_bf16((float)la, (float)lb);
      const int ia = la < 128 ? la : (int)(hb & 0xffffu) - 17024, ib = lb < 128 ? lb : (int)(hb >> 16) - 17024;
      o[j] = pack_f16(ktab[c][ia], ktab[c][ib]);
    }
    char* krow = ws + KREV_OFF + (2 * b + c) * KROW * 2;
    *(v4u*)(krow + un * 16) = o;
    if (un < 2) *(v4u*)(krow + 4096 + un * 16) = v4u{0u, 0u, 0u, 0u};
    if (un == 0) *(float*)(ws + KSC_OFF + (2 * b + c) * 4) = k_scale(bnd[2 * c] + bnd[2 * c + 1], -1);
  }
  STAMP(8);
  STAMP_FLUSH(0);
}

constexpr int KC_CH = 8 * 260 * 16;
constexpr int L_U = 2 * KC_CH;
constexpr int U_CH = 4672;
constexpr int L_R = L_U + 2 * U_CH;
constexpr int L_XB = L_R + 32768;
constexpr int RB_ROW = 144;
constexpr int L_S = L_R + 8 * 64 * RB_ROW;
constexpr int L_B1 = L_S;
constexpr int L_CS = L_B1 + 896;
constexpr int L_SP = L_CS + 896;
constexpr int L_AC = L_SP + 4096;
constexpr int L_B2 = L_AC + 64;
constexpr int L_DK = L_B2 + 256;
constexpr int L_END = L_DK + 16;
constexpr int MAIN_LDS = L_END;
constexpr int W2A = 8;

DI f32x2 gelu2(f32x2 x) {
  const f32x2 t = x * (x * x * -0.10294324f + -2.3022082f);
  f32x2 e; e.x = __builtin_amdgcn_exp2f(t.x); e.y = __builtin_amdgcn_exp2f(t.y);
  e = e + 1.0f;
  f32x2 r; r.x = __builtin_amdgcn_rcpf(e.x); r.y = __builtin_amdgcn_rcpf(e.y);
  return x * r;
}
DI u32 max4(v4u a) { return max(max(a[0], a[1]), max(a[2], a[3])); }
DI void conv_ln(LAS char* sm, int cc, int ui, const u32 (&xv)[4], u32 (&ub)[4], v4u f0, v4u f1) {
  v4u o;
#pragma unroll
  for (int j = 0; j < 4; ++j) {
    const u32 p = xv[j], s0 = j < 2 ? f0[2 * j] : f1[2 * j - 4], s1 = j < 2 ? f0[2 * j + 1] : f1[2 * j - 3];
    const u32 d = pack_bf16(bflo(p) - bflo(s0), bfhi(p) - bflo(s1));
    const u32 u = pack_bf16(bflo(d) * __builtin_amdgcn_rcpf(bfhi(s0)), bfhi(d) * __builtin_amdgcn_rcpf(bfhi(s1)));
    ub[j] = u;
    o[j] = pack_f16(bflo(u), bfhi(u));
  }
  *(LAS v4u*)(sm + L_U + cc * U_CH + 512 + ui * 16) = o;
}

template <bool COMM>
DI void main_body(LAS char* sm, const __amdgpu_buffer_rsrc_t rs, char* ws, u16* out, const u16* dskip, const u16* b1,
                  const u16* b2, int b0, int w0) {
  v4u wf[32];
  u32 xv[4], ub[4];
#define LOAD_W1(lo, hi) do { if constexpr (!COMM) { _Pragma("unroll") for (int ks = (lo); ks < (hi); ++ks) \
      wf[ks] = LD128(l16 + (ks & 3) * 1024, W1F_OFF + (7 * s + w) * 32768 + (ks >> 2) * 4096, 0); SBAR(); } } while (0)
  {
    const int b = opaque_s(b0), w = opaque_s(w0), tid = opaque_v(threadIdx.x), l16 = (tid & 63) * 16, s = (b >> 3) & 7;
    const int cc = (tid >> 2) & 1, ui = ((tid >> 3) << 2) | (tid & 3);
    const int c = tid >> 8, n = tid & 255;
    const v4u k0 = LD128(c * (KROW * 2) + (255 - n) * 16, KREV_OFF + 2 * b * (KROW * 2), 0);
    const v4u k1 = LD128(c * (KROW * 2) + (255 - n) * 16 + 16, KREV_OFF + 2 * b * (KROW * 2), 0);
    const v2u xa = LD64(ui * 8192 + cc * 8, XQ_OFF + 16 * b, 0), xb = LD64(ui * 8192 + cc * 8, XQ_OFF + 16 * b + 4096, 0);
    const v4u f0 = LD128(ui * 32, FST_OFF, 0), f1 = LD128(ui * 32 + 16, FST_OFF, 0);
    const float dk = bf2f(dskip[2 * b + cc]);
    const float ksc = __builtin_bit_cast(float, LD32(cc * 4, KSC_OFF + 8 * b, 0));
    const float bias2 = bf2f(b2[64 * s + (tid & 63)]);
    const float bb1 = bf2f(b1[224 * s + (tid < 224 ? tid : 0)]);
    SBAR();
    LOAD_W1(0, 8);
    if (tid < 16) {
      *(LAS v4u*)(sm + (tid >> 3) * KC_CH + (tid & 7) * 4160) = v4u{0u, 0u, 0u, 0u};
      *(LAS u32*)(sm + L_AC + tid * 4) = 0u;
    }
    if (tid >= 64 && tid < 128) *(LAS v4u*)(sm + L_U + ((tid >> 5) & 1) * U_CH + (tid & 31) * 16) = v4u{0u, 0u, 0u, 0u};
    u32 wd[8];
#pragma unroll
    for (int j = 0; j < 4; ++j) { wd[j] = k0[j]; wd[4 + j] = k1[j]; }
#pragma unroll
    for (int cp = 0; cp < 8; ++cp) {
      const int sg = 7 - cp;
      v4u o;
#pragma unroll
      for (int k = 0; k < 4; ++k)
        o[k] = (sg & 1) ? __builtin_amdgcn_alignbit(wd[(sg + 1) / 2 + k], wd[(sg - 1) / 2 + k], 16) : wd[sg / 2 + k];
      *(LAS v4u*)(sm + c * KC_CH + cp * 4160 + (n + 1) * 16) = o;
    }
    STAMP(1);
    xv[0] = xa[0]; xv[1] = xa[1]; xv[2] = xb[0]; xv[3] = xb[1];
    conv_ln(sm, cc, ui, xv, ub, f0, f1);
    if (tid < 224) *(LAS float*)(sm + L_B1 + tid * 4) = bb1;
    if (tid < 64) *(LAS float*)(sm + L_B2 + tid * 4) = bias2;
    if ((tid & ~4) == 0) *(LAS f32x2*)(sm + L_DK + cc * 8) = f32x2{dk, ksc};
  }

#pragma unroll 1
  for (int ly = 0; ly < 2; ++ly) {
    const int b = opaque_s(b0), w = opaque_s(w0);
    const int t = 4 * (b & 7) + (b >> 6), s = (b >> 3) & 7;
    int tid = opaque_v(threadIdx.x), l = tid & 63, l16 = l * 16;
    auto hint = [&](int idx) {
      if (l < 8) __hip_atomic_fetch_add((u32*)(ws + CNT_OFF + ((idx * 8 + l) * 8 + (b & 7)) * 128), 1u, __ATOMIC_RELAXED, __HIP_MEMORY_SCOPE_AGENT);
    };
    auto wait_hint = [&](int idx) {
#pragma unroll 1
      for (int sp = 0; sp < SPIN_CAP; ++sp) {
        CBAR();
        const u32 f = LD32((l & 7) * 128, CNT_OFF + (idx * 8 + (b & 7)) * 1024, SC1);
        if (__all(f == 32u)) break;
        __builtin_amdgcn_s_sleep(1);
      }
      CBAR();
    };
    STAMP(2 + 30 * ly);
    __syncthreads();
    STAMP(3 + 30 * ly);
    {
      const int cc = (tid >> 2) & 1, i7 = (((tid >> 3) << 2) | (tid & 3)) & 127;
      const int xmv = (i7 >> 2) * 32768 + cc * 64 + (i7 & 3) * 16;
      if (ly == 0) {
        const u32 on = (u32)opaque_v(-1);
        ST128((v4u{on, on, on, on}), xmv, XM_OFF + (2 << 20) + (w >> 2) * (1 << 20) + 128 * b, SC1);
        ST64((v2u{on, on}), l * 8, XQ_OFF + (2 << 20) + ((8 * t + w) * 512 + 64 * s) * 8, SC1);
        ST64((v2u{on, on}), l * 8, XQ_OFF + (2 << 20) + ((256 + 8 * t + w) * 512 + 64 * s) * 8, SC1);
      }
      LOAD_W1(8, 16);
      auto conv_out = [&](int half) {
        const int s0 = (2 * i7) ^ ((i7 >> 2) & 3), s1 = (2 * i7 + 1) ^ ((i7 >> 2) & 3);
        const LAS char* pr = sm + (half ? L_R : L_XB) + cc * 16384;
        const f32x2 dks = *(const LAS f32x2*)(sm + L_DK + cc * 8);
        const float dk = dks.x, ksc = dks.y;
        f32x4 y0 = {0.f, 0.f, 0.f, 0.f}, y1 = y0;
#pragma unroll
        for (int k = 0; k < 4; ++k) { y0 += *(const LAS f32x4*)(pr + k * 4096 + s0 * 16); y1 += *(const LAS f32x4*)(pr + k * 4096 + s1 * 16); }
        v4u o;
#pragma unroll
        for (int j = 0; j < 4; ++j) {
          const float ya = (j < 2 ? y0[2 * j] : y1[2 * j - 4]) * ksc, yb = (j < 2 ? y0[2 * j + 1] : y1[2 * j - 3]) * ksc;
          const u32 yp = pack_bf16(ya, yb);
          const u32 tp = pack_bf16(bflo(ub[j]) * dk, bfhi(ub[j]) * dk);
          const u32 sp = pack_bf16(bflo(yp) + bflo(tp), bfhi(yp) + bfhi(tp));
          o[j] = min(pack_bf16(bflo(xv[j]) + bflo(sp), bfhi(xv[j]) + bfhi(sp)), 0xfffffffeu);
        }
        ST128(o, xmv, XM_OFF + ly * (2 << 20) + half * (1 << 20) + 128 * b, SC1);
        u32 old = 0;
        if (l == 0) old = __hip_atomic_fetch_add((LAS u32*)(sm + L_AC + (2 * ly + half) * 4), 1u, __ATOMIC_RELAXED, __HIP_MEMORY_SCOPE_WORKGROUP);
        if (__builtin_amdgcn_readfirstlane(old) == 3u) hint(2 * ly + half);
      };
      const int c16 = l & 15, g4 = l >> 4;
      const LAS char* pa = sm + (w >> 2) * KC_CH + (c16 & 7) * 4160 + (3 + (c16 >> 3) - g4) * 16 + (w & 3) * 128;
      const LAS char* pb = sm + L_U + (w >> 2) * U_CH + (30 + 2 * c16 + g4) * 16 - (w & 3) * 128;
      LAS char* pp = sm + L_R + w * 4096 + ((4 * c16 + g4) ^ ((c16 >> 1) & 3)) * 16;
      {
        f32x4 acc[4];
#pragma unroll
        for (int i = 0; i < 4; ++i) acc[i] = f32x4{0.f, 0.f, 0.f, 0.f};
        v4u fa[2][4], fb[2][4];
#pragma unroll
        for (int tau = 0; tau < 2; ++tau)
#pragma unroll
          for (int i = 0; i < 4; ++i) { fa[tau][i] = *(const LAS v4u*)(pa + i * 512 + tau * 64); fb[tau][i] = *(const LAS v4u*)(pb + i * 512 - tau * 64); }
#pragma unroll
        for (int tau = 0; tau < 2; ++tau)
#pragma unroll
          for (int sx = 0; sx < 4; ++sx)
#pragma unroll
            for (int e = 0; e + sx < 4; ++e) acc[sx + e] = mfma16h(fa[tau][sx], fb[tau][e], acc[sx + e]);
#pragma unroll
        for (int i = 0; i < 4; ++i) *(LAS f32x4*)(pp + 32768 + i * 1024) = acc[i];
      }
      STAMP(4 + 30 * ly);
      __syncthreads();
      if constexpr (!COMM) { if (w < 4) conv_out(0); }
      STAMP(5 + 30 * ly);
      LOAD_W1(16, 24);
      {
        f32x4 acc[4];
#pragma unroll
        for (int i = 0; i < 4; ++i) acc[i] = f32x4{0.f, 0.f, 0.f, 0.f};
#pragma unroll
        for (int tau = 0; tau < 2; ++tau) {
          v4u fa[8], fb[8];
#pragma unroll
          for (int i = 0; i < 8; ++i) { fa[i] = *(const LAS v4u*)(pa + i * 512 + tau * 64); fb[i] = *(const LAS v4u*)(pb + i * 512 - tau * 64); }
#pragma unroll
          for (int e = 0; e < 8; ++e)
#pragma unroll
            for (int i = 0; i < 4; ++i) if (4 + i - e >= 0) acc[i] = mfma16h(fa[4 + i - e], fb[e], acc[i]);
        }
#pragma unroll
        for (int i = 0; i < 4; ++i) *(LAS f32x4*)(pp + i * 1024) = acc[i];
      }
      STAMP(6 + 30 * ly);
      __syncthreads();
      if (COMM || w >= 4) conv_out(1);
      STAMP(7 + 30 * ly);
      LOAD_W1(24, 32);
    }
    if constexpr (COMM) {
      wait_hint(2 * ly);
    } else {
      float cs = 0.f;
#pragma unroll
      for (int ks = 0; ks < 32; ++ks)
#pragma unroll
        for (int j = 0; j < 4; ++j) { const u32 p = wf[ks][j]; cs = dot2bf(p, 0x3f803f80u, cs); }
      cs = swap32_add(cs, cs);
      if (l < 32) *(LAS float*)(sm + L_CS + w * 128 + l * 4) = cs;
    }
    STAMP(9 + 30 * ly);
    __syncthreads();
    STAMP(10 + 30 * ly);
    tid = opaque_v(threadIdx.x); l = tid & 63; l16 = l * 16;
    const int h = l >> 5;
    const int xmo = XM_OFF + ly * (2 << 20) + t * 32768;
    const int w2off = W2F_OFF + ((s * 8 + w) * 28) * 1024;
    v4u wa[W2A];
    {
      v4u xs[4];
#pragma unroll 1
      for (int sp = 0; sp < SPIN_CAP; ++sp) {
        CBAR();
#pragma unroll
        for (int k = 0; k < 4; ++k) xs[k] = LD128(l16, xmo + k * 8192 + w * 1024, SC1);
        if (__all(max(max(max4(xs[0]), max4(xs[1])), max(max4(xs[2]), max4(xs[3]))) != 0xffffffffu)) break;
        __builtin_amdgcn_s_sleep(1);
      }
      if constexpr (!COMM) {
#pragma unroll
        for (int k = 0; k < W2A; ++k) wa[k] = LD128(l16 + (k & 3) * 1024, w2off + (k >> 2) * 4096, 0);
        SBAR();
      }
#pragma unroll
      for (int k = 0; k < 4; ++k) *(LAS v4u*)(sm + L_R + k * 8192 + tid * 16) = xs[k];
    }
    STAMP(11 + 30 * ly);
    __syncthreads();
    const LAS char* xb = sm + L_R + (8 * h + ((l & 15) >> 2)) * 64 + 32 * ((l >> 4) & 1) + 8 * (l & 3);
    auto stats_part = [&](int half) {
      float ss = 0.f, qq = 0.f;
#pragma unroll
      for (int k = 0; k < 4; ++k) {
        const v4u f = tr_frag(xb + half * 32768 + (4 * w + k) * 1024);
#pragma unroll
        for (int j = 0; j < 4; ++j) { const u32 p = f[j]; ss = dot2bf(p, 0x3f803f80u, ss); qq = dot2bf(p, p, qq); }
      }
      const float r = swap32_add(ss, qq);
      *(LAS float*)(sm + L_SP + ((half * 8 + w) * 32 + (l & 31)) * 8 + h * 4) = r;
    };
    auto stats_fin = [&](int half, float& al, f32x4 (&cb)[4]) {
      float ss = 0.f, qq = 0.f;
#pragma unroll
      for (int k = 0; k < 8; ++k) { const f32x2 p = *(const LAS f32x2*)(sm + L_SP + ((half * 8 + k) * 32 + (l & 31)) * 8); ss += p.x; qq += p.y; }
      const float m = ss * (1.0f / DM);
      const float v = fmaxf(qq * (1.0f / DM) - m * m, 0.f);
      const u32 pk = pack_bf16(m, __builtin_sqrtf(bfr(bfr(v) + 1e-5f)));
      const float mbf = bflo(pk), r1 = __builtin_amdgcn_rcpf(bfhi(pk));
      const u32 p2 = pack_bf16((m - mbf) * r1, v * r1 * r1);
      const float r2 = __builtin_amdgcn_rcpf(bfr(__builtin_sqrtf(bfr(bfhi(p2) + 1e-5f))));
      al = r1 * r2;
      const float be = (mbf * r1 + bflo(p2)) * r2;
#pragma unroll
      for (int q = 0; q < 4; ++q) {
        const f32x4 c4 = *(const LAS f32x4*)(sm + L_CS + w * 128 + (8 * q + 4 * h) * 4);
        const f32x4 b4 = *(const LAS f32x4*)(sm + L_B1 + (32 * w + 8 * q + 4 * h) * 4);
        cb[q] = b4 - c4 * be;
      }
    };
    f32x16 acc0, acc1;
    constexpr int XD = 4;
    if constexpr (!COMM) {
      v4u xf[XD];
#pragma unroll
      for (int k = 0; k < XD; ++k) xf[k] = tr_frag(xb + k * 1024);
      stats_part(0);
      acc0 = f32x16{};
#pragma unroll
      for (int ks = 0; ks < 32; ++ks) {
        acc0 = mfma32(wf[ks], xf[ks % XD], acc0);
        if (ks + XD < 32) xf[ks % XD] = tr_frag(xb + (ks + XD) * 1024);
        SBAR();
      }
    } else {
      stats_part(0);
      wait_hint(2 * ly + 1);
      STAMP(12 + 30 * ly);
#pragma unroll 1
      for (int sp = 0; sp < SPIN_CAP; ++sp) {
        CBAR();
        u32 m = 0;
#pragma unroll
        for (int k = 0; k < 32; ++k) wf[k] = LD128(l16 + (k & 3) * 1024, xmo + (1 << 20) + (k >> 2) * 4096, SC1);
#pragma unroll
        for (int k = 0; k < 32; ++k) { m = max(m, max4(wf[k])); *(LAS v4u*)(sm + L_XB + k * 1024 + l16) = wf[k]; }
        if (__all(m != 0xffffffffu)) break;
        __builtin_amdgcn_s_sleep(1);
      }
    }
    STAMP(13 + 30 * ly);
    __syncthreads();
    STAMP(14 + 30 * ly);
    const v2u xres0 = *(const LAS v2u*)(sm + L_R + (64 * s + l) * 64 + 8 * w);
    const v2u xres1 = *(const LAS v2u*)(sm + L_XB + (64 * s + l) * 64 + 8 * w);
    const int hoff = H_OFF + ly * H_LAYER + (t * 8 + s) * (28 * 1024) + (2 * w) * 1024;
    auto epi_pair = [&](const f32x16& acc, int r, float al, const f32x4 (&cb)[4]) -> u32 {
      const f32x2 a2 = {acc[r], acc[r + 1]};
      const f32x2 c2 = {cb[r >> 2][r & 3], cb[r >> 2][(r & 3) + 1]};
      const f32x2 g = gelu2(a2 * al + c2);
      return pack_bf16(g.x, g.y);
    };
    if constexpr (!COMM) {
      v4u xf[XD];
#pragma unroll
      for (int k = 0; k < XD; ++k) xf[k] = tr_frag(xb + 32768 + k * 1024);
      stats_part(1);
      float al;
      f32x4 cb[4];
      stats_fin(0, al, cb);
      v4u o;
      acc1 = f32x16{};
      SBAR();
#pragma unroll
      for (int ks = 0; ks < 32; ++ks) {
        acc1 = mfma32(wf[ks], xf[ks % XD], acc1);
        if (ks + XD < 32) xf[ks % XD] = tr_frag(xb + 32768 + (ks + XD) * 1024);
        if (ks % 3 != 0 && (2 * ks - 1) / 3 < 28 - W2A) {
          const int j = (2 * ks - 1) / 3, k = W2A + j;
          wf[j] = LD128(l16 + (k & 3) * 1024, w2off + (k >> 2) * 4096, 0);
        }
        if ((ks % 3) == 0 && ks < 24) {
          const int pr = ks / 3;
          o[pr & 3] = epi_pair(acc0, 2 * pr, al, cb);
          if ((pr & 3) == 3) ST128(o, l16, hoff + (pr >> 2) * 1024, SC1);
        }
        SBAR();
      }
      asm volatile("s_waitcnt vmcnt(6)" ::: "memory");
    } else {
      stats_part(1);
#pragma unroll
      for (int k = 0; k < W2A; ++k) wa[k] = LD128(l16 + (k & 3) * 1024, w2off + (k >> 2) * 4096, 0);
#pragma unroll
      for (int k = W2A; k < 28; ++k) wf[k - W2A] = LD128(l16 + (k & 3) * 1024, w2off + (k >> 2) * 4096, 0);
      SBAR();
    }
    STAMP(15 + 30 * ly);
    __syncthreads();
    const int hfo = HF_OFF + (2 * ly) * 4096 + t * 128;
    if (COMM && l == 0) ST32(1u, 0, hfo + s * 4, SC1);
    LAS u32* hrdy = (LAS u32*)(sm + L_AC + (8 + ly) * 4);
    v4u hf[14];
    const int hin = H_OFF + ly * H_LAYER + (t * 8 + w) * (28 * 1024);
    if constexpr (!COMM) {
      float al;
      f32x4 cb[4];
      stats_fin(1, al, cb);
#pragma unroll
      for (int sp = 0; sp < 2; ++sp) {
        v4u o;
#pragma unroll
        for (int j = 0; j < 4; ++j) o[j] = epi_pair(acc1, 8 * sp + 2 * j, al, cb);
        ST128(o, l16, hoff + (14 + sp) * 1024, SC1);
      }
      STAMP(16 + 30 * ly);
#pragma unroll 1
      for (int sp = 0; sp < (SPIN_CAP << 4); ++sp) {
        if (__hip_atomic_load(hrdy, __ATOMIC_RELAXED, __HIP_MEMORY_SCOPE_WORKGROUP) != 0u) break;
        __builtin_amdgcn_s_sleep(1);
      }
      CBAR();
    } else {
#pragma unroll 1
      for (int sp = 0; sp < SPIN_CAP; ++sp) {
        CBAR();
        const u32 f = LD32((l & 7) * 4, hfo, SC1);
        if (__all(f == 1u)) break;
        __builtin_amdgcn_s_sleep(1);
      }
      CBAR();
      if (l == 0) __hip_atomic_store(hrdy, 1u, __ATOMIC_RELAXED, __HIP_MEMORY_SCOPE_WORKGROUP);
    }
    STAMP(17 + 30 * ly);
#pragma unroll
    for (int k = 0; k < 14; ++k) { hf[k] = LD128(l16, hin + k * 1024, SC1); SBAR(); }
    asm volatile("s_waitcnt vmcnt(14)" ::: "memory");
    STAMP(18 + 30 * ly);
    __syncthreads();
    SBAR();
    if (COMM && l == 0) ST32(1u, 0, hfo + 4096 + s * 4, SC1);
    LAS char* rbw = sm + L_R + (w * 64 + (l & 31)) * RB_ROW + h * 16;
    auto g2_out = [&](int half, v2u xres) {
      const float bias2 = *(const LAS float*)(sm + L_B2 + l * 4);
      f32x4 o4 = {0.f, 0.f, 0.f, 0.f};
#pragma unroll
      for (int ww = 0; ww < 8; ++ww) o4 += *(const LAS f32x4*)(sm + L_R + (ww * 64 + l) * RB_ROW + w * 16);
      u32 o[2];
#pragma unroll
      for (int j = 0; j < 2; ++j) {
        const u32 op = pack_bf16(o4[2 * j], o4[2 * j + 1]);
        const u32 o2 = pack_bf16(bflo(op) + bias2, bfhi(op) + bias2);
        const u32 xp = xres[j];
        o[j] = pack_bf16(bflo(xp) + bflo(o2), bfhi(xp) + bfhi(o2));
      }
      const int tok = 1024 * half + 32 * t + 4 * w;
      if (ly) {
        u16* op = out + tok * DM + 64 * s + l;
        op[0] = (u16)(o[0] & 0xffffu); op[DM] = (u16)(o[0] >> 16); op[2 * DM] = (u16)(o[1] & 0xffffu); op[3 * DM] = (u16)(o[1] >> 16);
      } else {
        o[0] = min(o[0], 0xfffffffeu); o[1] = min(o[1], 0xfffffffeu);
        const float x0 = bflo(o[0]), x1 = bfhi(o[0]), x2 = bflo(o[1]), x3 = bfhi(o[1]);
        const float a0 = swap32_add(x0, x2), a1 = swap32_add(x0 * x0, x2 * x2), a2 = swap32_add(x1, x3), a3 = swap32_add(x1 * x1, x3 * x3);
        const float sv = row_sum16(swap16_add(a0, a2)), qv = row_sum16(swap16_add(a1, a3));
        if ((l & 14) == 0)
          ST64((v2u{__builtin_bit_cast(u32, (l & 1) ? qv : sv), (u32)opaque_v(1)}), (l >> 4) * 16 + (l & 1) * 8, PST_OFF + (((t * 2 + half) * 8 + w) * 8 + s) * 64, SC1);
        ST64((v2u{o[0], o[1]}), l * 8, XQ_OFF + (2 << 20) + ((tok >> 2) * 512 + 64 * s) * 8, SC1);
      }
    };
#define W2FRAG(k) ((k) < W2A ? wa[(k) < W2A ? (k) : 0] : wf[(k) < W2A ? 0 : (k) - W2A])
    {
      f32x16 a0 = {}, a1 = {};
#pragma unroll
      for (int k = 0; k < 14; ++k) { a0 = mfma32(hf[k], W2FRAG(2 * k), a0); a1 = mfma32(hf[k], W2FRAG(2 * k + 1), a1); }
      STAMP(19 + 30 * ly);
#pragma unroll 1
      for (int sp = 0; sp < SPIN_CAP; ++sp) {
        CBAR();
        const u32 f = LD32(0, hfo + 4096 + w * 4, SC1);
        if (__builtin_amdgcn_readfirstlane(f) == 1u) break;
        __builtin_amdgcn_s_sleep(1);
      }
      CBAR();
#pragma unroll
      for (int k = 0; k < 14; ++k) { hf[k] = LD128(l16, hin + (14 + k) * 1024, SC1); SBAR(); }
      STAMP(20 + 30 * ly);
#pragma unroll
      for (int q = 0; q < 4; ++q) {
        *(LAS f32x4*)(rbw + q * 32) = f32x4{a0[4 * q], a0[4 * q + 1], a0[4 * q + 2], a0[4 * q + 3]};
        *(LAS f32x4*)(rbw + 32 * RB_ROW + q * 32) = f32x4{a1[4 * q], a1[4 * q + 1], a1[4 * q + 2], a1[4 * q + 3]};
      }
    }
    __syncthreads();
    STAMP(21 + 30 * ly);
    g2_out(0, xres0);
    STAMP(22 + 30 * ly);
    __syncthreads();
    {
      f32x16 a0 = {}, a1 = {};
#pragma unroll
      for (int k = 0; k < 14; ++k) { a0 = mfma32(hf[k], W2FRAG(2 * k), a0); a1 = mfma32(hf[k], W2FRAG(2 * k + 1), a1); }
      STAMP(23 + 30 * ly);
#pragma unroll
      for (int q = 0; q < 4; ++q) {
        *(LAS f32x4*)(rbw + q * 32) = f32x4{a0[4 * q], a0[4 * q + 1], a0[4 * q + 2], a0[4 * q + 3]};
        *(LAS f32x4*)(rbw + 32 * RB_ROW + q * 32) = f32x4{a1[4 * q], a1[4 * q + 1], a1[4 * q + 2], a1[4 * q + 3]};
      }
    }
    __syncthreads();
    STAMP(24 + 30 * ly);
    g2_out(1, xres1);
    STAMP(25 + 30 * ly);
    if (ly == 0) {
      if constexpr (COMM) {
        v2u g0, g1;
        const int po = PST_OFF + ((t * 2) * 8 + s) * 512;
#pragma unroll 1
        for (int sp = 0; sp < SPIN_CAP; ++sp) {
          CBAR();
          g0 = LD64(l * 8, po, SC1);
          g1 = LD64(l * 8, po + 4096, SC1);
          if (__all((g0[1] & g1[1]) == 1u)) break;
          __builtin_amdgcn_s_sleep(1);
        }
        const u32 e0 = g0[0], e1 = g1[0];
        float v = swap32_add(__builtin_bit_cast(float, e0), __builtin_bit_cast(float, e1));
        v = swap16_add(v, v);
        v = dpp_add<0x128>(v);
        const float o = __builtin_bit_cast(float, __builtin_amdgcn_update_dpp(0, __builtin_bit_cast(int, v), 0xb1, 0xf, 0xf, false));
        if ((l & 25) == 0) ST32(ln_pack(v, o), (1024 * h + ((l >> 1) & 3)) * 4, FST_OFF + 8192 + (32 * t + 4 * s) * 4, SC1);
        hint(4);
        STAMP(26 + 30 * ly);
        wait_hint(4);
      }
      LOAD_W1(0, 8);
      STAMP(27 + 30 * ly);
      __syncthreads();
      const int cc = (tid >> 2) & 1, ui = ((tid >> 3) << 2) | (tid & 3);
      v4u f0, f1; v2u xa, xc;
#pragma unroll 1
      for (int sp = 0; sp < SPIN_CAP; ++sp) {
        CBAR();
        xa = LD64(ui * 8192 + cc * 8, XQ_OFF + (2 << 20) + 16 * b, SC1);
        xc = LD64(ui * 8192 + cc * 8, XQ_OFF + (2 << 20) + 16 * b + 4096, SC1);
        f0 = LD128(ui * 32, FST_OFF + 8192, SC1);
        f1 = LD128(ui * 32 + 16, FST_OFF + 8192, SC1);
        if (__all(max(max(max(xa[0], xa[1]), max(xc[0], xc[1])), max(max4(f0), max4(f1))) != 0xffffffffu)) break;
        __builtin_amdgcn_s_sleep(1);
      }
      xv[0] = xa[0]; xv[1] = xa[1]; xv[2] = xc[0]; xv[3] = xc[1];
      conv_ln(sm, cc, ui, xv, ub, f0, f1);
    }
  }
}

__global__ __launch_bounds__(512, 2) void k_main(const u16* __restrict__ dskip, const u16* __restrict__ b1,
    const u16* __restrict__ b2, char* __restrict__ ws, u16* __restrict__ out) {
  extern __shared__ __attribute__((aligned(16))) char smem[];
  LAS char* sm = (LAS char*)smem;
  const int b0 = blockIdx.x, w0 = __builtin_amdgcn_readfirstlane(threadIdx.x >> 6);
  const __amdgpu_buffer_rsrc_t rs = __builtin_amdgcn_make_buffer_rsrc(ws, 0, WS_BYTES, 0x00020000);
  STAMP(0);
  if (w0 == 7) main_body<true>(sm, rs, ws, out, dskip, b1, b2, b0, w0);
  else main_body<false>(sm, rs, ws, out, dskip, b1, b2, b0, w0);
  STAMP_FLUSH(1);
}

extern "C" void kernel_launch(void* const* d_in, const int* in_sizes, int n_in, void* d_out, int out_size, void* d_ws,
                              size_t ws_size, hipStream_t stream) {
  (void)hipFuncSetAttribute((const void*)k_main, hipFuncAttributeMaxDynamicSharedMemorySize, MAIN_LDS);
  k_prep<<<256, 512, 0, stream>>>((const u16*)d_in[0], (const u16*)d_in[1], (const u16*)d_in[2], (const u16*)d_in[3],
                                  (const u16*)d_in[4], (const u16*)d_in[5], (const u16*)d_in[8], (const u16*)d_in[10], (char*)d_ws);
  k_main<<<256, 512, MAIN_LDS, stream>>>((const u16*)d_in[6], (const u16*)d_in[9], (const u16*)d_in[11], (char*)d_ws, (u16*)d_out);
}
```

```cpp
#include <hip/hip_runtime.h>
#include <hip/hip_bf16.h>

typedef unsigned int u32;
typedef unsigned short u16;
typedef __attribute__((ext_vector_type(4))) u32 v4u;
typedef __attribute__((ext_vector_type(2))) u32 v2u;
typedef __attribute__((ext_vector_type(4))) float f32x4;
typedef __attribute__((ext_vector_type(2))) float f32x2;
typedef __attribute__((ext_vector_type(16))) float f32x16;
typedef __attribute__((ext_vector_type(8))) __bf16 bf16x8;
typedef __attribute__((ext_vector_type(2))) __bf16 bf16x2;
typedef __attribute__((ext_vector_type(8))) _Float16 f16x8;
typedef __attribute__((ext_vector_type(2))) _Float16 f16x2;
typedef __attribute__((ext_vector_type(4))) short s16x4;
#define LAS __attribute__((address_space(3)))
#define DI __device__ __forceinline__
#define STAMP(i)
#define STAMP_FLUSH(kid)

constexpr int SL = 2048, DM = 512, NS = 128, FF = 1792;
constexpr int KROW = 2064;
constexpr int XC_OFF = 0;
constexpr int XM_OFF = 4 << 20;
constexpr int KREV_OFF = 8 << 20;
constexpr int KSC_OFF = KREV_OFF + DM * KROW * 2;
constexpr int CS_OFF = KSC_OFF + 2048;
constexpr int W1F_OFF = 11 << 20;
constexpr int W2F_OFF = 13 << 20;
constexpr int FST_OFF = 15 << 20;
constexpr int CTL_OFF = FST_OFF + 32768;
constexpr int CNT_OFF = CTL_OFF;
constexpr int HF_OFF = CNT_OFF + 8192;
constexpr int PST_OFF = HF_OFF + 8192;
constexpr int CTL_BYTES = 8192 + 8192 + 262144;
constexpr int H_OFF = 16 << 20;
constexpr int H_LAYER = 32 * 8 * 2 * 14 * 1024;
constexpr int WS_BYTES = 32 << 20;
constexpr int SC1 = 16;
constexpr int SPIN_CAP = 1 << 16;
#define K0_AUX 0

DI u32 umin(u32 a, u32 b) { return a < b ? a : b; }
DI u32 umax(u32 a, u32 b) { return a > b ? a : b; }
DI float bf2f(u32 h) { return __builtin_bit_cast(float, h << 16); }
DI float bflo(u32 p) { return __builtin_bit_cast(float, p << 16); }
DI float bfhi(u32 p) { return __builtin_bit_cast(float, p & 0xffff0000u); }
DI u32 pack_bf16(float a, float b) { bf16x2 v = {(__bf16)a, (__bf16)b}; return __builtin_bit_cast(u32, v); }
DI float bfr(float a) { return bflo(pack_bf16(a, a)); }
DI u32 pack_f16(float a, float b) { f16x2 v = {(_Float16)a, (_Float16)b}; return __builtin_bit_cast(u32, v); }
DI float dot2bf(u32 a, u32 b, float c) {
  return __builtin_amdgcn_fdot2_f32_bf16(__builtin_bit_cast(bf16x2, a), __builtin_bit_cast(bf16x2, b), c, false);
}
DI f32x16 mfma32(v4u a, v4u b, f32x16 c) {
  return __builtin_amdgcn_mfma_f32_32x32x16_bf16(__builtin_bit_cast(bf16x8, a), __builtin_bit_cast(bf16x8, b), c, 0, 0, 0);
}
DI f32x4 mfma16h(v4u a, v4u b, f32x4 c) {
  return __builtin_amdgcn_mfma_f32_16x16x32_f16(__builtin_bit_cast(f16x8, a), __builtin_bit_cast(f16x8, b), c, 0, 0, 0);
}
DI f32x4 mfma16b(v4u a, v4u b, f32x4 c) {
  return __builtin_amdgcn_mfma_f32_16x16x32_bf16(__builtin_bit_cast(bf16x8, a), __builtin_bit_cast(bf16x8, b), c, 0, 0, 0);
}
DI v4u tr_frag(const LAS char* p) {
  const s16x4 a = __builtin_amdgcn_ds_read_tr16_b64_v4i16((LAS s16x4*)p);
  const s16x4 c = __builtin_amdgcn_ds_read_tr16_b64_v4i16((LAS s16x4*)(p + 256));
  return __builtin_bit_cast(v4u, __builtin_shufflevector(a, c, 0, 1, 2, 3, 4, 5, 6, 7));
}
template <int CTRL> DI float dpp_add(float v) {
  const int o = __builtin_amdgcn_update_dpp(0, __builtin_bit_cast(int, v), CTRL, 0xf, 0xf, false);
  return v + __builtin_bit_cast(float, o);
}
DI float row_sum16(float v) { v = dpp_add<0x128>(v); v = dpp_add<0x124>(v); v = dpp_add<0x122>(v); return dpp_add<0x121>(v); }
DI float swap32_add(float a, float c) {
  const auto r = __builtin_amdgcn_permlane32_swap(__builtin_bit_cast(u32, a), __builtin_bit_cast(u32, c), false, false);
  const u32 r0 = r[0], r1 = r[1];
  return __builtin_bit_cast(float, r0) + __builtin_bit_cast(float, r1);
}
DI float swap16_add(float a, float c) {
  const auto r = __builtin_amdgcn_permlane16_swap(__builtin_bit_cast(u32, a), __builtin_bit_cast(u32, c), false, false);
  const u32 r0 = r[0], r1 = r[1];
  return __builtin_bit_cast(float, r0) + __builtin_bit_cast(float, r1);
}
DI float wave_sum(float v) { v = row_sum16(v); v = swap16_add(v, v); return swap32_add(v, v); }
DI void ln_stats(float s, float q, float& m, float& v, float& mb, float& rs) {
  m = s * (1.0f / DM);
  v = fmaxf(q * (1.0f / DM) - m * m, 0.f);
  const u32 pk = pack_bf16(m, __builtin_amdgcn_sqrtf(bfr(bfr(v) + 1e-5f)));
  mb = bflo(pk); rs = __builtin_amdgcn_rcpf(bfhi(pk));
}
DI u32 npz(float a) { return umin(__builtin_bit_cast(u32, a), 0xfffffffeu); }
#define LD128(vo, so, aux) __builtin_amdgcn_raw_buffer_load_b128(rs, (vo), (so), (aux))
#define LD64(vo, so, aux) __builtin_amdgcn_raw_buffer_load_b64(rs, (vo), (so), (aux))
#define LD32(vo, so, aux) __builtin_amdgcn_raw_buffer_load_b32(rs, (vo), (so), (aux))
#define ST128(v, vo, so, aux) __builtin_amdgcn_raw_buffer_store_b128((v), rs, (vo) + (so), 0, (aux))
#define ST64(v, vo, so, aux) __builtin_amdgcn_raw_buffer_store_b64((v), rs, (vo), (so), (aux))
#define ST32(v, vo, so, aux) __builtin_amdgcn_raw_buffer_store_b32((v), rs, (vo), (so), (aux))
#define DRAIN() asm volatile("s_waitcnt vmcnt(0)" ::: "memory")
#define CBAR() asm volatile("" ::: "memory")
#define SBAR() __builtin_amdgcn_sched_barrier(0)
DI int opaque_v(int x) { asm volatile("" : "+v"(x)); return x; }
template <typename T, int N> DI void undef_regs(T (&a)[N]) {
#pragma unroll
  for (int i = 0; i < N; ++i) asm volatile("" : "=v"(a[i]));
}
DI int opaque_s(int x) { asm volatile("" : "+s"(x)); return x; }

DI void cexp_p(float arl, float rh, float rl, float p, float& re, float& im) {
  const float e = __builtin_amdgcn_exp2f(arl * p);
  const float r = __builtin_fmaf(rl, p, __builtin_amdgcn_fractf(rh * p));
  re = e * __builtin_amdgcn_cosf(r);
  im = e * __builtin_amdgcn_sinf(r);
}
DI float k_scale(float bs, int sign) {
  const int ex = (int)((__builtin_bit_cast(u32, bs) >> 23) & 0xff) - 126;
  int e = 15 - ex; e = e > 100 ? 100 : (e < -100 ? -100 : e);
  return __builtin_bit_cast(float, (u32)(127 + sign * e) << 23);
}
__global__ __launch_bounds__(512, 2) void k_prep(const u16* __restrict__ x0, const u16* __restrict__ logA,
    const u16* __restrict__ Aim, const u16* __restrict__ Cre, const u16* __restrict__ Cim,
    const u16* __restrict__ logdt, const u16* __restrict__ w1, const u16* __restrict__ w2, char* __restrict__ ws) {
  __shared__ __attribute__((aligned(16))) float part[8][768];
  __shared__ __attribute__((aligned(16))) float prm[256][8];
  __shared__ float ktab[2][644];
  __shared__ float bnd[4];
  __shared__ float csp[8][32];
  __shared__ u32 trt[64 * 33];
  const int b = blockIdx.x, tid = threadIdx.x, l = tid & 63, w = __builtin_amdgcn_readfirstlane(tid >> 6);
  const __amdgpu_buffer_rsrc_t rs = __builtin_amdgcn_make_buffer_rsrc(ws, 0, WS_BYTES, 0x00020000);
  STAMP(0);
  u32 rla = 0, rai = 0, rcr = 0, rci = 0, rdt = 0;
  if (w < 4) {
    const int o = 2 * b * NS + tid;
    rla = logA[o]; rai = Aim[o]; rcr = Cre[o]; rci = Cim[o]; rdt = logdt[2 * b + (w >> 1)];
  }
  const v4u xr = *(const v4u*)(x0 + (8 * b + w) * DM + 8 * l);
  const v4u xt = *(const v4u*)(x0 + (64 * (b >> 3) + (tid >> 3)) * DM + 64 * (b & 7) + 8 * (tid & 7));
  u32 a1[8], a2[8];
  const int h = l >> 5, c32 = l & 31;
  const int fr = (b < 112 ? b : b - 112) * 16 + 2 * w;
  if (b < 112) {
    const u16* p = w1 + (16 * (fr & 31) + 8 * h) * FF + 32 * (fr >> 5) + c32;
#pragma unroll
    for (int j = 0; j < 8; ++j) { a1[j] = p[j * FF]; a2[j] = p[(16 + j) * FF]; }
  } else if (b < 224) {
    const int r1 = fr >> 1, ks2 = r1 % 14, r2 = r1 / 14;
    const u16* p = w2 + (224 * (r2 & 7) + 16 * ks2 + 4 * h) * DM + 64 * (r2 >> 3) + c32;
#pragma unroll
    for (int j = 0; j < 8; ++j) { a1[j] = p[(8 * (j >> 2) + (j & 3)) * DM]; a2[j] = p[(8 * (j >> 2) + (j & 3)) * DM + 32]; }
  }
  ST128((v4u{~0u, ~0u, ~0u, ~0u}), tid * 16, XM_OFF + b * 8192, K0_AUX);
  if (tid >= 128 && tid < 132) ST128((v4u{~0u, ~0u, ~0u, ~0u}), (tid - 128) * 16, FST_OFF + 16384 + b * 64, K0_AUX);
  if (tid < CTL_BYTES / 16 / 256) ST128((v4u{0u, 0u, 0u, 0u}), tid * 16, CTL_OFF + b * (CTL_BYTES / 256), K0_AUX);
  STAMP(1);
  if (w < 4) {
    const float dt = bfr(expf(bf2f(rdt)));
    const float arl = -bfr(expf(bf2f(rla))) * dt * 1.4426950408889634f;
    double t = (double)(bf2f(rai) * dt) * 0.15915494309189533577;
    t -= __builtin_rint(t);
    const float rh = __builtin_bit_cast(float, __builtin_bit_cast(u32, (float)t) & 0xfffff000u);
    const float rl = (float)(t - (double)rh);
    const float cr = bf2f(rcr) * dt * 2.0f, ci = bf2f(rci) * dt * 2.0f;
    *(f32x4*)&prm[tid][0] = f32x4{arl, rh, rl, cr};
    prm[tid][4] = ci;
    const float bs = wave_sum(fabsf(cr) + fabsf(ci));
    if (l == 0) bnd[w] = bs;
  }
  STAMP(2);
  __syncthreads();
  STAMP(3);
  const int kk = l >> 4, i16 = l & 15;
  {
    const float sc = k_scale(bnd[2 * (w >> 2)] + bnd[2 * (w >> 2) + 1], 1);
    const float pB = (float)(8 * i16);
    const float pA0 = (float)(128 * (i16 >> 3) + (i16 & 7));
    const float pA1 = (float)(i16 < 8 ? 256 + 128 * (i16 >> 2) + 2 * (i16 & 3) : 512 + 128 * ((i16 - 8) >> 1) + 4 * (i16 & 1));
    const float pA2 = (float)(i16 < 8 ? 1024 + 128 * i16 : 2048);
    f32x4 acc0 = {0.f, 0.f, 0.f, 0.f}, acc1 = acc0, acc2 = acc0;
#pragma unroll
    for (int g = 0; g < 2; ++g) {
      v4u fa0, fa1, fa2, fbh, fbl;
#pragma unroll
      for (int j = 0; j < 4; ++j) {
        const f32x4 pv = *(const f32x4*)&prm[32 * w + 16 * g + 4 * kk + j][0];
        const float ci = prm[32 * w + 16 * g + 4 * kk + j][4] * sc;
        const float arl = pv[0], rh = pv[1], rl = pv[2], cr = pv[3] * sc;
        float br, bi, er, ei;
        cexp_p(arl, rh, rl, pB, br, bi);
        bi = -bi;
        const u32 hi = pack_f16(br, bi);
        const f16x2 hv = __builtin_bit_cast(f16x2, hi);
        fbh[j] = hi;
        fbl[j] = pack_f16(br - (float)hv.x, bi - (float)hv.y);
        cexp_p(arl, rh, rl, pA0, er, ei);
        fa0[j] = pack_f16(cr * er - ci * ei, cr * ei + ci * er);
        cexp_p(arl, rh, rl, pA1, er, ei);
        fa1[j] = pack_f16(cr * er - ci * ei, cr * ei + ci * er);
        cexp_p(arl, rh, rl, pA2, er, ei);
        fa2[j] = pack_f16(cr * er - ci * ei, cr * ei + ci * er);
      }
      acc0 = mfma16h(fa0, fbh, acc0); acc1 = mfma16h(fa1, fbh, acc1); acc2 = mfma16h(fa2, fbh, acc2);
      acc0 = mfma16h(fa0, fbl, acc0); acc1 = mfma16h(fa1, fbl, acc1); acc2 = mfma16h(fa2, fbl, acc2);
    }
    *(f32x4*)&part[w][i16 * 16 + 4 * kk] = acc0;
    *(f32x4*)&part[w][256 + i16 * 16 + 4 * kk] = acc1;
    *(f32x4*)&part[w][512 + i16 * 16 + 4 * kk] = acc2;
  }
  STAMP(4);
  {
    float s = 0.f, q = 0.f;
#pragma unroll
    for (int j = 0; j < 4; ++j) {
      const u32 p = xr[j];
      s = dot2bf(p, 0x3f803f80u, s); q = dot2bf(p, p, q);
      trt[(tid >> 3) * 33 + 4 * (tid & 7) + j] = xt[j];
    }
    s = wave_sum(s); q = wave_sum(q);
    float m, v, mb, rcs;
    ln_stats(s, q, m, v, mb, rcs);
    if (l < 2) ST32(__builtin_bit_cast(u32, l ? rcs : mb), l * 16, FST_OFF + (2 * b + (w >> 2)) * 32 + (w & 3) * 4, K0_AUX);
  }
  if (b < 224) {
    v4u o1, o2;
    float cs = 0.f;
#pragma unroll
    for (int j = 0; j < 4; ++j) {
      const u32 p1 = a1[2 * j] | (a1[2 * j + 1] << 16), p2 = a2[2 * j] | (a2[2 * j + 1] << 16);
      o1[j] = p1; o2[j] = p2;
      cs = dot2bf(p1, 0x3f803f80u, cs); cs = dot2bf(p2, 0x3f803f80u, cs);
    }
    ST128(o1, l * 16, (b < 112 ? W1F_OFF : W2F_OFF) + fr * 1024, K0_AUX);
    ST128(o2, l * 16 + 1024, (b < 112 ? W1F_OFF : W2F_OFF) + fr * 1024, K0_AUX);
    cs = swap32_add(cs, cs);
    if (l < 32) csp[w][l] = cs;
  }
  STAMP(5);
  __syncthreads();
  STAMP(6);
  {
    const int c = tid >> 8, x = tid & 255, col = x >> 4, row = x & 15;
    const int p0 = 128 * (row >> 3) + (row & 7) + 8 * col;
    const int p1 = (row < 8 ? 256 + 128 * (row >> 2) + 2 * (row & 3) : 512 + 128 * ((row - 8) >> 1) + 4 * (row & 1)) + 8 * col;
    const int c1 = row < 8 ? 128 + (p1 >> 1) : 256 + (p1 >> 2);
    const int c2 = row < 8 ? 512 + 16 * row + col : (x == 8 ? 640 : 641);
    ktab[c][p0] = (part[4 * c][x] + part[4 * c + 1][x]) + (part[4 * c + 2][x] + part[4 * c + 3][x]);
    ktab[c][c1] = (part[4 * c][256 + x] + part[4 * c + 1][256 + x]) + (part[4 * c + 2][256 + x] + part[4 * c + 3][256 + x]);
    ktab[c][c2] = (part[4 * c][512 + x] + part[4 * c + 1][512 + x]) + (part[4 * c + 2][512 + x] + part[4 * c + 3][512 + x]);
    if (b < 112 && tid < 32) {
      const float cs = ((csp[0][tid] + csp[1][tid]) + (csp[2][tid] + csp[3][tid])) + ((csp[4][tid] + csp[5][tid]) + (csp[6][tid] + csp[7][tid]));
      ST32(__builtin_bit_cast(u32, cs), tid * 4, CS_OFF + (b & 1) * (FF * 4) + (b >> 1) * 128, K0_AUX);
    }
    const int cq = tid >> 3, oct = tid & 7;
    const u16* t16 = (const u16*)trt;
    v4u o;
#pragma unroll
    for (int j = 0; j < 4; ++j)
      o[j] = (u32)t16[(8 * oct + 2 * j) * 66 + cq] | ((u32)t16[(8 * oct + 2 * j + 1) * 66 + cq] << 16);
    ST128(o, cq * 4096 + oct * 16, XC_OFF + (b & 7) * (64 * 4096) + (b >> 3) * 128, K0_AUX);
  }
  __syncthreads();
  STAMP(7);
  {
    const int c = tid >> 8, un = tid & 255;
    v4u o;
#pragma unroll
    for (int j = 0; j < 4; ++j) {
      const int la = 2047 - 8 * un - 2 * j, lb = la - 1;
      const u32 hb = pack_bf16((float)la, (float)lb);
      const int ia = la < 128 ? la : (int)(hb & 0xffffu) - 17024, ib = lb < 128 ? lb : (int)(hb >> 16) - 17024;
      o[j] = pack_f16(ktab[c][ia], ktab[c][ib]);
    }
    ST128(o, c * (KROW * 2) + un * 16, KREV_OFF + 2 * b * (KROW * 2), K0_AUX);
    if (un < 2) ST128((v4u{0u, 0u, 0u, 0u}), c * (KROW * 2) + 4096 + un * 16, KREV_OFF + 2 * b * (KROW * 2), K0_AUX);
    if (un == 0) ST32(__builtin_bit_cast(u32, k_scale(bnd[2 * c] + bnd[2 * c + 1], -1)), c * 4, KSC_OFF + 8 * b, K0_AUX);
  }
  STAMP(8);
  STAMP_FLUSH(0);
}

constexpr int KC_CH = 8 * 260 * 16;
constexpr int L_U = 2 * KC_CH;
constexpr int U_CH = 4672;
constexpr int L_R = L_U + 2 * U_CH;
constexpr int L_XB = L_R + 32768;
constexpr int L_PB = L_XB + 256;
constexpr int RB_ROW = 144;
constexpr int L_S = L_R + 8 * 64 * RB_ROW;
constexpr int L_B1 = L_S;
constexpr int L_CS = L_B1 + 896;
constexpr int L_SP = L_CS + 896;
constexpr int L_AC = L_SP + 4096;
constexpr int L_B2 = L_AC + 64;
constexpr int L_DK = L_B2 + 256;
constexpr int L_AB = L_DK + 16;
constexpr int L_END = L_AB + 512;
constexpr int MAIN_LDS = L_END;
#define MAPV 1
#define MAP_T(b) ((((b) >> 1) & 3) * 8 + ((b) >> 5))
#define MAP_S(b) (((b) & 1) * 4 + (((b) >> 3) & 3))
#define W1_SPLIT 0
constexpr int W2A = 12;

DI f32x2 gelu2(f32x2 x) {
  const f32x2 t = x * (x * x * -0.10294324f + -2.3022082f);
  f32x2 e; e.x = __builtin_amdgcn_exp2f(t.x); e.y = __builtin_amdgcn_exp2f(t.y);
  e = e + 1.0f;
  f32x2 r; r.x = __builtin_amdgcn_rcpf(e.x); r.y = __builtin_amdgcn_rcpf(e.y);
  return x * r;
}
DI u32 max4(v4u a) { return umax(umax(a[0], a[1]), umax(a[2], a[3])); }
DI f32x4 unpack4(v2u p) { const u32 a = p[0], c = p[1]; return f32x4{bflo(a), bfhi(a), bflo(c), bfhi(c)}; }
DI f32x4 conv_ln(LAS char* dst, v2u xp, v4u mm, v4u rr, float dk) {
  const f32x4 x = unpack4(xp);
  const f32x4 u = (x - __builtin_bit_cast(f32x4, mm)) * __builtin_bit_cast(f32x4, rr);
  *(LAS v2u*)dst = v2u{pack_f16(u[0], u[1]), pack_f16(u[2], u[3])};
  return x + u * dk;
}

template <bool COMM>
DI void main_body(LAS char* sm, const __amdgpu_buffer_rsrc_t rs, char* ws, u16* out, const u16* dskip, const u16* b1,
                  const u16* b2, int b0, int w0) {
  v4u wf[32], wa[W2A];
  f32x4 xr0, xr1;
#define LOAD_W(lo, hi) do { _Pragma("unroll") for (int ks = (lo); ks < (hi); ++ks) { \
      if constexpr (!COMM) wf[ks] = LD128(l16 + (ks & 3) * 1024, W1F_OFF + (7 * s + w) * 32768 + (ks >> 2) * 4096, 0); \
      else if (ks < W2A) wa[ks] = LD128(l16 + (ks & 3) * 1024, W2F_OFF + (s * 8 + 7) * 28672 + (ks >> 2) * 4096, 0); \
      else if (ks < 28) wf[ks - W2A] = LD128(l16 + (ks & 3) * 1024, W2F_OFF + (s * 8 + 7) * 28672 + (ks >> 2) * 4096, 0); } \
      SBAR(); } while (0)
  {
    const int b = opaque_s(b0), w = opaque_s(w0), tid = opaque_v(threadIdx.x), l16 = (tid & 63) * 16, s = MAP_S(b);
    const int cc = (tid >> 3) & 1, q8 = (tid >> 4) * 8 + (tid & 7);
    const int c = tid >> 8, n = tid & 255;
    const v4u k0 = LD128(c * (KROW * 2) + (255 - n) * 16, KREV_OFF + 2 * b * (KROW * 2), 0);
    const v4u k1 = LD128(c * (KROW * 2) + (255 - n) * 16 + 16, KREV_OFF + 2 * b * (KROW * 2), 0);
    const v2u xa = LD64(cc * 4096 + q8 * 8, XC_OFF + b * 8192, 0), xb = LD64(cc * 4096 + q8 * 8 + 2048, XC_OFF + b * 8192, 0);
    const v4u m0 = LD128(q8 * 32, FST_OFF, 0), r0 = LD128(q8 * 32 + 16, FST_OFF, 0);
    const v4u m1 = LD128(q8 * 32, FST_OFF + 8192, 0), r1 = LD128(q8 * 32 + 16, FST_OFF + 8192, 0);
    const float dk = bf2f(dskip[2 * b + cc]);
    const float ksc = __builtin_bit_cast(float, LD32(cc * 4, KSC_OFF + 8 * b, 0));
    const float bias2 = bf2f(b2[64 * s + (tid & 63)]);
    const float bb1 = bf2f(b1[224 * s + (tid < 224 ? tid : 0)]);
    const float cs = __builtin_bit_cast(float, LD32(tid * 4, CS_OFF + s * 896, 0)) + __builtin_bit_cast(float, LD32(tid * 4, CS_OFF + FF * 4 + s * 896, 0));
    SBAR();
    if (tid < 16) {
      *(LAS v4u*)(sm + (tid >> 3) * KC_CH + (tid & 7) * 4160) = v4u{0u, 0u, 0u, 0u};
      *(LAS u32*)(sm + L_AC + tid * 4) = 0u;
    }
    if (tid >= 64 && tid < 128) *(LAS v4u*)(sm + L_U + ((tid >> 5) & 1) * U_CH + (tid & 31) * 16) = v4u{0u, 0u, 0u, 0u};
    u32 wd[8];
#pragma unroll
    for (int j = 0; j < 4; ++j) { wd[j] = k0[j]; wd[4 + j] = k1[j]; }
#pragma unroll
    for (int cp = 0; cp < 8; ++cp) {
      const int sg = 7 - cp;
      v4u o;
#pragma unroll
      for (int k = 0; k < 4; ++k)
        o[k] = (sg & 1) ? __builtin_amdgcn_alignbit(wd[(sg + 1) / 2 + k], wd[(sg - 1) / 2 + k], 16) : wd[sg / 2 + k];
      *(LAS v4u*)(sm + c * KC_CH + cp * 4160 + (n + 1) * 16) = o;
    }
    STAMP(1);
    xr0 = conv_ln(sm + L_U + cc * U_CH + 512 + q8 * 8, xa, m0, r0, dk);
    xr1 = conv_ln(sm + L_U + cc * U_CH + 2560 + q8 * 8, xb, m1, r1, dk);
    if (tid < 224) { *(LAS float*)(sm + L_B1 + tid * 4) = bb1; *(LAS float*)(sm + L_CS + tid * 4) = cs; }
    if (tid < 64) *(LAS float*)(sm + L_B2 + tid * 4) = bias2;
    if ((tid & ~8) == 0) *(LAS f32x2*)(sm + L_DK + cc * 8) = f32x2{dk, ksc};
    SBAR();
    LOAD_W(0, 8);
#pragma unroll
    for (int i = (COMM ? 0 : 8); i < 32; ++i) asm volatile("" : "=v"(wf[i]));
    if constexpr (COMM) {
#pragma unroll
      for (int i = 8; i < W2A; ++i) asm volatile("" : "=v"(wa[i]));
    }
  }

#pragma unroll 1
  for (int ly = 0; ly < 2; ++ly) {
    const int b = opaque_s(b0), w = opaque_s(w0);
    const int t = MAP_T(b), s = MAP_S(b);
    int tid = opaque_v(threadIdx.x), l = tid & 63, l16 = l * 16;
    const int xmo = XM_OFF + ly * (2 << 20) + t * 32768 + w * 1024;
    const int w2off = W2F_OFF + ((s * 8 + w) * 28) * 1024;
    const int hoff = H_OFF + ly * H_LAYER + (t * 8 + s) * (28 * 1024) + (2 * w) * 1024;
    v4u xs[4];
    auto x_load = [&](int half) {
#pragma unroll
      for (int k = 0; k < 4; ++k) xs[k] = LD128(l16, xmo + half * (1 << 20) + k * 8192, SC1);
    };
    auto x_ok = [&]() { return __all(umax(umax(max4(xs[0]), max4(xs[1])), umax(max4(xs[2]), max4(xs[3]))) != 0xffffffffu); };
    auto x_stage = [&](int half) {
      if (!x_ok()) {
        int sp = 0;
#pragma unroll 1
        do {
          __builtin_amdgcn_s_sleep(4);
          CBAR();
          x_load(half);
        } while (!x_ok() && ++sp < SPIN_CAP);
      }
#pragma unroll
      for (int k = 0; k < 4; ++k) *(LAS v4u*)(sm + L_R + half * 32768 + k * 8192 + tid * 16) = xs[k];
    };
    STAMP(2 + 30 * ly);
    __syncthreads();
    STAMP(3 + 30 * ly);
    {
      const int c16 = l & 15, g4 = l >> 4;
      const LAS char* pa = sm + (w >> 2) * KC_CH + (c16 & 7) * 4160 + (3 + (c16 >> 3) - g4) * 16 + (w & 3) * 128;
      const LAS char* pb = sm + L_U + (w >> 2) * U_CH + (30 + 2 * c16 + g4) * 16 - (w & 3) * 128;
      LAS char* pp = sm + L_R + w * 4096 + (w >> 2) * 128 + ((4 * c16 + g4) ^ ((c16 >> 1) & 3)) * 16;
      const int xmv = (tid >> 4) * 32768 + (tid & 15) * 8;
      const LAS char* pr = sm + L_R + ((tid >> 3) & 1) * 16512 + ((((tid >> 4) << 3) | (tid & 7)) ^ ((tid >> 4) & 3)) * 16;
      auto conv_out = [&](int half, f32x4 xr) {
        const LAS char* p = pr + (half ? L_PB - L_R : 0);
        const float ksc = *(const LAS float*)(sm + L_DK + ((tid >> 3) & 1) * 8 + 4);
        const f32x4 y = (*(const LAS f32x4*)p + *(const LAS f32x4*)(p + 4096)) + (*(const LAS f32x4*)(p + 8192) + *(const LAS f32x4*)(p + 12288));
        const f32x4 o = y * ksc + xr;
        ST64((v2u{umin(pack_bf16(o[0], o[1]), 0xfffffffeu), umin(pack_bf16(o[2], o[3]), 0xfffffffeu)}), xmv,
             XM_OFF + ly * (2 << 20) + half * (1 << 20) + 128 * b, SC1);
      };
      {
        f32x4 acc[4];
#pragma unroll
        for (int i = 0; i < 4; ++i) acc[i] = f32x4{0.f, 0.f, 0.f, 0.f};
        v4u fa[2][4], fb[2][4];
#pragma unroll
        for (int tau = 0; tau < 2; ++tau)
#pragma unroll
          for (int i = 0; i < 4; ++i) { fa[tau][i] = *(const LAS v4u*)(pa + i * 512 + tau * 64); fb[tau][i] = *(const LAS v4u*)(pb + i * 512 - tau * 64); }
#pragma unroll
        for (int tau = 0; tau < 2; ++tau)
#pragma unroll
          for (int sx = 0; sx < 4; ++sx)
#pragma unroll
            for (int e = 0; e + sx < 4; ++e) acc[sx + e] = mfma16h(fa[tau][sx], fb[tau][e], acc[sx + e]);
#pragma unroll
        for (int i = 0; i < 4; ++i) *(LAS f32x4*)(pp + i * 1024) = acc[i];
      }
      v4u fa[8], fb[8];
#pragma unroll
      for (int i = 0; i < 8; ++i) { fb[i] = *(const LAS v4u*)(pb + i * 512); fa[7 - i] = *(const LAS v4u*)(pa + (7 - i) * 512); }
      STAMP(4 + 30 * ly);
      __syncthreads();
      conv_out(0, xr0);
      STAMP(5 + 30 * ly);
      if (ly == 0) LOAD_W(8, 20);
      if constexpr (!COMM) {
        const u32 on = (u32)opaque_v(-1);
#pragma unroll
        for (int k = 0; k < 4; ++k) ST128((v4u{on, on, on, on}), l16 + (k & 1) * 1024, hoff + (k >> 1) * 14336, SC1);
        SBAR();
      }
      {
        f32x4 acc[4];
#pragma unroll
        for (int i = 0; i < 4; ++i) acc[i] = f32x4{0.f, 0.f, 0.f, 0.f};
#pragma unroll
        for (int e = 0; e < 8; ++e) {
#pragma unroll
          for (int i = 0; i < 4; ++i) if (4 + i - e >= 0) acc[i] = mfma16h(fa[4 + i - e], fb[e], acc[i]);
          fb[e] = *(const LAS v4u*)(pb + e * 512 - 64); fa[7 - e] = *(const LAS v4u*)(pa + (7 - e) * 512 + 64);
          SBAR();
        }
        if (ly == 0) LOAD_W(20, 32);
#pragma unroll
        for (int e = 0; e < 8; ++e)
#pragma unroll
          for (int i = 0; i < 4; ++i) if (4 + i - e >= 0) acc[i] = mfma16h(fa[4 + i - e], fb[e], acc[i]);
#pragma unroll
        for (int i = 0; i < 4; ++i) *(LAS f32x4*)(pp + (L_PB - L_R) + i * 1024) = acc[i];
      }
      STAMP(6 + 30 * ly);
      __syncthreads();
      x_load(0);
      SBAR();
      conv_out(1, xr1);
      STAMP(7 + 30 * ly);
      x_stage(0);
      if (ly == 0) {
        const u32 on = (u32)opaque_v(-1);
        ST64((v2u{on, on}), xmv, XM_OFF + (2 << 20) + 128 * b, SC1);
        ST64((v2u{on, on}), xmv, XM_OFF + (3 << 20) + 128 * b, SC1);
        ST64((v2u{on, on}), (tid >> 3) * 4096 + (tid & 7) * 8, XC_OFF + (2 << 20) + s * (64 * 4096) + t * 64, SC1);
        ST64((v2u{on, on}), (tid >> 3) * 4096 + (tid & 7) * 8, XC_OFF + (2 << 20) + s * (64 * 4096) + t * 64 + 2048, SC1);
      }
    }
    STAMP(11 + 30 * ly);
    __syncthreads();
    tid = opaque_v(threadIdx.x); l = tid & 63; l16 = l * 16;
    const int h = l >> 5;
    const LAS char* xb = sm + L_R + (8 * h + ((l & 15) >> 2)) * 64 + 32 * ((l >> 4) & 1) + 8 * (l & 3);
    auto stats = [&](int half) {
      const LAS char* xq = sm + L_R + half * 32768 + (8 * (l >> 4) + ((l & 15) >> 2)) * 64 + 8 * (l & 3);
      const u32 one = (u32)opaque_v(0x3f803f80);
      const v4u ones = {one, one, one, one};
      f32x4 s0 = {0.f, 0.f, 0.f, 0.f}, q0 = s0, s1 = s0, q1 = s0;
      v4u f0[4], f1[4];
#pragma unroll
      for (int k = 0; k < 4; ++k) { f0[k] = tr_frag(xq + k * 2048); f1[k] = tr_frag(xq + k * 2048 + 32); }
#pragma unroll
      for (int k = 0; k < 16; ++k) {
        s0 = mfma16b(ones, f0[k & 3], s0); q0 = mfma16b(f0[k & 3], f0[k & 3], q0);
        s1 = mfma16b(ones, f1[k & 3], s1); q1 = mfma16b(f1[k & 3], f1[k & 3], q1);
        if (k + 4 < 16) { f0[k & 3] = tr_frag(xq + (k + 4) * 2048); f1[k & 3] = tr_frag(xq + (k + 4) * 2048 + 32); }
        SBAR();
      }
      const float d0 = (l & 2) ? ((l & 1) ? q0[3] : q0[2]) : ((l & 1) ? q0[1] : q0[0]);
      const float d1 = (l & 2) ? ((l & 1) ? q1[3] : q1[2]) : ((l & 1) ? q1[1] : q1[0]);
#pragma unroll
      for (int g = 0; g < 2; ++g) {
        float m, v, mb, r1;
        ln_stats(g ? s1[0] : s0[0], g ? d1 : d0, m, v, mb, r1);
        const u32 p2 = pack_bf16((m - mb) * r1, v * r1 * r1);
        const float r2 = __builtin_amdgcn_rcpf(bfr(__builtin_amdgcn_sqrtf(bfr(bfhi(p2) + 1e-5f))));
        if ((l >> 4) == ((l >> 2) & 3)) *(LAS f32x2*)(sm + L_AB + (half * 32 + 16 * g + (l & 15)) * 8) = f32x2{r1 * r2, (mb * r1 + bflo(p2)) * r2};
      }
    };
    auto epi_quad = [&](const f32x16& acc, int q, f32x2 ab, u32& o0, u32& o1) {
      const f32x4 c4 = *(const LAS f32x4*)(sm + L_CS + (32 * w + 8 * q + 4 * h) * 4);
      const f32x4 b4 = *(const LAS f32x4*)(sm + L_B1 + (32 * w + 8 * q + 4 * h) * 4);
      const f32x4 x = f32x4{acc[4 * q], acc[4 * q + 1], acc[4 * q + 2], acc[4 * q + 3]} * ab.x + (b4 - c4 * ab.y);
      const f32x2 g0 = gelu2(f32x2{x[0], x[1]}), g1 = gelu2(f32x2{x[2], x[3]});
      o0 = umin(pack_bf16(g0.x, g0.y), 0xfffffffeu); o1 = umin(pack_bf16(g1.x, g1.y), 0xfffffffeu);
    };
    f32x16 acc0, acc1;
    constexpr int XD = 3;
    if constexpr (!COMM) {
      v4u xf[XD];
#pragma unroll
      for (int k = 0; k < XD; ++k) xf[k] = tr_frag(xb + k * 1024);
      acc0 = f32x16{};
#pragma unroll
      for (int ks = 0; ks < 32; ++ks) {
        acc0 = mfma32(wf[ks], xf[ks % XD], acc0);
        if (ks + XD < 32) xf[ks % XD] = tr_frag(xb + (ks + XD) * 1024);
        if (ks < 2 * W2A && !(ks & 1)) { const int k = ks >> 1; wa[k] = LD128(l16 + (k & 3) * 1024, w2off + (k >> 2) * 4096, 0); }
        if (ks == 15) x_load(1);
        SBAR();
      }
    } else {
      __builtin_amdgcn_s_setprio(2);
      stats(0);
      __builtin_amdgcn_s_setprio(0);
      x_load(1);
    }
    STAMP(12 + 30 * ly);
    x_stage(1);
    STAMP(13 + 30 * ly);
    __syncthreads();
    STAMP(14 + 30 * ly);
    const int orow = ly ? l : 8 * w + (l >> 3), oq = ly ? w : (l & 7);
    const v2u xres0 = *(const LAS v2u*)(sm + L_R + (64 * s + orow) * 64 + 8 * oq);
    const v2u xres1 = *(const LAS v2u*)(sm + L_XB + (64 * s + orow) * 64 + 8 * oq);
    const int hfo = HF_OFF + ly * 4096 + t * 128;
    if constexpr (!COMM) {
      v4u xf[XD];
#pragma unroll
      for (int k = 0; k < XD; ++k) xf[k] = tr_frag(xb + 32768 + k * 1024);
      const f32x2 ab = *(const LAS f32x2*)(sm + L_AB + (l & 31) * 8);
      u32 o[4];
      acc1 = f32x16{};
#pragma unroll
      for (int ks = 0; ks < 32; ++ks) {
        acc1 = mfma32(wf[ks], xf[ks % XD], acc1);
        if (ks + XD < 32) xf[ks % XD] = tr_frag(xb + 32768 + (ks + XD) * 1024);
        if (ks & 1) { const int j = ks >> 1, k = W2A + j; wf[j] = LD128(l16 + (k & 3) * 1024, w2off + (k >> 2) * 4096, 0); }
        if (ks % 6 == 1 && ks < 24) { const int q = ks / 6; epi_quad(acc0, q, ab, o[2 * (q & 1)], o[2 * (q & 1) + 1]); }
        if (ks == 11 || ks == 23) ST128((v4u{o[0], o[1], o[2], o[3]}), l16 + (ks / 12) * 1024, hoff, SC1);
        if (ks % 6 == 5 || ks >= 24) SBAR();
      }
    } else {
      if (l == 0) ST32(1u, 0, hfo + s * 4, SC1);
      __builtin_amdgcn_s_setprio(2);
      stats(1);
      __builtin_amdgcn_s_setprio(0);
#pragma unroll 1
      for (int sp = 0; sp < SPIN_CAP; ++sp) {
        CBAR();
        const u32 f = LD32((l & 7) * 4, hfo, SC1);
        if (__all(f == 1u)) break;
        __builtin_amdgcn_s_sleep(1);
      }
      CBAR();
    }
    STAMP(15 + 30 * ly);
    __syncthreads();
    v4u hf[14];
    const int hin = H_OFF + ly * H_LAYER + (t * 8 + w) * (28 * 1024);
    auto h_load = [&](int half, int lo, int hi) {
#pragma unroll
      for (int k = lo; k < hi; ++k) { hf[k] = LD128(l16, hin + (14 * half + k) * 1024, SC1); SBAR(); }
    };
    h_load(0, 0, 7);
    STAMP(16 + 30 * ly);
    if constexpr (!COMM) {
      const f32x2 ab = *(const LAS f32x2*)(sm + L_AB + (32 + (l & 31)) * 8);
#pragma unroll
      for (int sp = 0; sp < 2; ++sp) {
        u32 o[4];
        epi_quad(acc1, 2 * sp, ab, o[0], o[1]);
        epi_quad(acc1, 2 * sp + 1, ab, o[2], o[3]);
        ST128((v4u{o[0], o[1], o[2], o[3]}), l16 + sp * 1024, hoff + 14336, SC1);
      }
      SBAR();
    }
    h_load(0, 7, 14);
    STAMP(17 + 30 * ly);
    LAS char* rbw = sm + L_R + (w * 64 + (l & 31)) * RB_ROW + h * 16;
    auto g2_out = [&](int half, v2u xres) {
      const LAS char* rp = sm + L_R + orow * RB_ROW + oq * 16;
      f32x4 o4 = unpack4(xres) + *(const LAS float*)(sm + L_B2 + orow * 4);
      f32x4 p4 = *(const LAS f32x4*)rp;
#pragma unroll
      for (int ww = 1; ww < 8; ++ww) { const f32x4 v = *(const LAS f32x4*)(rp + ww * (64 * RB_ROW)); if (ww & 1) o4 += v; else p4 += v; }
      o4 += p4;
      u32 o0 = pack_bf16(o4[0], o4[1]), o1 = pack_bf16(o4[2], o4[3]);
      if (ly) {
        u16* op = out + (1024 * half + 32 * t + 4 * w) * DM + 64 * s + l;
        op[0] = (u16)(o0 & 0xffffu); op[DM] = (u16)(o0 >> 16); op[2 * DM] = (u16)(o1 & 0xffffu); op[3 * DM] = (u16)(o1 >> 16);
      } else {
        o0 = umin(o0, 0xfffffffeu); o1 = umin(o1, 0xfffffffeu);
        ST64((v2u{o0, o1}), (tid >> 3) * 4096 + (tid & 7) * 8, XC_OFF + (2 << 20) + s * (64 * 4096) + t * 64 + half * 2048, SC1);
        const float x0 = bflo(o0), x1 = bfhi(o0), x2 = bflo(o1), x3 = bfhi(o1);
        const float a0 = swap32_add(x0, x2), a1 = swap32_add(x0 * x0, x2 * x2), a2 = swap32_add(x1, x3), a3 = swap32_add(x1 * x1, x3 * x3);
        const float sv = dpp_add<0x128>(swap16_add(a0, a2)), qv = dpp_add<0x128>(swap16_add(a1, a3));
        if ((l & 8) == 0) *(LAS f32x2*)(sm + L_SP + ((half * 8 + w) * 32 + 4 * (l & 7) + (l >> 4)) * 8) = f32x2{sv, qv};
      }
    };
    auto pst_out = [&](int half) {
      float v = 0.f;
#pragma unroll
      for (int k = 0; k < 8; ++k) v += *(const LAS float*)(sm + L_SP + (half * 8 + k) * 256 + l * 4);
      ST64((v2u{__builtin_bit_cast(u32, v), (u32)opaque_v(1)}), (l >> 3) * 512 + (l & 7) * 8, PST_OFF + (t * 2 + half) * 4096 + s * 64, SC1);
    };
#define W2FRAG(k) ((k) < W2A ? wa[(k) < W2A ? (k) : 0] : wf[(k) < W2A ? 0 : (k) - W2A])
    auto gemm2 = [&](int half) {
      f32x16 a0, a1;
#pragma unroll 1
      for (int sp = 0; sp < SPIN_CAP; ++sp) {
        a0 = f32x16{}; a1 = f32x16{};
        u32 mx = 0;
#pragma unroll
        for (int k = 0; k < 14; ++k) {
          a0 = mfma32(hf[k], W2FRAG(2 * k), a0); a1 = mfma32(hf[k], W2FRAG(2 * k + 1), a1);
          mx = umax(mx, max4(hf[k]));
        }
        if (__all(mx != 0xffffffffu)) break;
        __builtin_amdgcn_s_sleep(2);
        CBAR();
        h_load(half, 0, 14);
      }
      STAMP(19 + 4 * half + 30 * ly);
      if (half == 0) h_load(1, 0, 14);
      else if constexpr (!COMM) {
        if (ly == 0) LOAD_W(16, 32);
        else {
#pragma unroll
          for (int i = 16; i < 32; ++i) asm volatile("" : "=v"(wf[i]));
        }
      }
      STAMP(20 + 8 * half + 30 * ly);
#pragma unroll
      for (int q = 0; q < 4; ++q) {
        *(LAS f32x4*)(rbw + q * 32) = f32x4{a0[4 * q], a0[4 * q + 1], a0[4 * q + 2], a0[4 * q + 3]};
        *(LAS f32x4*)(rbw + 32 * RB_ROW + q * 32) = f32x4{a1[4 * q], a1[4 * q + 1], a1[4 * q + 2], a1[4 * q + 3]};
      }
    };
    gemm2(0);
    __syncthreads();
    STAMP(21 + 30 * ly);
    g2_out(0, xres0);
    STAMP(22 + 30 * ly);
    __syncthreads();
    if (COMM && ly == 0) pst_out(0);
    gemm2(1);
    __syncthreads();
    STAMP(24 + 30 * ly);
    g2_out(1, xres1);
    STAMP(25 + 30 * ly);
    if (ly == 0) {
      if constexpr (!COMM) LOAD_W(0, W1_SPLIT);
      __syncthreads();
      if constexpr (!COMM) LOAD_W(W1_SPLIT, 16);
      if constexpr (COMM) {
        pst_out(1);
        v2u g0, g1;
        const int po = PST_OFF + ((t * 2) * 8 + s) * 512;
#pragma unroll 1
        for (int sp = 0; sp < SPIN_CAP; ++sp) {
          CBAR();
          g0 = LD64(l * 8, po, SC1);
          g1 = LD64(l * 8, po + 4096, SC1);
          if (__all((g0[1] & g1[1]) == 1u)) break;
          __builtin_amdgcn_s_sleep(1);
        }
        const u32 e0 = g0[0], e1 = g1[0];
        float v = swap32_add(__builtin_bit_cast(float, e0), __builtin_bit_cast(float, e1));
        v = swap16_add(v, v);
        v = dpp_add<0x128>(v);
        const float o = __builtin_bit_cast(float, __builtin_amdgcn_update_dpp(0, __builtin_bit_cast(int, v), 0xb1, 0xf, 0xf, false));
        float m, vr, mb, rcs;
        ln_stats((l & 1) ? o : v, (l & 1) ? v : o, m, vr, mb, rcs);
        if ((l & 24) == 0) ST32(npz((l & 1) ? rcs : mb), h * 8192 + (l & 1) * 16 + ((l >> 1) & 3) * 4, FST_OFF + 16384 + (8 * t + s) * 32, SC1);
        if (l < 8) __hip_atomic_fetch_add((u32*)(ws + CNT_OFF + (l * 8 + (b & 7)) * 128), 1u, __ATOMIC_RELAXED, __HIP_MEMORY_SCOPE_AGENT);
        STAMP(26 + 30 * ly);
#pragma unroll 1
        for (int sp = 0; sp < SPIN_CAP; ++sp) {
          CBAR();
          const u32 f = LD32((l & 7) * 128, CNT_OFF + (b & 7) * 1024, SC1);
          if (__all(f == 32u)) break;
          __builtin_amdgcn_s_sleep(1);
        }
        CBAR();
      }
      STAMP(27 + 30 * ly);
      __syncthreads();
      const int cc = (tid >> 3) & 1, q8 = (tid >> 4) * 8 + (tid & 7);
      v4u m0, r0, m1, r1; v2u xa, xc;
#pragma unroll 1
      for (int sp = 0; sp < SPIN_CAP; ++sp) {
        CBAR();
        xa = LD64(cc * 4096 + q8 * 8, XC_OFF + (2 << 20) + b * 8192, SC1);
        xc = LD64(cc * 4096 + q8 * 8 + 2048, XC_OFF + (2 << 20) + b * 8192, SC1);
        m0 = LD128(q8 * 32, FST_OFF + 16384, SC1); r0 = LD128(q8 * 32 + 16, FST_OFF + 16384, SC1);
        m1 = LD128(q8 * 32, FST_OFF + 24576, SC1); r1 = LD128(q8 * 32 + 16, FST_OFF + 24576, SC1);
        if (__all(umax(umax(umax(xa[0], xa[1]), umax(xc[0], xc[1])), umax(umax(max4(m0), max4(r0)), umax(max4(m1), max4(r1)))) != 0xffffffffu)) break;
        __builtin_amdgcn_s_sleep(1);
      }
      const float dk = *(const LAS float*)(sm + L_DK + cc * 8);
      xr0 = conv_ln(sm + L_U + cc * U_CH + 512 + q8 * 8, xa, m0, r0, dk);
      xr1 = conv_ln(sm + L_U + cc * U_CH + 2560 + q8 * 8, xc, m1, r1, dk);
    }
  }
}

__global__ __launch_bounds__(512, 2) void k_main(const u16* __restrict__ dskip, const u16* __restrict__ b1,
    const u16* __restrict__ b2, char* __restrict__ ws, u16* __restrict__ out) {
  extern __shared__ __attribute__((aligned(16))) char smem[];
  LAS char* sm = (LAS char*)smem;
  const int b0 = blockIdx.x, w0 = __builtin_amdgcn_readfirstlane(threadIdx.x >> 6);
  const __amdgpu_buffer_rsrc_t rs = __builtin_amdgcn_make_buffer_rsrc(ws, 0, WS_BYTES, 0x00020000);
  STAMP(0);
  if (w0 == 7) main_body<true>(sm, rs, ws, out, dskip, b1, b2, b0, w0);
  else main_body<false>(sm, rs, ws, out, dskip, b1, b2, b0, w0);
  STAMP_FLUSH(1);
}

extern "C" void kernel_launch(void* const* d_in, const int* in_sizes, int n_in, void* d_out, int out_size, void* d_ws,
                              size_t ws_size, hipStream_t stream) {
  (void)hipFuncSetAttribute((const void*)k_main, hipFuncAttributeMaxDynamicSharedMemorySize, MAIN_LDS);
  k_prep<<<256, 512, 0, stream>>>((const u16*)d_in[0], (const u16*)d_in[1], (const u16*)d_in[2], (const u16*)d_in[3],
                                  (const u16*)d_in[4], (const u16*)d_in[5], (const u16*)d_in[8], (const u16*)d_in[10], (char*)d_ws);
  k_main<<<256, 512, MAIN_LDS, stream>>>((const u16*)d_in[6], (const u16*)d_in[9], (const u16*)d_in[11], (char*)d_ws, (u16*)d_out);
}
```

```cpp
#include <hip/hip_runtime.h>
#include <hip/hip_bf16.h>

typedef unsigned int u32;
typedef unsigned short u16;
typedef __attribute__((ext_vector_type(4))) u32 v4u;
typedef __attribute__((ext_vector_type(2))) u32 v2u;
typedef __attribute__((ext_vector_type(4))) float f32x4;
typedef __attribute__((ext_vector_type(2))) float f32x2;
typedef __attribute__((ext_vector_type(16))) float f32x16;
typedef __attribute__((ext_vector_type(8))) __bf16 bf16x8;
typedef __attribute__((ext_vector_type(2))) __bf16 bf16x2;
typedef __attribute__((ext_vector_type(8))) _Float16 f16x8;
typedef __attribute__((ext_vector_type(2))) _Float16 f16x2;
typedef __attribute__((ext_vector_type(4))) short s16x4;
#define LAS __attribute__((address_space(3)))
#define DI __device__ __forceinline__
#define STAMP(i)
#define STAMP_FLUSH(kid)

constexpr int SL = 2048, DM = 512, NS = 128, FF = 1792;
constexpr int XC_OFF = 0;
constexpr int XM_OFF = 4 << 20;
constexpr int PRM_OFF = 8 << 20;
constexpr int KSC_OFF = PRM_OFF + DM * 2560;
constexpr int CS_OFF = KSC_OFF + 2048;
constexpr int W1F_OFF = 11 << 20;
constexpr int W2F_OFF = 13 << 20;
constexpr int FST_OFF = 15 << 20;
constexpr int CTL_OFF = FST_OFF + 32768;
constexpr int CNT_OFF = CTL_OFF;
constexpr int HF_OFF = CNT_OFF + 8192;
constexpr int PST_OFF = HF_OFF + 8192;
constexpr int CTL_BYTES = 8192 + 8192 + 262144;
constexpr int H_OFF = 16 << 20;
constexpr int H_LAYER = 32 * 8 * 2 * 14 * 1024;
constexpr int WS_BYTES = 32 << 20;
constexpr int SC1 = 16;
constexpr int SPIN_CAP = 1 << 16;
#define K0_AUX 0

DI u32 umin(u32 a, u32 b) { return a < b ? a : b; }
DI u32 umax(u32 a, u32 b) { return a > b ? a : b; }
DI float bf2f(u32 h) { return __builtin_bit_cast(float, h << 16); }
DI float bflo(u32 p) { return __builtin_bit_cast(float, p << 16); }
DI float bfhi(u32 p) { return __builtin_bit_cast(float, p & 0xffff0000u); }
DI u32 pack_bf16(float a, float b) { bf16x2 v = {(__bf16)a, (__bf16)b}; return __builtin_bit_cast(u32, v); }
DI float bfr(float a) { return bflo(pack_bf16(a, a)); }
DI u32 pack_f16(float a, float b) { f16x2 v = {(_Float16)a, (_Float16)b}; return __builtin_bit_cast(u32, v); }
DI float dot2bf(u32 a, u32 b, float c) {
  return __builtin_amdgcn_fdot2_f32_bf16(__builtin_bit_cast(bf16x2, a), __builtin_bit_cast(bf16x2, b), c, false);
}
DI f32x16 mfma32(v4u a, v4u b, f32x16 c) {
  return __builtin_amdgcn_mfma_f32_32x32x16_bf16(__builtin_bit_cast(bf16x8, a), __builtin_bit_cast(bf16x8, b), c, 0, 0, 0);
}
DI f32x4 mfma16h(v4u a, v4u b, f32x4 c) {
  return __builtin_amdgcn_mfma_f32_16x16x32_f16(__builtin_bit_cast(f16x8, a), __builtin_bit_cast(f16x8, b), c, 0, 0, 0);
}
DI f32x4 mfma16b(v4u a, v4u b, f32x4 c) {
  return __builtin_amdgcn_mfma_f32_16x16x32_bf16(__builtin_bit_cast(bf16x8, a), __builtin_bit_cast(bf16x8, b), c, 0, 0, 0);
}
DI v4u tr_frag(const LAS char* p) {
  const s16x4 a = __builtin_amdgcn_ds_read_tr16_b64_v4i16((LAS s16x4*)p);
  const s16x4 c = __builtin_amdgcn_ds_read_tr16_b64_v4i16((LAS s16x4*)(p + 256));
  return __builtin_bit_cast(v4u, __builtin_shufflevector(a, c, 0, 1, 2, 3, 4, 5, 6, 7));
}
template <int CTRL> DI float dpp_add(float v) {
  const int o = __builtin_amdgcn_update_dpp(0, __builtin_bit_cast(int, v), CTRL, 0xf, 0xf, false);
  return v + __builtin_bit_cast(float, o);
}
DI float row_sum16(float v) { v = dpp_add<0x128>(v); v = dpp_add<0x124>(v); v = dpp_add<0x122>(v); return dpp_add<0x121>(v); }
DI float swap32_add(float a, float c) {
  const auto r = __builtin_amdgcn_permlane32_swap(__builtin_bit_cast(u32, a), __builtin_bit_cast(u32, c), false, false);
  const u32 r0 = r[0], r1 = r[1];
  return __builtin_bit_cast(float, r0) + __builtin_bit_cast(float, r1);
}
DI float swap16_add(float a, float c) {
  const auto r = __builtin_amdgcn_permlane16_swap(__builtin_bit_cast(u32, a), __builtin_bit_cast(u32, c), false, false);
  const u32 r0 = r[0], r1 = r[1];
  return __builtin_bit_cast(float, r0) + __builtin_bit_cast(float, r1);
}
DI float wave_sum(float v) { v = row_sum16(v); v = swap16_add(v, v); return swap32_add(v, v); }
DI void ln_stats(float s, float q, float& m, float& v, float& mb, float& rs) {
  m = s * (1.0f / DM);
  v = fmaxf(q * (1.0f / DM) - m * m, 0.f);
  const u32 pk = pack_bf16(m, __builtin_amdgcn_sqrtf(bfr(bfr(v) + 1e-5f)));
  mb = bflo(pk); rs = __builtin_amdgcn_rcpf(bfhi(pk));
}
DI u32 npz(float a) { return umin(__builtin_bit_cast(u32, a), 0xfffffffeu); }
#define LD128(vo, so, aux) __builtin_amdgcn_raw_buffer_load_b128(rs, (vo), (so), (aux))
#define LD64(vo, so, aux) __builtin_amdgcn_raw_buffer_load_b64(rs, (vo), (so), (aux))
#define LD32(vo, so, aux) __builtin_amdgcn_raw_buffer_load_b32(rs, (vo), (so), (aux))
#define ST128(v, vo, so, aux) __builtin_amdgcn_raw_buffer_store_b128((v), rs, (vo) + (so), 0, (aux))
#define ST64(v, vo, so, aux) __builtin_amdgcn_raw_buffer_store_b64((v), rs, (vo), (so), (aux))
#define ST32(v, vo, so, aux) __builtin_amdgcn_raw_buffer_store_b32((v), rs, (vo), (so), (aux))
#define DRAIN() asm volatile("s_waitcnt vmcnt(0)" ::: "memory")
#define CBAR() asm volatile("" ::: "memory")
#define SBAR() __builtin_amdgcn_sched_barrier(0)
DI int opaque_v(int x) { asm volatile("" : "+v"(x)); return x; }
template <typename T, int N> DI void undef_regs(T (&a)[N]) {
#pragma unroll
  for (int i = 0; i < N; ++i) asm volatile("" : "=v"(a[i]));
}
DI int opaque_s(int x) { asm volatile("" : "+s"(x)); return x; }

DI void cexp_p(float arl, float rh, float rl, float p, float& re, float& im) {
  const float e = __builtin_amdgcn_exp2f(arl * p);
  const float r = __builtin_fmaf(rl, p, __builtin_amdgcn_fractf(rh * p));
  re = e * __builtin_amdgcn_cosf(r);
  im = e * __builtin_amdgcn_sinf(r);
}
DI float k_scale(float bs, int sign) {
  const int ex = (int)((__builtin_bit_cast(u32, bs) >> 23) & 0xff) - 126;
  int e = 15 - ex; e = e > 100 ? 100 : (e < -100 ? -100 : e);
  return __builtin_bit_cast(float, (u32)(127 + sign * e) << 23);
}
__global__ __launch_bounds__(512, 2) void k_prep(const u16* __restrict__ x0, const u16* __restrict__ logA,
    const u16* __restrict__ Aim, const u16* __restrict__ Cre, const u16* __restrict__ Cim,
    const u16* __restrict__ logdt, const u16* __restrict__ w1, const u16* __restrict__ w2, char* __restrict__ ws) {
  __shared__ float bnd[4];
  __shared__ float csp[8][32];
  __shared__ u32 trt[64 * 33];
  const int b = blockIdx.x, tid = threadIdx.x, l = tid & 63, w = __builtin_amdgcn_readfirstlane(tid >> 6);
  const __amdgpu_buffer_rsrc_t rs = __builtin_amdgcn_make_buffer_rsrc(ws, 0, WS_BYTES, 0x00020000);
  STAMP(0);
  u32 rla = 0, rai = 0, rcr = 0, rci = 0, rdt = 0;
  if (w < 4) {
    const int o = 2 * b * NS + tid;
    rla = logA[o]; rai = Aim[o]; rcr = Cre[o]; rci = Cim[o]; rdt = logdt[2 * b + (w >> 1)];
  }
  const v4u xr = *(const v4u*)(x0 + (8 * b + w) * DM + 8 * l);
  const v4u xt = *(const v4u*)(x0 + (64 * (b >> 3) + (tid >> 3)) * DM + 64 * (b & 7) + 8 * (tid & 7));
  u32 a1[8], a2[8];
  const int h = l >> 5, c32 = l & 31;
  const int fr = (b < 112 ? b : b - 112) * 16 + 2 * w;
  if (b < 112) {
    const u16* p = w1 + (16 * (fr & 31) + 8 * h) * FF + 32 * (fr >> 5) + c32;
#pragma unroll
    for (int j = 0; j < 8; ++j) { a1[j] = p[j * FF]; a2[j] = p[(16 + j) * FF]; }
  } else if (b < 224) {
    const int r1 = fr >> 1, ks2 = r1 % 14, r2 = r1 / 14;
    const u16* p = w2 + (224 * (r2 & 7) + 16 * ks2 + 4 * h) * DM + 64 * (r2 >> 3) + c32;
#pragma unroll
    for (int j = 0; j < 8; ++j) { a1[j] = p[(8 * (j >> 2) + (j & 3)) * DM]; a2[j] = p[(8 * (j >> 2) + (j & 3)) * DM + 32]; }
  }
  ST128((v4u{~0u, ~0u, ~0u, ~0u}), tid * 16, XM_OFF + b * 8192, K0_AUX);
  if (tid >= 128 && tid < 132) ST128((v4u{~0u, ~0u, ~0u, ~0u}), (tid - 128) * 16, FST_OFF + 16384 + b * 64, K0_AUX);
  if (tid < CTL_BYTES / 16 / 256) ST128((v4u{0u, 0u, 0u, 0u}), tid * 16, CTL_OFF + b * (CTL_BYTES / 256), K0_AUX);
  STAMP(1);
  float arl = 0.f, rh = 0.f, rl = 0.f, cr = 0.f, ci = 0.f;
  if (w < 4) {
    const float dt = bfr(expf(bf2f(rdt)));
    arl = -bfr(expf(bf2f(rla))) * dt * 1.4426950408889634f;
    double t = (double)(bf2f(rai) * dt) * 0.15915494309189533577;
    t -= __builtin_rint(t);
    rh = __builtin_bit_cast(float, __builtin_bit_cast(u32, (float)t) & 0xfffff000u);
    rl = (float)(t - (double)rh);
    cr = bf2f(rcr) * dt * 2.0f; ci = bf2f(rci) * dt * 2.0f;
    const float bs = wave_sum(fabsf(cr) + fabsf(ci));
    if (l == 0) bnd[w] = bs;
  }
  STAMP(2);
  {
    float s = 0.f, q = 0.f;
#pragma unroll
    for (int j = 0; j < 4; ++j) {
      const u32 p = xr[j];
      s = dot2bf(p, 0x3f803f80u, s); q = dot2bf(p, p, q);
      trt[(tid >> 3) * 33 + 4 * (tid & 7) + j] = xt[j];
    }
    s = wave_sum(s); q = wave_sum(q);
    float m, v, mb, rcs;
    ln_stats(s, q, m, v, mb, rcs);
    if (l < 2) ST32(__builtin_bit_cast(u32, l ? rcs : mb), l * 16, FST_OFF + (2 * b + (w >> 2)) * 32 + (w & 3) * 4, K0_AUX);
  }
  if (b < 224) {
    v4u o1, o2;
    float cs = 0.f;
#pragma unroll
    for (int j = 0; j < 4; ++j) {
      const u32 p1 = a1[2 * j] | (a1[2 * j + 1] << 16), p2 = a2[2 * j] | (a2[2 * j + 1] << 16);
      o1[j] = p1; o2[j] = p2;
      cs = dot2bf(p1, 0x3f803f80u, cs); cs = dot2bf(p2, 0x3f803f80u, cs);
    }
    ST128(o1, l * 16, (b < 112 ? W1F_OFF : W2F_OFF) + fr * 1024, K0_AUX);
    ST128(o2, l * 16 + 1024, (b < 112 ? W1F_OFF : W2F_OFF) + fr * 1024, K0_AUX);
    cs = swap32_add(cs, cs);
    if (l < 32) csp[w][l] = cs;
  }
  STAMP(5);
  __syncthreads();
  STAMP(6);
  {
    if (w < 4) {
      const float bs = bnd[w & 2] + bnd[(w & 2) + 1], sc = k_scale(bs, 1);
      const int vo = (tid >> 2) * 80 + (tid & 3) * 4;
      ST32(__builtin_bit_cast(u32, arl), vo, PRM_OFF + b * 5120, K0_AUX);
      ST32(__builtin_bit_cast(u32, rh), vo + 16, PRM_OFF + b * 5120, K0_AUX);
      ST32(__builtin_bit_cast(u32, rl), vo + 32, PRM_OFF + b * 5120, K0_AUX);
      ST32(__builtin_bit_cast(u32, cr * sc), vo + 48, PRM_OFF + b * 5120, K0_AUX);
      ST32(__builtin_bit_cast(u32, ci * sc), vo + 64, PRM_OFF + b * 5120, K0_AUX);
      if ((tid & 127) == 0) ST32(__builtin_bit_cast(u32, k_scale(bs, -1)), (tid >> 7) * 4, KSC_OFF + 8 * b, K0_AUX);
    }
    if (b < 112 && tid < 32) {
      const float cs = ((csp[0][tid] + csp[1][tid]) + (csp[2][tid] + csp[3][tid])) + ((csp[4][tid] + csp[5][tid]) + (csp[6][tid] + csp[7][tid]));
      ST32(__builtin_bit_cast(u32, cs), tid * 4, CS_OFF + (b & 1) * (FF * 4) + (b >> 1) * 128, K0_AUX);
    }
    const int cq = tid >> 3, oct = tid & 7;
    const u16* t16 = (const u16*)trt;
    v4u o;
#pragma unroll
    for (int j = 0; j < 4; ++j)
      o[j] = (u32)t16[(8 * oct + 2 * j) * 66 + cq] | ((u32)t16[(8 * oct + 2 * j + 1) * 66 + cq] << 16);
    ST128(o, cq * 4096 + oct * 16, XC_OFF + (b & 7) * (64 * 4096) + (b >> 3) * 128, K0_AUX);
  }
  STAMP(8);
  STAMP_FLUSH(0);
}

constexpr int KC_CH = 8 * 260 * 16;
constexpr int L_U = 2 * KC_CH;
constexpr int U_CH = 4672;
constexpr int L_R = L_U + 2 * U_CH;
constexpr int L_XB = L_R + 32768;
constexpr int L_PB = L_XB + 256;
constexpr int L_KT = L_R + 24576 + 16;
constexpr int KT_CH = 1312;
constexpr int RB_ROW = 144;
constexpr int L_S = L_R + 8 * 64 * RB_ROW;
constexpr int L_B1 = L_S;
constexpr int L_CS = L_B1 + 896;
constexpr int L_SP = L_CS + 896;
constexpr int L_AC = L_SP + 4096;
constexpr int L_B2 = L_AC + 64;
constexpr int L_DK = L_B2 + 256;
constexpr int L_AB = L_DK + 16;
constexpr int L_END = L_AB + 512;
constexpr int MAIN_LDS = L_END;
#define MAPV 1
#define MAP_T(b) ((((b) >> 1) & 3) * 8 + ((b) >> 5))
#define MAP_S(b) (((b) & 1) * 4 + (((b) >> 3) & 3))
#define W1_SPLIT 0
constexpr int W2A = 12;

DI f32x2 gelu2(f32x2 x) {
  const f32x2 t = x * (x * x * -0.10294324f + -2.3022082f);
  f32x2 e; e.x = __builtin_amdgcn_exp2f(t.x); e.y = __builtin_amdgcn_exp2f(t.y);
  e = e + 1.0f;
  f32x2 r; r.x = __builtin_amdgcn_rcpf(e.x); r.y = __builtin_amdgcn_rcpf(e.y);
  return x * r;
}
DI u32 max4(v4u a) { return umax(umax(a[0], a[1]), umax(a[2], a[3])); }
DI f32x4 unpack4(v2u p) { const u32 a = p[0], c = p[1]; return f32x4{bflo(a), bfhi(a), bflo(c), bfhi(c)}; }
DI f32x4 conv_ln(LAS char* dst, v2u xp, v4u mm, v4u rr, float dk) {
  const f32x4 x = unpack4(xp);
  const f32x4 u = (x - __builtin_bit_cast(f32x4, mm)) * __builtin_bit_cast(f32x4, rr);
  *(LAS v2u*)dst = v2u{pack_f16(u[0], u[1]), pack_f16(u[2], u[3])};
  return x + u * dk;
}

template <bool COMM>
DI void main_body(LAS char* sm, const __amdgpu_buffer_rsrc_t rs, char* ws, u16* out, const u16* dskip, const u16* b1,
                  const u16* b2, int b0, int w0) {
  v4u wf[32], wa[W2A];
  f32x4 xr0, xr1;
#define LOAD_W(lo, hi) do { _Pragma("unroll") for (int ks = (lo); ks < (hi); ++ks) { \
      if constexpr (!COMM) wf[ks] = LD128(l16 + (ks & 3) * 1024, W1F_OFF + (7 * s + w) * 32768 + (ks >> 2) * 4096, 0); \
      else if (ks < 28) wf[ks] = LD128(l16 + (ks & 3) * 1024, W2F_OFF + (s * 8 + 7) * 28672 + (ks >> 2) * 4096, 0); } \
      SBAR(); } while (0)
  {
    const int b = opaque_s(b0), w = opaque_s(w0), tid = opaque_v(threadIdx.x), l = tid & 63, l16 = l * 16, s = MAP_S(b);
    const int cc = (tid >> 3) & 1, q8 = (tid >> 4) * 8 + (tid & 7);
    const int kk = l >> 4, i16 = l & 15;
    v4u pv[2][5];
#pragma unroll
    for (int g = 0; g < 2; ++g)
#pragma unroll
      for (int f = 0; f < 5; ++f) pv[g][f] = LD128(kk * 80 + g * 320 + f * 16, PRM_OFF + b * 5120 + w * 640, 0);
    const v2u xa = LD64(cc * 4096 + q8 * 8, XC_OFF + b * 8192, 0), xb = LD64(cc * 4096 + q8 * 8 + 2048, XC_OFF + b * 8192, 0);
    const v4u m0 = LD128(q8 * 32, FST_OFF, 0), r0 = LD128(q8 * 32 + 16, FST_OFF, 0);
    const v4u m1 = LD128(q8 * 32, FST_OFF + 8192, 0), r1 = LD128(q8 * 32 + 16, FST_OFF + 8192, 0);
    const float dk = bf2f(dskip[2 * b + cc]);
    const float ksc = __builtin_bit_cast(float, LD32(cc * 4, KSC_OFF + 8 * b, 0));
    const float bias2 = bf2f(b2[64 * s + l]);
    const float bb1 = bf2f(b1[224 * s + (tid < 224 ? tid : 0)]);
    const float cs = __builtin_bit_cast(float, LD32(tid * 4, CS_OFF + s * 896, 0)) + __builtin_bit_cast(float, LD32(tid * 4, CS_OFF + FF * 4 + s * 896, 0));
    SBAR();
    if (tid < 16) *(LAS v4u*)(sm + (tid >> 3) * KC_CH + (tid & 7) * 4160) = v4u{0u, 0u, 0u, 0u};
    if (tid >= 64 && tid < 128) *(LAS v4u*)(sm + L_U + ((tid >> 5) & 1) * U_CH + (tid & 31) * 16) = v4u{0u, 0u, 0u, 0u};
    if (tid >= 128 && tid < 130) *(LAS v4u*)(sm + L_KT - 16 + (tid - 128) * KT_CH) = v4u{0u, 0u, 0u, 0u};
    const int c = tid >> 8, x = tid & 255;
    int tix, gix[8];
    {
      const int col = x >> 4, row = x & 15;
      const int p0 = 128 * (row >> 3) + (row & 7) + 8 * col;
      const int p1 = (row < 8 ? 256 + 128 * (row >> 2) + 2 * (row & 3) : 512 + 128 * ((row - 8) >> 1) + 4 * (row & 1)) + 8 * col;
      const int c1 = row < 8 ? 128 + (p1 >> 1) : 256 + (p1 >> 2);
      const int c2 = row < 8 ? 512 + 16 * row + col : (x == 8 ? 640 : 641);
      tix = opaque_v(p0 | (c1 << 10) | (c2 << 20));
#pragma unroll
      for (int j = 0; j < 8; ++j) {
        const int la = 8 * x + 7 - 2 * j, lb = la - 1;
        const u32 hb = pack_bf16((float)la, (float)lb);
        const int ia = la < 128 ? la : (int)(hb & 0xffffu) - 17024, ib = lb < 128 ? lb : (int)(hb >> 16) - 17024;
        gix[j] = opaque_v((L_KT + c * KT_CH + 2 * ia - L_R) | ((L_KT + c * KT_CH + 2 * ib - L_R) << 16));
      }
    }
    const float pB = (float)(8 * i16);
    const float pA0 = (float)(128 * (i16 >> 3) + (i16 & 7));
    const float pA1 = (float)(i16 < 8 ? 256 + 128 * (i16 >> 2) + 2 * (i16 & 3) : 512 + 128 * ((i16 - 8) >> 1) + 4 * (i16 & 1));
    const float pA2 = (float)(i16 < 8 ? 1024 + 128 * i16 : 2048);
    f32x4 acc0 = {0.f, 0.f, 0.f, 0.f}, acc1 = acc0, acc2 = acc0;
    STAMP(1);
#pragma unroll
    for (int g = 0; g < 2; ++g) {
      v4u fa0, fa1, fa2, fbh, fbl;
#pragma unroll
      for (int j = 0; j < 4; ++j) {
        const u32 u0 = pv[g][0][j], u1 = pv[g][1][j], u2 = pv[g][2][j], u3 = pv[g][3][j], u4 = pv[g][4][j];
        const float arl = __builtin_bit_cast(float, u0), rh = __builtin_bit_cast(float, u1), rl = __builtin_bit_cast(float, u2);
        const float cr = __builtin_bit_cast(float, u3), ci = __builtin_bit_cast(float, u4);
        float br, bi, er, ei;
        cexp_p(arl, rh, rl, pB, br, bi);
        bi = -bi;
        const u32 hi = pack_f16(br, bi);
        const f16x2 hv = __builtin_bit_cast(f16x2, hi);
        fbh[j] = hi;
        fbl[j] = pack_f16(br - (float)hv.x, bi - (float)hv.y);
        cexp_p(arl, rh, rl, pA0, er, ei);
        fa0[j] = pack_f16(cr * er - ci * ei, cr * ei + ci * er);
        cexp_p(arl, rh, rl, pA1, er, ei);
        fa1[j] = pack_f16(cr * er - ci * ei, cr * ei + ci * er);
        cexp_p(arl, rh, rl, pA2, er, ei);
        fa2[j] = pack_f16(cr * er - ci * ei, cr * ei + ci * er);
        SBAR();
        LOAD_W(16 * g + 4 * j, 16 * g + 4 * j + 4);
      }
      acc0 = mfma16h(fa0, fbh, acc0); acc1 = mfma16h(fa1, fbh, acc1); acc2 = mfma16h(fa2, fbh, acc2);
      acc0 = mfma16h(fa0, fbl, acc0); acc1 = mfma16h(fa1, fbl, acc1); acc2 = mfma16h(fa2, fbl, acc2);
    }
    *(LAS f32x4*)(sm + L_R + w * 3072 + i16 * 64 + kk * 16) = acc0;
    *(LAS f32x4*)(sm + L_R + w * 3072 + 1024 + i16 * 64 + kk * 16) = acc1;
    *(LAS f32x4*)(sm + L_R + w * 3072 + 2048 + i16 * 64 + kk * 16) = acc2;
    STAMP(60);
    xr0 = conv_ln(sm + L_U + cc * U_CH + 512 + q8 * 8, xa, m0, r0, dk);
    xr1 = conv_ln(sm + L_U + cc * U_CH + 2560 + q8 * 8, xb, m1, r1, dk);
    if (tid < 224) { *(LAS float*)(sm + L_B1 + tid * 4) = bb1; *(LAS float*)(sm + L_CS + tid * 4) = cs; }
    if (tid < 64) *(LAS float*)(sm + L_B2 + tid * 4) = bias2;
    if ((tid & ~8) == 0) *(LAS f32x2*)(sm + L_DK + cc * 8) = f32x2{dk, ksc};
    STAMP(61);
    __syncthreads();
    {
      const LAS float* pp = (const LAS float*)(sm + L_R + c * 12288) + x;
      LAS _Float16* kt = (LAS _Float16*)(sm + L_KT + c * KT_CH);
      kt[tix & 1023] = (_Float16)((pp[0] + pp[768]) + (pp[1536] + pp[2304]));
      kt[(tix >> 10) & 1023] = (_Float16)((pp[256] + pp[1024]) + (pp[1792] + pp[2560]));
      kt[tix >> 20] = (_Float16)((pp[512] + pp[1280]) + (pp[2048] + pp[2816]));
    }
    __syncthreads();
    STAMP(62);
    {
      u32 wd[8];
#pragma unroll
      for (int j = 0; j < 8; ++j)
        wd[j] = (u32)*(const LAS u16*)(sm + L_R + (gix[j] & 0xffff)) | ((u32)*(const LAS u16*)(sm + L_R + ((u32)gix[j] >> 16)) << 16);
#pragma unroll
      for (int cp = 0; cp < 8; ++cp) {
        const int sg = 7 - cp;
        v4u o;
#pragma unroll
        for (int k = 0; k < 4; ++k)
          o[k] = (sg & 1) ? __builtin_amdgcn_alignbit(wd[(sg + 1) / 2 + k], wd[(sg - 1) / 2 + k], 16) : wd[sg / 2 + k];
        *(LAS v4u*)(sm + c * KC_CH + cp * 4160 + (x + 1) * 16) = o;
      }
    }
  }

#pragma unroll 1
  for (int ly = 0; ly < 2; ++ly) {
    const int b = opaque_s(b0), w = opaque_s(w0);
    const int t = MAP_T(b), s = MAP_S(b);
    int tid = opaque_v(threadIdx.x), l = tid & 63, l16 = l * 16;
    const int xmo = XM_OFF + ly * (2 << 20) + t * 32768 + w * 1024;
    const int w2off = W2F_OFF + ((s * 8 + w) * 28) * 1024;
    const int hoff = H_OFF + ly * H_LAYER + (t * 8 + s) * (28 * 1024) + (2 * w) * 1024;
    v4u xs[4];
    auto x_load = [&](int half) {
#pragma unroll
      for (int k = 0; k < 4; ++k) xs[k] = LD128(l16, xmo + half * (1 << 20) + k * 8192, SC1);
    };
    auto x_ok = [&]() { return __all(umax(umax(max4(xs[0]), max4(xs[1])), umax(max4(xs[2]), max4(xs[3]))) != 0xffffffffu); };
    auto x_stage = [&](int half) {
      if (!x_ok()) {
        int sp = 0;
#pragma unroll 1
        do {
          __builtin_amdgcn_s_sleep(4);
          CBAR();
          x_load(half);
        } while (!x_ok() && ++sp < SPIN_CAP);
      }
#pragma unroll
      for (int k = 0; k < 4; ++k) *(LAS v4u*)(sm + L_R + half * 32768 + k * 8192 + tid * 16) = xs[k];
    };
    STAMP(2 + 30 * ly);
    __syncthreads();
    STAMP(3 + 30 * ly);
    {
      const int c16 = l & 15, g4 = l >> 4;
      const LAS char* pa = sm + (w >> 2) * KC_CH + (c16 & 7) * 4160 + (3 + (c16 >> 3) - g4) * 16 + (w & 3) * 128;
      const LAS char* pb = sm + L_U + (w >> 2) * U_CH + (30 + 2 * c16 + g4) * 16 - (w & 3) * 128;
      LAS char* pp = sm + L_R + w * 4096 + (w >> 2) * 128 + ((4 * c16 + g4) ^ ((c16 >> 1) & 3)) * 16;
      const int xmv = (tid >> 4) * 32768 + (tid & 15) * 8;
      const LAS char* pr = sm + L_R + ((tid >> 3) & 1) * 16512 + ((((tid >> 4) << 3) | (tid & 7)) ^ ((tid >> 4) & 3)) * 16;
      auto conv_out = [&](int half, f32x4 xr) {
        const LAS char* p = pr + (half ? L_PB - L_R : 0);
        const float ksc = *(const LAS float*)(sm + L_DK + ((tid >> 3) & 1) * 8 + 4);
        const f32x4 y = (*(const LAS f32x4*)p + *(const LAS f32x4*)(p + 4096)) + (*(const LAS f32x4*)(p + 8192) + *(const LAS f32x4*)(p + 12288));
        const f32x4 o = y * ksc + xr;
        ST64((v2u{umin(pack_bf16(o[0], o[1]), 0xfffffffeu), umin(pack_bf16(o[2], o[3]), 0xfffffffeu)}), xmv,
             XM_OFF + ly * (2 << 20) + half * (1 << 20) + 128 * b, SC1);
      };
      {
        f32x4 acc[4];
#pragma unroll
        for (int i = 0; i < 4; ++i) acc[i] = f32x4{0.f, 0.f, 0.f, 0.f};
        v4u fa[2][4], fb[2][4];
#pragma unroll
        for (int tau = 0; tau < 2; ++tau)
#pragma unroll
          for (int i = 0; i < 4; ++i) { fa[tau][i] = *(const LAS v4u*)(pa + i * 512 + tau * 64); fb[tau][i] = *(const LAS v4u*)(pb + i * 512 - tau * 64); }
#pragma unroll
        for (int tau = 0; tau < 2; ++tau)
#pragma unroll
          for (int sx = 0; sx < 4; ++sx)
#pragma unroll
            for (int e = 0; e + sx < 4; ++e) acc[sx + e] = mfma16h(fa[tau][sx], fb[tau][e], acc[sx + e]);
#pragma unroll
        for (int i = 0; i < 4; ++i) *(LAS f32x4*)(pp + i * 1024) = acc[i];
      }
      v4u fa[8], fb[8];
#pragma unroll
      for (int i = 0; i < 8; ++i) { fb[i] = *(const LAS v4u*)(pb + i * 512); fa[7 - i] = *(const LAS v4u*)(pa + (7 - i) * 512); }
      STAMP(4 + 30 * ly);
      __syncthreads();
      conv_out(0, xr0);
      STAMP(5 + 30 * ly);
      if constexpr (!COMM) {
        const u32 on = (u32)opaque_v(-1);
#pragma unroll
        for (int k = 0; k < 4; ++k) ST128((v4u{on, on, on, on}), l16 + (k & 1) * 1024, hoff + (k >> 1) * 14336, SC1);
        SBAR();
      }
      {
        f32x4 acc[4];
#pragma unroll
        for (int i = 0; i < 4; ++i) acc[i] = f32x4{0.f, 0.f, 0.f, 0.f};
#pragma unroll
        for (int e = 0; e < 8; ++e) {
#pragma unroll
          for (int i = 0; i < 4; ++i) if (4 + i - e >= 0) acc[i] = mfma16h(fa[4 + i - e], fb[e], acc[i]);
          fb[e] = *(const LAS v4u*)(pb + e * 512 - 64); fa[7 - e] = *(const LAS v4u*)(pa + (7 - e) * 512 + 64);
          SBAR();
        }
#pragma unroll
        for (int e = 0; e < 8; ++e)
#pragma unroll
          for (int i = 0; i < 4; ++i) if (4 + i - e >= 0) acc[i] = mfma16h(fa[4 + i - e], fb[e], acc[i]);
#pragma unroll
        for (int i = 0; i < 4; ++i) *(LAS f32x4*)(pp + (L_PB - L_R) + i * 1024) = acc[i];
      }
      STAMP(6 + 30 * ly);
      __syncthreads();
      x_load(0);
      SBAR();
      conv_out(1, xr1);
      STAMP(7 + 30 * ly);
      x_stage(0);
      if (ly == 0) {
        const u32 on = (u32)opaque_v(-1);
        ST64((v2u{on, on}), xmv, XM_OFF + (2 << 20) + 128 * b, SC1);
        ST64((v2u{on, on}), xmv, XM_OFF + (3 << 20) + 128 * b, SC1);
        ST64((v2u{on, on}), (tid >> 3) * 4096 + (tid & 7) * 8, XC_OFF + (2 << 20) + s * (64 * 4096) + t * 64, SC1);
        ST64((v2u{on, on}), (tid >> 3) * 4096 + (tid & 7) * 8, XC_OFF + (2 << 20) + s * (64 * 4096) + t * 64 + 2048, SC1);
      }
    }
    STAMP(11 + 30 * ly);
    __syncthreads();
    tid = opaque_v(threadIdx.x); l = tid & 63; l16 = l * 16;
    const int h = l >> 5;
    const LAS char* xb = sm + L_R + (8 * h + ((l & 15) >> 2)) * 64 + 32 * ((l >> 4) & 1) + 8 * (l & 3);
    auto stats = [&](int half) {
      const LAS char* xq = sm + L_R + half * 32768 + (8 * (l >> 4) + ((l & 15) >> 2)) * 64 + 8 * (l & 3);
      const u32 one = (u32)opaque_v(0x3f803f80);
      const v4u ones = {one, one, one, one};
      f32x4 s0 = {0.f, 0.f, 0.f, 0.f}, q0 = s0, s1 = s0, q1 = s0;
      v4u f0[4], f1[4];
#pragma unroll
      for (int k = 0; k < 4; ++k) { f0[k] = tr_frag(xq + k * 2048); f1[k] = tr_frag(xq + k * 2048 + 32); }
#pragma unroll
      for (int k = 0; k < 16; ++k) {
        s0 = mfma16b(ones, f0[k & 3], s0); q0 = mfma16b(f0[k & 3], f0[k & 3], q0);
        s1 = mfma16b(ones, f1[k & 3], s1); q1 = mfma16b(f1[k & 3], f1[k & 3], q1);
        if (k + 4 < 16) { f0[k & 3] = tr_frag(xq + (k + 4) * 2048); f1[k & 3] = tr_frag(xq + (k + 4) * 2048 + 32); }
        SBAR();
      }
      const float d0 = (l & 2) ? ((l & 1) ? q0[3] : q0[2]) : ((l & 1) ? q0[1] : q0[0]);
      const float d1 = (l & 2) ? ((l & 1) ? q1[3] : q1[2]) : ((l & 1) ? q1[1] : q1[0]);
#pragma unroll
      for (int g = 0; g < 2; ++g) {
        float m, v, mb, r1;
        ln_stats(g ? s1[0] : s0[0], g ? d1 : d0, m, v, mb, r1);
        const u32 p2 = pack_bf16((m - mb) * r1, v * r1 * r1);
        const float r2 = __builtin_amdgcn_rcpf(bfr(__builtin_amdgcn_sqrtf(bfr(bfhi(p2) + 1e-5f))));
        if ((l >> 4) == ((l >> 2) & 3)) *(LAS f32x2*)(sm + L_AB + (half * 32 + 16 * g + (l & 15)) * 8) = f32x2{r1 * r2, (mb * r1 + bflo(p2)) * r2};
      }
    };
    auto epi_quad = [&](const f32x16& acc, int q, f32x2 ab, u32& o0, u32& o1) {
      const f32x4 c4 = *(const LAS f32x4*)(sm + L_CS + (32 * w + 8 * q + 4 * h) * 4);
      const f32x4 b4 = *(const LAS f32x4*)(sm + L_B1 + (32 * w + 8 * q + 4 * h) * 4);
      const f32x4 x = f32x4{acc[4 * q], acc[4 * q + 1], acc[4 * q + 2], acc[4 * q + 3]} * ab.x + (b4 - c4 * ab.y);
      const f32x2 g0 = gelu2(f32x2{x[0], x[1]}), g1 = gelu2(f32x2{x[2], x[3]});
      o0 = umin(pack_bf16(g0.x, g0.y), 0xfffffffeu); o1 = umin(pack_bf16(g1.x, g1.y), 0xfffffffeu);
    };
    f32x16 acc0, acc1;
    constexpr int XD = 3;
    if constexpr (!COMM) {
      v4u xf[XD];
#pragma unroll
      for (int k = 0; k < XD; ++k) xf[k] = tr_frag(xb + k * 1024);
      acc0 = f32x16{};
#pragma unroll
      for (int ks = 0; ks < 32; ++ks) {
        acc0 = mfma32(wf[ks], xf[ks % XD], acc0);
        if (ks + XD < 32) xf[ks % XD] = tr_frag(xb + (ks + XD) * 1024);
        if (ks < 2 * W2A && !(ks & 1)) { const int k = ks >> 1; wa[k] = LD128(l16 + (k & 3) * 1024, w2off + (k >> 2) * 4096, 0); }
        if (ks == 15) x_load(1);
        SBAR();
      }
    } else {
      __builtin_amdgcn_s_setprio(2);
      stats(0);
      __builtin_amdgcn_s_setprio(0);
      x_load(1);
    }
    STAMP(12 + 30 * ly);
    x_stage(1);
    STAMP(13 + 30 * ly);
    __syncthreads();
    STAMP(14 + 30 * ly);
    const int orow = ly ? l : 8 * w + (l >> 3), oq = ly ? w : (l & 7);
    const v2u xres0 = *(const LAS v2u*)(sm + L_R + (64 * s + orow) * 64 + 8 * oq);
    const v2u xres1 = *(const LAS v2u*)(sm + L_XB + (64 * s + orow) * 64 + 8 * oq);
    const int hfo = HF_OFF + ly * 4096 + t * 128;
    if constexpr (!COMM) {
      v4u xf[XD];
#pragma unroll
      for (int k = 0; k < XD; ++k) xf[k] = tr_frag(xb + 32768 + k * 1024);
      const f32x2 ab = *(const LAS f32x2*)(sm + L_AB + (l & 31) * 8);
      u32 o[4];
      acc1 = f32x16{};
#pragma unroll
      for (int ks = 0; ks < 32; ++ks) {
        acc1 = mfma32(wf[ks], xf[ks % XD], acc1);
        if (ks + XD < 32) xf[ks % XD] = tr_frag(xb + 32768 + (ks + XD) * 1024);
        if (ks & 1) { const int j = ks >> 1, k = W2A + j; wf[j] = LD128(l16 + (k & 3) * 1024, w2off + (k >> 2) * 4096, 0); }
        if (ks % 6 == 1 && ks < 24) { const int q = ks / 6; epi_quad(acc0, q, ab, o[2 * (q & 1)], o[2 * (q & 1) + 1]); }
        if (ks == 11 || ks == 23) ST128((v4u{o[0], o[1], o[2], o[3]}), l16 + (ks / 12) * 1024, hoff, SC1);
        if (ks % 6 == 5 || ks >= 24) SBAR();
      }
    } else {
      if (l == 0) ST32(1u, 0, hfo + s * 4, SC1);
      __builtin_amdgcn_s_setprio(2);
      stats(1);
      __builtin_amdgcn_s_setprio(0);
#pragma unroll 1
      for (int sp = 0; sp < SPIN_CAP; ++sp) {
        CBAR();
        const u32 f = LD32((l & 7) * 4, hfo, SC1);
        if (__all(f == 1u)) break;
        __builtin_amdgcn_s_sleep(1);
      }
      CBAR();
    }
    STAMP(15 + 30 * ly);
    __syncthreads();
    v4u hf[14];
    const int hin = H_OFF + ly * H_LAYER + (t * 8 + w) * (28 * 1024);
    auto h_load = [&](int half, int lo, int hi) {
#pragma unroll
      for (int k = lo; k < hi; ++k) { hf[k] = LD128(l16, hin + (14 * half + k) * 1024, SC1); SBAR(); }
    };
    h_load(0, 0, 7);
    STAMP(16 + 30 * ly);
    if constexpr (!COMM) {
      const f32x2 ab = *(const LAS f32x2*)(sm + L_AB + (32 + (l & 31)) * 8);
#pragma unroll
      for (int sp = 0; sp < 2; ++sp) {
        u32 o[4];
        epi_quad(acc1, 2 * sp, ab, o[0], o[1]);
        epi_quad(acc1, 2 * sp + 1, ab, o[2], o[3]);
        ST128((v4u{o[0], o[1], o[2], o[3]}), l16 + sp * 1024, hoff + 14336, SC1);
      }
      SBAR();
    }
    h_load(0, 7, 14);
    STAMP(17 + 30 * ly);
    LAS char* rbw = sm + L_R + (w * 64 + (l & 31)) * RB_ROW + h * 16;
    auto g2_out = [&](int half, v2u xres) {
      const LAS char* rp = sm + L_R + orow * RB_ROW + oq * 16;
      f32x4 o4 = unpack4(xres) + *(const LAS float*)(sm + L_B2 + orow * 4);
      f32x4 p4 = *(const LAS f32x4*)rp;
#pragma unroll
      for (int ww = 1; ww < 8; ++ww) { const f32x4 v = *(const LAS f32x4*)(rp + ww * (64 * RB_ROW)); if (ww & 1) o4 += v; else p4 += v; }
      o4 += p4;
      u32 o0 = pack_bf16(o4[0], o4[1]), o1 = pack_bf16(o4[2], o4[3]);
      if (ly) {
        u16* op = out + (1024 * half + 32 * t + 4 * w) * DM + 64 * s + l;
        op[0] = (u16)(o0 & 0xffffu); op[DM] = (u16)(o0 >> 16); op[2 * DM] = (u16)(o1 & 0xffffu); op[3 * DM] = (u16)(o1 >> 16);
      } else {
        o0 = umin(o0, 0xfffffffeu); o1 = umin(o1, 0xfffffffeu);
        ST64((v2u{o0, o1}), (tid >> 3) * 4096 + (tid & 7) * 8, XC_OFF + (2 << 20) + s * (64 * 4096) + t * 64 + half * 2048, SC1);
        const float x0 = bflo(o0), x1 = bfhi(o0), x2 = bflo(o1), x3 = bfhi(o1);
        const float a0 = swap32_add(x0, x2), a1 = swap32_add(x0 * x0, x2 * x2), a2 = swap32_add(x1, x3), a3 = swap32_add(x1 * x1, x3 * x3);
        const float sv = dpp_add<0x128>(swap16_add(a0, a2)), qv = dpp_add<0x128>(swap16_add(a1, a3));
        if ((l & 8) == 0) *(LAS f32x2*)(sm + L_SP + ((half * 8 + w) * 32 + 4 * (l & 7) + (l >> 4)) * 8) = f32x2{sv, qv};
      }
    };
    auto pst_out = [&](int half) {
      float v = 0.f;
#pragma unroll
      for (int k = 0; k < 8; ++k) v += *(const LAS float*)(sm + L_SP + (half * 8 + k) * 256 + l * 4);
      ST64((v2u{__builtin_bit_cast(u32, v), (u32)opaque_v(1)}), (l >> 3) * 512 + (l & 7) * 8, PST_OFF + (t * 2 + half) * 4096 + s * 64, SC1);
    };
#define W2FRAG(k) (COMM ? wf[k] : (k) < W2A ? wa[(k) < W2A ? (k) : 0] : wf[(k) < W2A ? 0 : (k) - W2A])
    auto gemm2 = [&](int half) {
      f32x16 a0, a1;
#pragma unroll 1
      for (int sp = 0; sp < SPIN_CAP; ++sp) {
        a0 = f32x16{}; a1 = f32x16{};
        u32 mx = 0;
#pragma unroll
        for (int k = 0; k < 14; ++k) {
          a0 = mfma32(hf[k], W2FRAG(2 * k), a0); a1 = mfma32(hf[k], W2FRAG(2 * k + 1), a1);
          mx = umax(mx, max4(hf[k]));
        }
        if (__all(mx != 0xffffffffu)) break;
        __builtin_amdgcn_s_sleep(2);
        CBAR();
        h_load(half, 0, 14);
      }
      STAMP(19 + 4 * half + 30 * ly);
      if (half == 0) h_load(1, 0, 14);
      else if constexpr (!COMM) {
        if (ly == 0) LOAD_W(16, 32);
        else {
#pragma unroll
          for (int i = 16; i < 32; ++i) asm volatile("" : "=v"(wf[i]));
        }
      }
      STAMP(20 + 8 * half + 30 * ly);
#pragma unroll
      for (int q = 0; q < 4; ++q) {
        *(LAS f32x4*)(rbw + q * 32) = f32x4{a0[4 * q], a0[4 * q + 1], a0[4 * q + 2], a0[4 * q + 3]};
        *(LAS f32x4*)(rbw + 32 * RB_ROW + q * 32) = f32x4{a1[4 * q], a1[4 * q + 1], a1[4 * q + 2], a1[4 * q + 3]};
      }
    };
    gemm2(0);
    __syncthreads();
    STAMP(21 + 30 * ly);
    g2_out(0, xres0);
    STAMP(22 + 30 * ly);
    __syncthreads();
    if (COMM && ly == 0) pst_out(0);
    gemm2(1);
    __syncthreads();
    STAMP(24 + 30 * ly);
    g2_out(1, xres1);
    STAMP(25 + 30 * ly);
    if (ly == 0) {
      if constexpr (!COMM) LOAD_W(0, W1_SPLIT);
      __syncthreads();
      if constexpr (!COMM) LOAD_W(W1_SPLIT, 16);
      if constexpr (COMM) {
        pst_out(1);
        v2u g0, g1;
        const int po = PST_OFF + ((t * 2) * 8 + s) * 512;
#pragma unroll 1
        for (int sp = 0; sp < SPIN_CAP; ++sp) {
          CBAR();
          g0 = LD64(l * 8, po, SC1);
          g1 = LD64(l * 8, po + 4096, SC1);
          if (__all((g0[1] & g1[1]) == 1u)) break;
          __builtin_amdgcn_s_sleep(1);
        }
        const u32 e0 = g0[0], e1 = g1[0];
        float v = swap32_add(__builtin_bit_cast(float, e0), __builtin_bit_cast(float, e1));
        v = swap16_add(v, v);
        v = dpp_add<0x128>(v);
        const float o = __builtin_bit_cast(float, __builtin_amdgcn_update_dpp(0, __builtin_bit_cast(int, v), 0xb1, 0xf, 0xf, false));
        float m, vr, mb, rcs;
        ln_stats((l & 1) ? o : v, (l & 1) ? v : o, m, vr, mb, rcs);
        if ((l & 24) == 0) ST32(npz((l & 1) ? rcs : mb), h * 8192 + (l & 1) * 16 + ((l >> 1) & 3) * 4, FST_OFF + 16384 + (8 * t + s) * 32, SC1);
        if (l < 8) __hip_atomic_fetch_add((u32*)(ws + CNT_OFF + (l * 8 + (b & 7)) * 128), 1u, __ATOMIC_RELAXED, __HIP_MEMORY_SCOPE_AGENT);
        STAMP(26 + 30 * ly);
#pragma unroll 1
        for (int sp = 0; sp < SPIN_CAP; ++sp) {
          CBAR();
          const u32 f = LD32((l & 7) * 128, CNT_OFF + (b & 7) * 1024, SC1);
          if (__all(f == 32u)) break;
          __builtin_amdgcn_s_sleep(1);
        }
        CBAR();
      }
      STAMP(27 + 30 * ly);
      __syncthreads();
      const int cc = (tid >> 3) & 1, q8 = (tid >> 4) * 8 + (tid & 7);
      v4u m0, r0, m1, r1; v2u xa, xc;
#pragma unroll 1
      for (int sp = 0; sp < SPIN_CAP; ++sp) {
        CBAR();
        xa = LD64(cc * 4096 + q8 * 8, XC_OFF + (2 << 20) + b * 8192, SC1);
        xc = LD64(cc * 4096 + q8 * 8 + 2048, XC_OFF + (2 << 20) + b * 8192, SC1);
        m0 = LD128(q8 * 32, FST_OFF + 16384, SC1); r0 = LD128(q8 * 32 + 16, FST_OFF + 16384, SC1);
        m1 = LD128(q8 * 32, FST_OFF + 24576, SC1); r1 = LD128(q8 * 32 + 16, FST_OFF + 24576, SC1);
        if (__all(umax(umax(umax(xa[0], xa[1]), umax(xc[0], xc[1])), umax(umax(max4(m0), max4(r0)), umax(max4(m1), max4(r1)))) != 0xffffffffu)) break;
        __builtin_amdgcn_s_sleep(1);
      }
      const float dk = *(const LAS float*)(sm + L_DK + cc * 8);
      xr0 = conv_ln(sm + L_U + cc * U_CH + 512 + q8 * 8, xa, m0, r0, dk);
      xr1 = conv_ln(sm + L_U + cc * U_CH + 2560 + q8 * 8, xc, m1, r1, dk);
    }
  }
}

__global__ __launch_bounds__(512, 2) void k_main(const u16* __restrict__ dskip, const u16* __restrict__ b1,
    const u16* __restrict__ b2, char* __restrict__ ws, u16* __restrict__ out) {
  extern __shared__ __attribute__((aligned(16))) char smem[];
  LAS char* sm = (LAS char*)smem;
  const int b0 = blockIdx.x, w0 = __builtin_amdgcn_readfirstlane(threadIdx.x >> 6);
  const __amdgpu_buffer_rsrc_t rs = __builtin_amdgcn_make_buffer_rsrc(ws, 0, WS_BYTES, 0x00020000);
  STAMP(0);
  if (w0 == 7) main_body<true>(sm, rs, ws, out, dskip, b1, b2, b0, w0);
  else main_body<false>(sm, rs, ws, out, dskip, b1, b2, b0, w0);
  STAMP_FLUSH(1);
}

extern "C" void kernel_launch(void* const* d_in, const int* in_sizes, int n_in, void* d_out, int out_size, void* d_ws,
                              size_t ws_size, hipStream_t stream) {
  (void)hipFuncSetAttribute((const void*)k_main, hipFuncAttributeMaxDynamicSharedMemorySize, MAIN_LDS);
  k_prep<<<256, 512, 0, stream>>>((const u16*)d_in[0], (const u16*)d_in[1], (const u16*)d_in[2], (const u16*)d_in[3],
                                  (const u16*)d_in[4], (const u16*)d_in[5], (const u16*)d_in[8], (const u16*)d_in[10], (char*)d_ws);
  k_main<<<256, 512, MAIN_LDS, stream>>>((const u16*)d_in[6], (const u16*)d_in[9], (const u16*)d_in[11], (char*)d_ws, (u16*)d_out);
}
```

```cpp
#include <hip/hip_runtime.h>
#include <hip/hip_bf16.h>

typedef unsigned int u32;
typedef unsigned short u16;
typedef __attribute__((ext_vector_type(4))) u32 v4u;
typedef __attribute__((ext_vector_type(2))) u32 v2u;
typedef __attribute__((ext_vector_type(4))) float f32x4;
typedef __attribute__((ext_vector_type(2))) float f32x2;
typedef __attribute__((ext_vector_type(16))) float f32x16;
typedef __attribute__((ext_vector_type(8))) __bf16 bf16x8;
typedef __attribute__((ext_vector_type(2))) __bf16 bf16x2;
typedef __attribute__((ext_vector_type(8))) _Float16 f16x8;
typedef __attribute__((ext_vector_type(2))) _Float16 f16x2;
typedef __attribute__((ext_vector_type(4))) short s16x4;
#define LAS __attribute__((address_space(3)))
#define DI __device__ __forceinline__
#define STAMP(i) __builtin_amdgcn_sched_barrier(0)
#define STAMP_FLUSH(kid)

constexpr int SL = 2048, DM = 512, NS = 128, FF = 1792;
constexpr int XC_OFF = 0;
constexpr int XM_OFF = 4 << 20;
constexpr int PRM_OFF = 8 << 20;
constexpr int KSC_OFF = PRM_OFF + DM * 2560;
constexpr int CS_OFF = KSC_OFF + 2048;
constexpr int IDX_OFF = CS_OFF + 16384;
constexpr int W1F_OFF = 11 << 20;
constexpr int W2F_OFF = 13 << 20;
constexpr int FST_OFF = 15 << 20;
constexpr int CTL_OFF = FST_OFF + 32768;
constexpr int CNT_OFF = CTL_OFF;
constexpr int HF_OFF = CNT_OFF + 8192;
constexpr int PST_OFF = HF_OFF + 8192;
constexpr int CTL_BYTES = 8192 + 8192 + 262144;
constexpr int H_OFF = 16 << 20;
constexpr int H_LAYER = 32 * 8 * 2 * 14 * 1024;
constexpr int WS_BYTES = 32 << 20;
constexpr int SC1 = 16;
constexpr int SPIN_CAP = 1 << 16;
#define K0_AUX 0

DI u32 umin(u32 a, u32 b) { return a < b ? a : b; }
DI u32 umax(u32 a, u32 b) { return a > b ? a : b; }
DI float bf2f(u32 h) { return __builtin_bit_cast(float, h << 16); }
DI float bflo(u32 p) { return __builtin_bit_cast(float, p << 16); }
DI float bfhi(u32 p) { return __builtin_bit_cast(float, p & 0xffff0000u); }
DI u32 pack_bf16(float a, float b) { bf16x2 v = {(__bf16)a, (__bf16)b}; return __builtin_bit_cast(u32, v); }
DI float bfr(float a) { return bflo(pack_bf16(a, a)); }
DI u32 pack_f16(float a, float b) { f16x2 v = {(_Float16)a, (_Float16)b}; return __builtin_bit_cast(u32, v); }
DI float dot2bf(u32 a, u32 b, float c) {
  return __builtin_amdgcn_fdot2_f32_bf16(__builtin_bit_cast(bf16x2, a), __builtin_bit_cast(bf16x2, b), c, false);
}
DI f32x16 mfma32(v4u a, v4u b, f32x16 c) {
  return __builtin_amdgcn_mfma_f32_32x32x16_bf16(__builtin_bit_cast(bf16x8, a), __builtin_bit_cast(bf16x8, b), c, 0, 0, 0);
}
DI f32x4 mfma16h(v4u a, v4u b, f32x4 c) {
  return __builtin_amdgcn_mfma_f32_16x16x32_f16(__builtin_bit_cast(f16x8, a), __builtin_bit_cast(f16x8, b), c, 0, 0, 0);
}
DI f32x4 mfma16b(v4u a, v4u b, f32x4 c) {
  return __builtin_amdgcn_mfma_f32_16x16x32_bf16(__builtin_bit_cast(bf16x8, a), __builtin_bit_cast(bf16x8, b), c, 0, 0, 0);
}
DI v4u tr_frag(const LAS char* p) {
  const s16x4 a = __builtin_amdgcn_ds_read_tr16_b64_v4i16((LAS s16x4*)p);
  const s16x4 c = __builtin_amdgcn_ds_read_tr16_b64_v4i16((LAS s16x4*)(p + 256));
  return __builtin_bit_cast(v4u, __builtin_shufflevector(a, c, 0, 1, 2, 3, 4, 5, 6, 7));
}
template <int CTRL> DI float dpp_add(float v) {
  const int o = __builtin_amdgcn_update_dpp(0, __builtin_bit_cast(int, v), CTRL, 0xf, 0xf, false);
  return v + __builtin_bit_cast(float, o);
}
DI float row_sum16(float v) { v = dpp_add<0x128>(v); v = dpp_add<0x124>(v); v = dpp_add<0x122>(v); return dpp_add<0x121>(v); }
DI float swap32_add(float a, float c) {
  const auto r = __builtin_amdgcn_permlane32_swap(__builtin_bit_cast(u32, a), __builtin_bit_cast(u32, c), false, false);
  const u32 r0 = r[0], r1 = r[1];
  return __builtin_bit_cast(float, r0) + __builtin_bit_cast(float, r1);
}
DI float swap16_add(float a, float c) {
  const auto r = __builtin_amdgcn_permlane16_swap(__builtin_bit_cast(u32, a), __builtin_bit_cast(u32, c), false, false);
  const u32 r0 = r[0], r1 = r[1];
  return __builtin_bit_cast(float, r0) + __builtin_bit_cast(float, r1);
}
DI float wave_sum(float v) { v = row_sum16(v); v = swap16_add(v, v); return swap32_add(v, v); }
DI void ln_stats(float s, float q, float& m, float& v, float& mb, float& rs) {
  m = s * (1.0f / DM);
  v = fmaxf(q * (1.0f / DM) - m * m, 0.f);
  const u32 pk = pack_bf16(m, __builtin_amdgcn_sqrtf(bfr(bfr(v) + 1e-5f)));
  mb = bflo(pk); rs = __builtin_amdgcn_rcpf(bfhi(pk));
}
DI u32 npz(float a) { return umin(__builtin_bit_cast(u32, a), 0xfffffffeu); }
#define LD128(vo, so, aux) __builtin_amdgcn_raw_buffer_load_b128(rs, (vo), (so), (aux))
#define LD64(vo, so, aux) __builtin_amdgcn_raw_buffer_load_b64(rs, (vo), (so), (aux))
#define LD32(vo, so, aux) __builtin_amdgcn_raw_buffer_load_b32(rs, (vo), (so), (aux))
#define ST128(v, vo, so, aux) __builtin_amdgcn_raw_buffer_store_b128((v), rs, (vo) + (so), 0, (aux))
#define ST64(v, vo, so, aux) __builtin_amdgcn_raw_buffer_store_b64((v), rs, (vo), (so), (aux))
#define ST32(v, vo, so, aux) __builtin_amdgcn_raw_buffer_store_b32((v), rs, (vo), (so), (aux))
#define DRAIN() asm volatile("s_waitcnt vmcnt(0)" ::: "memory")
#define CBAR() asm volatile("" ::: "memory")
#define SBAR() __builtin_amdgcn_sched_barrier(0)
DI int opaque_v(int x) { asm volatile("" : "+v"(x)); return x; }
template <typename T, int N> DI void undef_regs(T (&a)[N]) {
#pragma unroll
  for (int i = 0; i < N; ++i) asm volatile("" : "=v"(a[i]));
}
DI int opaque_s(int x) { asm volatile("" : "+s"(x)); return x; }

constexpr int KT_REL = 24576 + 16;
constexpr int KT_CH = 1312;
DI void cexp_p(float arl, float rh, float rl, float p, float& re, float& im) {
  const float e = __builtin_amdgcn_exp2f(arl * p);
  const float r = __builtin_fmaf(rl, p, __builtin_amdgcn_fractf(rh * p));
  re = e * __builtin_amdgcn_cosf(r);
  im = e * __builtin_amdgcn_sinf(r);
}
DI float k_scale(float bs, int sign) {
  const int ex = (int)((__builtin_bit_cast(u32, bs) >> 23) & 0xff) - 126;
  int e = 15 - ex; e = e > 100 ? 100 : (e < -100 ? -100 : e);
  return __builtin_bit_cast(float, (u32)(127 + sign * e) << 23);
}
__global__ __launch_bounds__(512, 2) void k_prep(const u16* __restrict__ x0, const u16* __restrict__ logA,
    const u16* __restrict__ Aim, const u16* __restrict__ Cre, const u16* __restrict__ Cim,
    const u16* __restrict__ logdt, const u16* __restrict__ w1, const u16* __restrict__ w2, char* __restrict__ ws) {
  __shared__ float bnd[4];
  __shared__ float csp[8][32];
  __shared__ __attribute__((aligned(16))) char wtile[8 * 2048];
  __shared__ u32 trt[64 * 33];
  const int b = blockIdx.x, tid = threadIdx.x, l = tid & 63, w = __builtin_amdgcn_readfirstlane(tid >> 6);
  const __amdgpu_buffer_rsrc_t rs = __builtin_amdgcn_make_buffer_rsrc(ws, 0, WS_BYTES, 0x00020000);
  STAMP(0);
  u32 rla = 0, rai = 0, rcr = 0, rci = 0, rdt = 0;
  if (w < 4) {
    const int o = 2 * b * NS + tid;
    rla = logA[o]; rai = Aim[o]; rcr = Cre[o]; rci = Cim[o]; rdt = logdt[2 * b + (w >> 1)];
  }
  const v4u xr = *(const v4u*)(x0 + (8 * b + w) * DM + 8 * l);
  const v4u xt = *(const v4u*)(x0 + (64 * (b >> 3) + (tid >> 3)) * DM + 64 * (b & 7) + 8 * (tid & 7));
  v4u wr0 = {0u, 0u, 0u, 0u}, wr1 = wr0;
  const int fr = (b < 112 ? b : b - 112) * 16 + 2 * w;
  if (b < 112) {
    const u16* p = w1 + (16 * (fr & 31) + (l >> 2)) * FF + 32 * (fr >> 5) + 8 * (l & 3);
    wr0 = *(const v4u*)p; wr1 = *(const v4u*)(p + 16 * FF);
  } else if (b < 224) {
    const int r1 = fr >> 1, ks2 = r1 % 14, r2 = r1 / 14;
    const u16* p = w2 + (224 * (r2 & 7) + 16 * ks2 + (l >> 2)) * DM + 64 * (r2 >> 3) + 8 * (l & 3);
    wr0 = *(const v4u*)p; wr1 = *(const v4u*)(p + 32);
  }
  ST128((v4u{~0u, ~0u, ~0u, ~0u}), tid * 16, XM_OFF + b * 8192, K0_AUX);
  if (tid >= 128 && tid < 132) ST128((v4u{~0u, ~0u, ~0u, ~0u}), (tid - 128) * 16, FST_OFF + 16384 + b * 64, K0_AUX);
  if (tid < CTL_BYTES / 16 / 256) ST128((v4u{0u, 0u, 0u, 0u}), tid * 16, CTL_OFF + b * (CTL_BYTES / 256), K0_AUX);
  if (b == 255) {
    const int c = tid >> 8, x = tid & 255, col = x >> 4, row = x & 15, i16 = l & 15;
    const int p0 = 128 * (row >> 3) + (row & 7) + 8 * col;
    const int p1 = (row < 8 ? 256 + 128 * (row >> 2) + 2 * (row & 3) : 512 + 128 * ((row - 8) >> 1) + 4 * (row & 1)) + 8 * col;
    const int c1 = row < 8 ? 128 + (p1 >> 1) : 256 + (p1 >> 2);
    const int c2 = row < 8 ? 512 + 16 * row + col : (x == 8 ? 640 : 641);
    const int kb = KT_REL + c * KT_CH;
    u32 gx[8];
#pragma unroll
    for (int j = 0; j < 8; ++j) {
      const int la = 8 * x + 7 - 2 * j, lb = la - 1;
      const u32 hb = pack_bf16((float)la, (float)lb);
      const int ia = la < 128 ? la : (int)(hb & 0xffffu) - 17024, ib = lb < 128 ? lb : (int)(hb >> 16) - 17024;
      gx[j] = (u32)(kb + 2 * ia) | ((u32)(kb + 2 * ib) << 16);
    }
    const f32x4 pp = {(float)(8 * i16), (float)(128 * (i16 >> 3) + (i16 & 7)),
                      (float)(i16 < 8 ? 256 + 128 * (i16 >> 2) + 2 * (i16 & 3) : 512 + 128 * ((i16 - 8) >> 1) + 4 * (i16 & 1)),
                      (float)(i16 < 8 ? 1024 + 128 * i16 : 2048)};
    ST128(__builtin_bit_cast(v4u, pp), tid * 16, IDX_OFF, K0_AUX);
    ST128((v4u{(u32)(kb + 2 * p0), (u32)(kb + 2 * c1), (u32)(kb + 2 * c2), 0u}), tid * 16, IDX_OFF + 8192, K0_AUX);
    ST128((v4u{gx[0], gx[1], gx[2], gx[3]}), tid * 16, IDX_OFF + 16384, K0_AUX);
    ST128((v4u{gx[4], gx[5], gx[6], gx[7]}), tid * 16, IDX_OFF + 24576, K0_AUX);
  }
  STAMP(1);
  float arl = 0.f, rh = 0.f, rl = 0.f, cr = 0.f, ci = 0.f;
  if (w < 4) {
    const float dt = bfr(expf(bf2f(rdt)));
    arl = -bfr(expf(bf2f(rla))) * dt * 1.4426950408889634f;
    double t = (double)(bf2f(rai) * dt) * 0.15915494309189533577;
    t -= __builtin_rint(t);
    rh = __builtin_bit_cast(float, __builtin_bit_cast(u32, (float)t) & 0xfffff000u);
    rl = (float)(t - (double)rh);
    cr = bf2f(rcr) * dt * 2.0f; ci = bf2f(rci) * dt * 2.0f;
    const float bs = wave_sum(fabsf(cr) + fabsf(ci));
    if (l == 0) bnd[w] = bs;
  }
  STAMP(2);
  {
    float s = 0.f, q = 0.f;
#pragma unroll
    for (int j = 0; j < 4; ++j) {
      const u32 p = xr[j];
      s = dot2bf(p, 0x3f803f80u, s); q = dot2bf(p, p, q);
      trt[(tid >> 3) * 33 + 4 * (tid & 7) + j] = xt[j];
    }
    s = wave_sum(s); q = wave_sum(q);
    float m, v, mb, rcs;
    ln_stats(s, q, m, v, mb, rcs);
    if (l < 2) ST32(__builtin_bit_cast(u32, l ? rcs : mb), l * 16, FST_OFF + (2 * b + (w >> 2)) * 32 + (w & 3) * 4, K0_AUX);
  }
  if (b < 224) {
    LAS char* wl = (LAS char*)wtile + w * 2048;
    *(LAS v4u*)(wl + l * 16) = wr0; *(LAS v4u*)(wl + 1024 + l * 16) = wr1;
    const int hs = b < 112 ? 512 : 256, ts = b < 112 ? 256 : 512;
    const LAS char* rp = wl + (l >> 5) * hs + ((l & 15) >> 2) * 64 + 32 * ((l >> 4) & 1) + 8 * (l & 3);
    const s16x4 t0 = __builtin_amdgcn_ds_read_tr16_b64_v4i16((LAS s16x4*)rp), t1 = __builtin_amdgcn_ds_read_tr16_b64_v4i16((LAS s16x4*)(rp + ts));
    const s16x4 t2 = __builtin_amdgcn_ds_read_tr16_b64_v4i16((LAS s16x4*)(rp + 1024)), t3 = __builtin_amdgcn_ds_read_tr16_b64_v4i16((LAS s16x4*)(rp + 1024 + ts));
    const v4u o1 = __builtin_bit_cast(v4u, __builtin_shufflevector(t0, t1, 0, 1, 2, 3, 4, 5, 6, 7));
    const v4u o2 = __builtin_bit_cast(v4u, __builtin_shufflevector(t2, t3, 0, 1, 2, 3, 4, 5, 6, 7));
    float cs = 0.f;
#pragma unroll
    for (int j = 0; j < 4; ++j) { const u32 p1 = o1[j], p2 = o2[j]; cs = dot2bf(p1, 0x3f803f80u, cs); cs = dot2bf(p2, 0x3f803f80u, cs); }
    ST128(o1, l * 16, (b < 112 ? W1F_OFF : W2F_OFF) + fr * 1024, K0_AUX);
    ST128(o2, l * 16 + 1024, (b < 112 ? W1F_OFF : W2F_OFF) + fr * 1024, K0_AUX);
    cs = swap32_add(cs, cs);
    if (l < 32) csp[w][l] = cs;
  }
  STAMP(5);
  __syncthreads();
  STAMP(6);
  {
    if (w < 4) {
      const float bs = bnd[w & 2] + bnd[(w & 2) + 1], sc = k_scale(bs, 1);
      const int vo = (tid >> 2) * 80 + (tid & 3) * 4;
      ST32(__builtin_bit_cast(u32, arl), vo, PRM_OFF + b * 5120, K0_AUX);
      ST32(__builtin_bit_cast(u32, rh), vo + 16, PRM_OFF + b * 5120, K0_AUX);
      ST32(__builtin_bit_cast(u32, rl), vo + 32, PRM_OFF + b * 5120, K0_AUX);
      ST32(__builtin_bit_cast(u32, cr * sc), vo + 48, PRM_OFF + b * 5120, K0_AUX);
      ST32(__builtin_bit_cast(u32, ci * sc), vo + 64, PRM_OFF + b * 5120, K0_AUX);
      if ((tid & 127) == 0) ST32(__builtin_bit_cast(u32, k_scale(bs, -1)), (tid >> 7) * 4, KSC_OFF + 8 * b, K0_AUX);
    }
    if (b < 112 && tid < 32) {
      const float cs = ((csp[0][tid] + csp[1][tid]) + (csp[2][tid] + csp[3][tid])) + ((csp[4][tid] + csp[5][tid]) + (csp[6][tid] + csp[7][tid]));
      ST32(__builtin_bit_cast(u32, cs), tid * 4, CS_OFF + (b & 1) * (FF * 4) + (b >> 1) * 128, K0_AUX);
    }
    const int cq = tid >> 3, oct = tid & 7;
    const u16* t16 = (const u16*)trt;
    v4u o;
#pragma unroll
    for (int j = 0; j < 4; ++j)
      o[j] = (u32)t16[(8 * oct + 2 * j) * 66 + cq] | ((u32)t16[(8 * oct + 2 * j + 1) * 66 + cq] << 16);
    ST128(o, cq * 4096 + oct * 16, XC_OFF + (b & 7) * (64 * 4096) + (b >> 3) * 128, K0_AUX);
  }
  STAMP(8);
  STAMP_FLUSH(0);
}

constexpr int KC_CH = 8 * 260 * 16;
constexpr int L_U = 2 * KC_CH;
constexpr int U_CH = 4672;
constexpr int L_R = L_U + 2 * U_CH;
constexpr int L_XB = L_R + 32768;
constexpr int L_PB = L_XB + 256;
constexpr int L_KT = L_R + KT_REL;
constexpr int RB_ROW = 144;
constexpr int L_S = L_R + 8 * 64 * RB_ROW;
constexpr int L_B1 = L_S;
constexpr int L_CS = L_B1 + 896;
constexpr int L_SP = L_CS + 896;
constexpr int L_AC = L_SP + 4096;
constexpr int L_B2 = L_AC + 64;
constexpr int L_DK = L_B2 + 256;
constexpr int L_AB = L_DK + 16;
constexpr int L_END = L_AB + 512;
constexpr int MAIN_LDS = L_END;
#define MAPV 1
#define MAP_T(b) ((((b) >> 1) & 3) * 8 + ((b) >> 5))
#define MAP_S(b) (((b) & 1) * 4 + (((b) >> 3) & 3))
#define W1_SPLIT 0
constexpr int W2A = 12;

DI f32x2 gelu2(f32x2 x) {
  const f32x2 t = x * (x * x * -0.10294324f + -2.3022082f);
  f32x2 e; e.x = __builtin_amdgcn_exp2f(t.x); e.y = __builtin_amdgcn_exp2f(t.y);
  e = e + 1.0f;
  f32x2 r; r.x = __builtin_amdgcn_rcpf(e.x); r.y = __builtin_amdgcn_rcpf(e.y);
  return x * r;
}
DI u32 max4(v4u a) { return umax(umax(a[0], a[1]), umax(a[2], a[3])); }
DI f32x4 unpack4(v2u p) { const u32 a = p[0], c = p[1]; return f32x4{bflo(a), bfhi(a), bflo(c), bfhi(c)}; }
DI f32x4 conv_ln(LAS char* dst, v2u xp, v4u mm, v4u rr, float dk) {
  const f32x4 x = unpack4(xp);
  const f32x4 u = (x - __builtin_bit_cast(f32x4, mm)) * __builtin_bit_cast(f32x4, rr);
  *(LAS v2u*)dst = v2u{pack_f16(u[0], u[1]), pack_f16(u[2], u[3])};
  return x + u * dk;
}

template <bool COMM>
DI void main_body(LAS char* sm, const __amdgpu_buffer_rsrc_t rs, char* ws, u16* out, const u16* dskip, const u16* b1,
                  const u16* b2, int b0, int w0) {
  v4u wf[32], wa[W2A];
  f32x4 xr0, xr1;
#define LOAD_W(lo, hi) do { _Pragma("unroll") for (int ks = (lo); ks < (hi); ++ks) { \
      if constexpr (!COMM) wf[ks] = LD128(l16 + (ks & 3) * 1024, W1F_OFF + (7 * s + w) * 32768 + (ks >> 2) * 4096, 0); \
      else if (ks < 28) wf[ks] = LD128(l16 + (ks & 3) * 1024, W2F_OFF + (s * 8 + 7) * 28672 + (ks >> 2) * 4096, 0); } \
      SBAR(); } while (0)
#define CW(k) do { if (!COMM || ly == 0) LOAD_W(k, (k) + 1); } while (0)
  {
    const int b = opaque_s(b0), w = opaque_s(w0), tid = opaque_v(threadIdx.x), l = tid & 63, l16 = l * 16, s = MAP_S(b);
    const int cc = (tid >> 3) & 1, q8 = (tid >> 4) * 8 + (tid & 7);
    const int kk = l >> 4, i16 = l & 15;
    {
      const v4u pr = LD128(tid * 16, PRM_OFF + b * 5120, 0);
      if (tid < 16) *(LAS v4u*)(sm + (tid >> 3) * KC_CH + (tid & 7) * 4160) = v4u{0u, 0u, 0u, 0u};
      if (tid >= 64 && tid < 128) *(LAS v4u*)(sm + L_U + ((tid >> 5) & 1) * U_CH + (tid & 31) * 16) = v4u{0u, 0u, 0u, 0u};
      if (tid >= 128 && tid < 130) *(LAS v4u*)(sm + L_KT - 16 + (tid - 128) * KT_CH) = v4u{0u, 0u, 0u, 0u};
      *(LAS v4u*)(sm + L_XB + tid * 16) = pr;
    }
    const f32x4 pp4 = __builtin_bit_cast(f32x4, LD128(l16, IDX_OFF, 0));
    {
      SBAR();
      const u32 on = (u32)opaque_v(-1);
      const int t = MAP_T(b);
      ST64((v2u{on, on}), (tid >> 4) * 32768 + (tid & 15) * 8, XM_OFF + (2 << 20) + 128 * b, SC1);
      ST64((v2u{on, on}), (tid >> 4) * 32768 + (tid & 15) * 8, XM_OFF + (3 << 20) + 128 * b, SC1);
      ST64((v2u{on, on}), (tid >> 3) * 4096 + (tid & 7) * 8, XC_OFF + (2 << 20) + s * (64 * 4096) + t * 64, SC1);
      ST64((v2u{on, on}), (tid >> 3) * 4096 + (tid & 7) * 8, XC_OFF + (2 << 20) + s * (64 * 4096) + t * 64 + 2048, SC1);
      SBAR();
    }
    v4u tix, gx0, gx1, m0, r0, m1, r1;
    v2u xa, xb;
    float dk, ksc, bias2, bb1, cs0, cs1;
    __syncthreads();
    const int c = tid >> 8, x = tid & 255;
    const float pB = pp4[0], pA0 = pp4[1], pA1 = pp4[2], pA2 = pp4[3];
    f32x4 acc0 = {0.f, 0.f, 0.f, 0.f}, acc1 = acc0, acc2 = acc0;
    STAMP(1);
#pragma unroll
    for (int g = 0; g < 2; ++g) {
      v4u fa0, fa1, fa2, fbh, fbl;
#pragma unroll
      for (int j = 0; j < 4; ++j) {
        const LAS float* pq = (const LAS float*)(sm + L_XB + w * 640 + g * 320 + kk * 80) + j;
        const float arl = pq[0], rh = pq[4], rl = pq[8], cr = pq[12], ci = pq[16];
        float br, bi, er, ei;
        cexp_p(arl, rh, rl, pB, br, bi);
        bi = -bi;
        const u32 hi = pack_f16(br, bi);
        const f16x2 hv = __builtin_bit_cast(f16x2, hi);
        fbh[j] = hi;
        fbl[j] = pack_f16(br - (float)hv.x, bi - (float)hv.y);
        cexp_p(arl, rh, rl, pA0, er, ei);
        fa0[j] = pack_f16(cr * er - ci * ei, cr * ei + ci * er);
        cexp_p(arl, rh, rl, pA1, er, ei);
        fa1[j] = pack_f16(cr * er - ci * ei, cr * ei + ci * er);
        cexp_p(arl, rh, rl, pA2, er, ei);
        fa2[j] = pack_f16(cr * er - ci * ei, cr * ei + ci * er);
        SBAR();
        LOAD_W(8 * g + 2 * j, 8 * g + 2 * j + 2);
        if (g == 0 && j == 0) { xa = LD64(cc * 4096 + q8 * 8, XC_OFF + b * 8192, 0); xb = LD64(cc * 4096 + q8 * 8 + 2048, XC_OFF + b * 8192, 0); }
        if (g == 0 && j == 1) { m0 = LD128(q8 * 32, FST_OFF, 0); r0 = LD128(q8 * 32 + 16, FST_OFF, 0); }
        if (g == 0 && j == 2) { m1 = LD128(q8 * 32, FST_OFF + 8192, 0); r1 = LD128(q8 * 32 + 16, FST_OFF + 8192, 0); }
        if (g == 0 && j == 3) { dk = bf2f(dskip[2 * b + cc]); ksc = __builtin_bit_cast(float, LD32(cc * 4, KSC_OFF + 8 * b, 0)); }
        if (g == 1 && j == 0) { bias2 = bf2f(b2[64 * s + l]); bb1 = bf2f(b1[224 * s + (tid < 224 ? tid : 0)]); }
        if (g == 1 && j == 1) { cs0 = __builtin_bit_cast(float, LD32(tid * 4, CS_OFF + s * 896, 0)); cs1 = __builtin_bit_cast(float, LD32(tid * 4, CS_OFF + FF * 4 + s * 896, 0)); }
        if (g == 1 && j == 2) { tix = LD128(tid * 16, IDX_OFF + 8192, 0); }
        if (g == 1 && j == 3) { gx0 = LD128(tid * 16, IDX_OFF + 16384, 0); gx1 = LD128(tid * 16, IDX_OFF + 24576, 0); }
        SBAR();
      }
      acc0 = mfma16h(fa0, fbh, acc0); acc1 = mfma16h(fa1, fbh, acc1); acc2 = mfma16h(fa2, fbh, acc2);
      acc0 = mfma16h(fa0, fbl, acc0); acc1 = mfma16h(fa1, fbl, acc1); acc2 = mfma16h(fa2, fbl, acc2);
    }
    *(LAS f32x4*)(sm + L_R + w * 3072 + i16 * 64 + kk * 16) = acc0;
    *(LAS f32x4*)(sm + L_R + w * 3072 + 1024 + i16 * 64 + kk * 16) = acc1;
    *(LAS f32x4*)(sm + L_R + w * 3072 + 2048 + i16 * 64 + kk * 16) = acc2;
    STAMP(60);
    xr0 = conv_ln(sm + L_U + cc * U_CH + 512 + q8 * 8, xa, m0, r0, dk);
    xr1 = conv_ln(sm + L_U + cc * U_CH + 2560 + q8 * 8, xb, m1, r1, dk);
    if (tid < 224) { *(LAS float*)(sm + L_B1 + tid * 4) = bb1; *(LAS float*)(sm + L_CS + tid * 4) = cs0 + cs1; }
    if (tid < 64) *(LAS float*)(sm + L_B2 + tid * 4) = bias2;
    if ((tid & ~8) == 0) *(LAS f32x2*)(sm + L_DK + cc * 8) = f32x2{dk, ksc};
    STAMP(61);
    __syncthreads();
    {
      const LAS float* pp = (const LAS float*)(sm + L_R + c * 12288) + x;
      *(LAS _Float16*)(sm + L_R + tix[0]) = (_Float16)((pp[0] + pp[768]) + (pp[1536] + pp[2304]));
      *(LAS _Float16*)(sm + L_R + tix[1]) = (_Float16)((pp[256] + pp[1024]) + (pp[1792] + pp[2560]));
      *(LAS _Float16*)(sm + L_R + tix[2]) = (_Float16)((pp[512] + pp[1280]) + (pp[2048] + pp[2816]));
    }
    __syncthreads();
    STAMP(62);
    {
      u32 wd[8];
#pragma unroll
      for (int j = 0; j < 8; ++j) {
        const u32 g = j < 4 ? gx0[j & 3] : gx1[j & 3];
        wd[j] = (u32)*(const LAS u16*)(sm + L_R + (g & 0xffffu)) | ((u32)*(const LAS u16*)(sm + L_R + (g >> 16)) << 16);
      }
#pragma unroll
      for (int cp = 0; cp < 8; ++cp) {
        const int sg = 7 - cp;
        v4u o;
#pragma unroll
        for (int k = 0; k < 4; ++k)
          o[k] = (sg & 1) ? __builtin_amdgcn_alignbit(wd[(sg + 1) / 2 + k], wd[(sg - 1) / 2 + k], 16) : wd[sg / 2 + k];
        *(LAS v4u*)(sm + c * KC_CH + cp * 4160 + (x + 1) * 16) = o;
      }
    }
    if constexpr (COMM) {
#pragma unroll
      for (int i = 16; i < 28; ++i) asm volatile("" : "=v"(wf[i]));
    }
  }

#pragma unroll 1
  for (int ly = 0; ly < 2; ++ly) {
    const int b = opaque_s(b0), w = opaque_s(w0);
    const int t = MAP_T(b), s = MAP_S(b);
    int tid = opaque_v(threadIdx.x), l = tid & 63, l16 = l * 16;
    const int xmo = XM_OFF + ly * (2 << 20) + t * 32768 + w * 1024;
    const int w2off = W2F_OFF + ((s * 8 + w) * 28) * 1024;
    const int hoff = H_OFF + ly * H_LAYER + (t * 8 + s) * (28 * 1024) + (2 * w) * 1024;
    v4u xs[4];
    auto x_load = [&](int half) {
#pragma unroll
      for (int k = 0; k < 4; ++k) xs[k] = LD128(l16, xmo + half * (1 << 20) + k * 8192, SC1);
    };
    auto x_ok = [&]() { return __all(umax(umax(max4(xs[0]), max4(xs[1])), umax(max4(xs[2]), max4(xs[3]))) != 0xffffffffu); };
    auto x_stage = [&](int half) {
      if (!x_ok()) {
        int sp = 0;
#pragma unroll 1
        do {
          STAMP(56 + half + 2 * ly);
          __builtin_amdgcn_s_sleep(4);
          CBAR();
          x_load(half);
        } while (!x_ok() && ++sp < SPIN_CAP);
      }
#pragma unroll
      for (int k = 0; k < 4; ++k) *(LAS v4u*)(sm + L_R + half * 32768 + k * 8192 + tid * 16) = xs[k];
    };
    v2u pg;
    bool fin0 = true;
    auto fin_load = [&](int hf) { pg = LD64(l * 8, PST_OFF + ((t * 2 + hf) * 8 + s) * 512, SC1); };
    auto fin_try = [&](int hf) -> bool {
      if (!__all(pg[1] == 1u)) return false;
      const u32 e0 = pg[0];
      float v = __builtin_bit_cast(float, e0);
      v = swap32_add(v, v); v = swap16_add(v, v); v = dpp_add<0x128>(v);
      const float o = __builtin_bit_cast(float, __builtin_amdgcn_update_dpp(0, __builtin_bit_cast(int, v), 0xb1, 0xf, 0xf, false));
      float mm, vr, mb, rcs;
      ln_stats((l & 1) ? o : v, (l & 1) ? v : o, mm, vr, mb, rcs);
      if (l < 8) ST32(npz((l & 1) ? rcs : mb), (l & 1) * 16 + (l >> 1) * 4, FST_OFF + 16384 + hf * 8192 + (8 * t + s) * 32, SC1);
      return true;
    };
    auto fin_wait = [&](int hf) {
#pragma unroll 1
      for (int sp = 0; sp < SPIN_CAP; ++sp) {
        CBAR();
        fin_load(hf);
        if (fin_try(hf)) break;
        __builtin_amdgcn_s_sleep(1);
      }
    };
    v2u ix; v4u im, ir;
    auto cin_load = [&](int hf) {
      const int cc = (tid >> 3) & 1, q8 = (tid >> 4) * 8 + (tid & 7);
      ix = LD64(cc * 4096 + q8 * 8, XC_OFF + (2 << 20) + b * 8192 + hf * 2048, SC1);
      im = LD128(q8 * 32, FST_OFF + 16384 + hf * 8192, SC1); ir = LD128(q8 * 32 + 16, FST_OFF + 16384 + hf * 8192, SC1);
    };
    auto cin_ok = [&]() { return __all(umax(umax(ix[0], ix[1]), umax(max4(im), max4(ir))) != 0xffffffffu); };
    auto cin_ln = [&](int hf) -> f32x4 {
      if (!cin_ok()) {
        int sp = 0;
#pragma unroll 1
        do {
          STAMP(29 + 2 * hf);
          __builtin_amdgcn_s_sleep(4);
          CBAR();
          cin_load(hf);
        } while (!cin_ok() && ++sp < SPIN_CAP);
      }
      const int cc = (tid >> 3) & 1, q8 = (tid >> 4) * 8 + (tid & 7);
      return conv_ln(sm + L_U + cc * U_CH + 512 + hf * 2048 + q8 * 8, ix, im, ir, *(const LAS float*)(sm + L_DK + cc * 8));
    };
    CW(16); CW(17); CW(18); CW(19);
    STAMP(2 + 30 * ly);
    __syncthreads();
    STAMP(3 + 30 * ly);
    {
      const int c16 = l & 15, g4 = l >> 4;
      const LAS char* pa = sm + (w >> 2) * KC_CH + (c16 & 7) * 4160 + (3 + (c16 >> 3) - g4) * 16 + (w & 3) * 128;
      const LAS char* pb = sm + L_U + (w >> 2) * U_CH + (30 + 2 * c16 + g4) * 16 - (w & 3) * 128;
      LAS char* pp = sm + L_R + w * 4096 + (w >> 2) * 128 + ((4 * c16 + g4) ^ ((c16 >> 1) & 3)) * 16;
      const int xmv = (tid >> 4) * 32768 + (tid & 15) * 8;
      const LAS char* pr = sm + L_R + ((tid >> 3) & 1) * 16512 + ((((tid >> 4) << 3) | (tid & 7)) ^ ((tid >> 4) & 3)) * 16;
      auto conv_out = [&](int half, f32x4 xr) {
        const LAS char* p = pr + (half ? L_PB - L_R : 0);
        const float ksc = *(const LAS float*)(sm + L_DK + ((tid >> 3) & 1) * 8 + 4);
        const f32x4 y = (*(const LAS f32x4*)p + *(const LAS f32x4*)(p + 4096)) + (*(const LAS f32x4*)(p + 8192) + *(const LAS f32x4*)(p + 12288));
        const f32x4 o = y * ksc + xr;
        ST64((v2u{umin(pack_bf16(o[0], o[1]), 0xfffffffeu), umin(pack_bf16(o[2], o[3]), 0xfffffffeu)}), xmv,
             XM_OFF + ly * (2 << 20) + half * (1 << 20) + 128 * b, SC1);
      };
      {
        f32x4 acc[4];
#pragma unroll
        for (int i = 0; i < 4; ++i) acc[i] = f32x4{0.f, 0.f, 0.f, 0.f};
        v4u fa[2][4], fb[2][4];
#pragma unroll
        for (int tau = 0; tau < 2; ++tau)
#pragma unroll
          for (int i = 0; i < 4; ++i) { fa[tau][i] = *(const LAS v4u*)(pa + i * 512 + tau * 64); fb[tau][i] = *(const LAS v4u*)(pb + i * 512 - tau * 64); }
#pragma unroll
        for (int tau = 0; tau < 2; ++tau)
#pragma unroll
          for (int sx = 0; sx < 4; ++sx) {
#pragma unroll
            for (int e = 0; e + sx < 4; ++e) acc[sx + e] = mfma16h(fa[tau][sx], fb[tau][e], acc[sx + e]);
            if (sx == 0) { SBAR(); CW(20 + tau); }
          }
#pragma unroll
        for (int i = 0; i < 4; ++i) *(LAS f32x4*)(pp + i * 1024) = acc[i];
      }
      v4u fa[8], fb[4];
#pragma unroll
      for (int i = 0; i < 4; ++i) { fb[i] = *(const LAS v4u*)(pb + i * 512); fa[7 - i] = *(const LAS v4u*)(pa + (7 - i) * 512); }
#pragma unroll
      for (int i = 1; i < 4; ++i) fa[i] = *(const LAS v4u*)(pa + i * 512);
      SBAR();
      CW(22); CW(23);
      STAMP(4 + 30 * ly);
      __syncthreads();
      conv_out(0, xr0);
      STAMP(5 + 30 * ly);
      {
        f32x4 acc[4];
#pragma unroll
        for (int i = 0; i < 4; ++i) acc[i] = f32x4{0.f, 0.f, 0.f, 0.f};
#pragma unroll
        for (int e = 0; e < 4; ++e) {
#pragma unroll
          for (int i = 0; i < 4; ++i) acc[i] = mfma16h(fa[4 + i - e], fb[e], acc[i]);
          fb[e] = *(const LAS v4u*)(pb + e * 512 - 64); fa[7 - e] = *(const LAS v4u*)(pa + (7 - e) * 512 + 64);
          SBAR();
          if (e & 1) CW(24 + (e >> 1));
        }
#pragma unroll
        for (int i = 3; i >= 1; --i) fa[i] = *(const LAS v4u*)(pa + i * 512 + 64);
        if (ly) cin_load(1);
        else { asm volatile("" : "=v"(ix), "=v"(im), "=v"(ir)); }
        SBAR();
#pragma unroll
        for (int e = 0; e < 4; ++e) {
#pragma unroll
          for (int i = 0; i < 4; ++i) acc[i] = mfma16h(fa[4 + i - e], fb[e], acc[i]);
          SBAR();
          if (e & 1) CW(26 + (e >> 1));
        }
#pragma unroll
        for (int i = 0; i < 4; ++i) { fa[i] = *(const LAS v4u*)(pa + i * 512); fa[4 + i] = *(const LAS v4u*)(pa + i * 512 + 64); }
        STAMP(8 + 30 * ly);
        if (ly) xr1 = cin_ln(1);
        SBAR();
        CW(28); CW(29);
        STAMP(9 + 30 * ly);
        __syncthreads();
        v4u fc[8];
#pragma unroll
        for (int i = 0; i < 4; ++i) { fc[i] = *(const LAS v4u*)(pb + (4 + i) * 512); fc[4 + i] = *(const LAS v4u*)(pb + (4 + i) * 512 - 64); }
#pragma unroll
        for (int tau = 0; tau < 2; ++tau)
#pragma unroll
          for (int e = 0; e < 4; ++e) {
#pragma unroll
            for (int i = e; i < 4; ++i) acc[i] = mfma16h(fa[4 * tau + i - e], fc[4 * tau + e], acc[i]);
            if (e == 0) { SBAR(); CW(30 + tau); }
          }
#pragma unroll
        for (int i = 0; i < 4; ++i) *(LAS f32x4*)(pp + (L_PB - L_R) + i * 1024) = acc[i];
      }
      STAMP(6 + 30 * ly);
      __syncthreads();
      x_load(0);
      SBAR();
      conv_out(1, xr1);
      STAMP(7 + 30 * ly);
      x_stage(0);
      SBAR();
      if constexpr (!COMM) {
        const u32 on = (u32)opaque_v(-1);
#pragma unroll
        for (int k = 0; k < 4; ++k) ST128((v4u{on, on, on, on}), l16 + (k & 1) * 1024, hoff + (k >> 1) * 14336, SC1);
        SBAR();
      }
    }
    STAMP(11 + 30 * ly);
    __syncthreads();
    tid = opaque_v(threadIdx.x); l = tid & 63; l16 = l * 16;
    const int h = l >> 5;
    const LAS char* xb = sm + L_R + (8 * h + ((l & 15) >> 2)) * 64 + 32 * ((l >> 4) & 1) + 8 * (l & 3);
    auto stats = [&](int half) {
      const LAS char* xq = sm + L_R + half * 32768 + (8 * (l >> 4) + ((l & 15) >> 2)) * 64 + 8 * (l & 3);
      const u32 one = (u32)opaque_v(0x3f803f80);
      const v4u ones = {one, one, one, one};
      f32x4 s0 = {0.f, 0.f, 0.f, 0.f}, q0 = s0, s1 = s0, q1 = s0;
      v4u f0[4], f1[4];
#pragma unroll
      for (int k = 0; k < 4; ++k) { f0[k] = tr_frag(xq + k * 2048); f1[k] = tr_frag(xq + k * 2048 + 32); }
#pragma unroll
      for (int k = 0; k < 16; ++k) {
        s0 = mfma16b(ones, f0[k & 3], s0); q0 = mfma16b(f0[k & 3], f0[k & 3], q0);
        s1 = mfma16b(ones, f1[k & 3], s1); q1 = mfma16b(f1[k & 3], f1[k & 3], q1);
        if (k + 4 < 16) { f0[k & 3] = tr_frag(xq + (k + 4) * 2048); f1[k & 3] = tr_frag(xq + (k + 4) * 2048 + 32); }
        if (half == 0 && k == 9) x_load(1);
        SBAR();
      }
      const float d0 = (l & 2) ? ((l & 1) ? q0[3] : q0[2]) : ((l & 1) ? q0[1] : q0[0]);
      const float d1 = (l & 2) ? ((l & 1) ? q1[3] : q1[2]) : ((l & 1) ? q1[1] : q1[0]);
#pragma unroll
      for (int g = 0; g < 2; ++g) {
        float m, v, mb, r1;
        ln_stats(g ? s1[0] : s0[0], g ? d1 : d0, m, v, mb, r1);
        const u32 p2 = pack_bf16((m - mb) * r1, v * r1 * r1);
        const float r2 = __builtin_amdgcn_rcpf(bfr(__builtin_amdgcn_sqrtf(bfr(bfhi(p2) + 1e-5f))));
        if ((l >> 4) == ((l >> 2) & 3)) *(LAS f32x2*)(sm + L_AB + (half * 32 + 16 * g + (l & 15)) * 8) = f32x2{r1 * r2, (mb * r1 + bflo(p2)) * r2};
      }
    };
    auto epi_quad = [&](const f32x16& acc, int q, f32x2 ab, u32& o0, u32& o1) {
      const f32x4 c4 = *(const LAS f32x4*)(sm + L_CS + (32 * w + 8 * q + 4 * h) * 4);
      const f32x4 b4 = *(const LAS f32x4*)(sm + L_B1 + (32 * w + 8 * q + 4 * h) * 4);
      const f32x4 x = f32x4{acc[4 * q], acc[4 * q + 1], acc[4 * q + 2], acc[4 * q + 3]} * ab.x + (b4 - c4 * ab.y);
      const f32x2 g0 = gelu2(f32x2{x[0], x[1]}), g1 = gelu2(f32x2{x[2], x[3]});
      o0 = umin(pack_bf16(g0.x, g0.y), 0xfffffffeu); o1 = umin(pack_bf16(g1.x, g1.y), 0xfffffffeu);
    };
    f32x16 acc0, acc1;
    constexpr int XD = 3;
    if constexpr (!COMM) {
      v4u xf[XD];
#pragma unroll
      for (int k = 0; k < XD; ++k) xf[k] = tr_frag(xb + k * 1024);
      acc0 = f32x16{};
#pragma unroll
      for (int ks = 0; ks < 32; ++ks) {
        acc0 = mfma32(wf[ks], xf[ks % XD], acc0);
        if (ks + XD < 32) xf[ks % XD] = tr_frag(xb + (ks + XD) * 1024);
        if (ks < 2 * W2A && !(ks & 1)) { const int k = ks >> 1; wa[k] = LD128(l16 + (k & 3) * 1024, w2off + (k >> 2) * 4096, 0); }
        if (ks == 15) x_load(1);
        SBAR();
      }
    } else {
      __builtin_amdgcn_s_setprio(2);
      stats(0);
      __builtin_amdgcn_s_setprio(0);
    }
    STAMP(12 + 30 * ly);
    x_stage(1);
    STAMP(13 + 30 * ly);
    __syncthreads();
    STAMP(14 + 30 * ly);
    const int orow = ly ? l : 8 * w + (l >> 3), oq = ly ? w : (l & 7);
    const v2u xres0 = *(const LAS v2u*)(sm + L_R + (64 * s + orow) * 64 + 8 * oq);
    const v2u xres1 = *(const LAS v2u*)(sm + L_XB + (64 * s + orow) * 64 + 8 * oq);
    const int hfo = HF_OFF + ly * 4096 + t * 128;
    if constexpr (!COMM) {
      v4u xf[XD];
#pragma unroll
      for (int k = 0; k < XD; ++k) xf[k] = tr_frag(xb + 32768 + k * 1024);
      const f32x2 ab = *(const LAS f32x2*)(sm + L_AB + (l & 31) * 8);
      u32 o[4];
      acc1 = f32x16{};
#pragma unroll
      for (int ks = 0; ks < 32; ++ks) {
        acc1 = mfma32(wf[ks], xf[ks % XD], acc1);
        if (ks + XD < 32) xf[ks % XD] = tr_frag(xb + 32768 + (ks + XD) * 1024);
        if (ks & 1) { const int j = ks >> 1, k = W2A + j; wf[j] = LD128(l16 + (k & 3) * 1024, w2off + (k >> 2) * 4096, 0); }
        if (ks % 6 == 1 && ks < 24) { const int q = ks / 6; epi_quad(acc0, q, ab, o[2 * (q & 1)], o[2 * (q & 1) + 1]); }
        if (ks == 11 || ks == 23) ST128((v4u{o[0], o[1], o[2], o[3]}), l16 + (ks / 12) * 1024, hoff, SC1);
        if (ks % 6 == 5 || ks >= 24) SBAR();
      }
    } else {
      if (l == 0) ST32(1u, 0, hfo + s * 4, SC1);
      __builtin_amdgcn_s_setprio(2);
      stats(1);
      __builtin_amdgcn_s_setprio(0);
#pragma unroll 1
      for (int sp = 0; sp < SPIN_CAP; ++sp) {
        CBAR();
        const u32 f = LD32((l & 7) * 4, hfo, SC1);
        if (__all(f == 1u)) break;
        __builtin_amdgcn_s_sleep(1);
      }
      CBAR();
    }
    STAMP(15 + 30 * ly);
    __syncthreads();
    v4u hf[14];
    const int hin = H_OFF + ly * H_LAYER + (t * 8 + w) * (28 * 1024);
    auto h_load = [&](int half, int lo, int hi) {
#pragma unroll
      for (int k = lo; k < hi; ++k) { hf[k] = LD128(l16, hin + (14 * half + k) * 1024, SC1); SBAR(); }
    };
    h_load(0, 0, 7);
    STAMP(16 + 30 * ly);
    if constexpr (!COMM) {
      const f32x2 ab = *(const LAS f32x2*)(sm + L_AB + (32 + (l & 31)) * 8);
#pragma unroll
      for (int sp = 0; sp < 2; ++sp) {
        u32 o[4];
        epi_quad(acc1, 2 * sp, ab, o[0], o[1]);
        epi_quad(acc1, 2 * sp + 1, ab, o[2], o[3]);
        ST128((v4u{o[0], o[1], o[2], o[3]}), l16 + sp * 1024, hoff + 14336, SC1);
      }
      SBAR();
    }
    h_load(0, 7, 14);
    STAMP(17 + 30 * ly);
    LAS char* rbw = sm + L_R + (w * 64 + (l & 31)) * RB_ROW + h * 16;
    auto g2_out = [&](int half, v2u xres) {
      const LAS char* rp = sm + L_R + orow * RB_ROW + oq * 16;
      f32x4 o4 = unpack4(xres) + *(const LAS float*)(sm + L_B2 + orow * 4);
      f32x4 p4 = *(const LAS f32x4*)rp;
#pragma unroll
      for (int ww = 1; ww < 8; ++ww) { const f32x4 v = *(const LAS f32x4*)(rp + ww * (64 * RB_ROW)); if (ww & 1) o4 += v; else p4 += v; }
      o4 += p4;
      u32 o0 = pack_bf16(o4[0], o4[1]), o1 = pack_bf16(o4[2], o4[3]);
      if (ly) {
        u16* op = out + (1024 * half + 32 * t + 4 * w) * DM + 64 * s + l;
        op[0] = (u16)(o0 & 0xffffu); op[DM] = (u16)(o0 >> 16); op[2 * DM] = (u16)(o1 & 0xffffu); op[3 * DM] = (u16)(o1 >> 16);
      } else {
        o0 = umin(o0, 0xfffffffeu); o1 = umin(o1, 0xfffffffeu);
        ST64((v2u{o0, o1}), (tid >> 3) * 4096 + (tid & 7) * 8, XC_OFF + (2 << 20) + s * (64 * 4096) + t * 64 + half * 2048, SC1);
        const float x0 = bflo(o0), x1 = bfhi(o0), x2 = bflo(o1), x3 = bfhi(o1);
        const float a0 = swap32_add(x0, x2), a1 = swap32_add(x0 * x0, x2 * x2), a2 = swap32_add(x1, x3), a3 = swap32_add(x1 * x1, x3 * x3);
        const float sv = dpp_add<0x128>(swap16_add(a0, a2)), qv = dpp_add<0x128>(swap16_add(a1, a3));
        if ((l & 8) == 0) *(LAS f32x2*)(sm + L_SP + ((half * 8 + w) * 32 + 4 * (l & 7) + (l >> 4)) * 8) = f32x2{sv, qv};
      }
    };
    auto pst_out = [&](int half) {
      float v = 0.f;
#pragma unroll
      for (int k = 0; k < 8; ++k) v += *(const LAS float*)(sm + L_SP + (half * 8 + k) * 256 + l * 4);
      ST64((v2u{__builtin_bit_cast(u32, v), (u32)opaque_v(1)}), (l >> 3) * 512 + (l & 7) * 8, PST_OFF + (t * 2 + half) * 4096 + s * 64, SC1);
    };
#define W2FRAG(k) (COMM ? wf[k] : (k) < W2A ? wa[(k) < W2A ? (k) : 0] : wf[(k) < W2A ? 0 : (k) - W2A])
    auto gemm2 = [&](int half) {
      f32x16 a0, a1;
#pragma unroll 1
      for (int sp = 0; sp < SPIN_CAP; ++sp) {
        a0 = f32x16{}; a1 = f32x16{};
        u32 mx = 0;
#pragma unroll
        for (int k = 0; k < 14; ++k) {
          a0 = mfma32(hf[k], W2FRAG(2 * k), a0); a1 = mfma32(hf[k], W2FRAG(2 * k + 1), a1);
          mx = umax(mx, max4(hf[k]));
        }
        if (__all(mx != 0xffffffffu)) break;
        STAMP(10 + 8 * half + 30 * ly);
        __builtin_amdgcn_s_sleep(2);
        CBAR();
        h_load(half, 0, 14);
      }
      STAMP(19 + 4 * half + 30 * ly);
      if (!COMM && half == 0) h_load(1, 0, 14);
      if (half == 0) STAMP(20 + 30 * ly);
#pragma unroll
      for (int q = 0; q < 4; ++q) {
        *(LAS f32x4*)(rbw + q * 32) = f32x4{a0[4 * q], a0[4 * q + 1], a0[4 * q + 2], a0[4 * q + 3]};
        *(LAS f32x4*)(rbw + 32 * RB_ROW + q * 32) = f32x4{a1[4 * q], a1[4 * q + 1], a1[4 * q + 2], a1[4 * q + 3]};
      }
      if constexpr (!COMM) {
        if (half == 1) {
          SBAR();
          if (ly == 0) {
            if (w == 0) {
#pragma unroll 1
              for (int sp = 0; sp < 3; ++sp) { CBAR(); fin_load(0); fin0 = fin_try(0); if (fin0) break; }
            }
            LOAD_W(0, 16);
          } else {
#pragma unroll
            for (int i = 0; i < 16; ++i) asm volatile("" : "=v"(wf[i]));
          }
        }
      }
    };
    gemm2(0);
    __syncthreads();
    if constexpr (COMM) h_load(1, 0, 14);
    STAMP(21 + 30 * ly);
    g2_out(0, xres0);
    STAMP(22 + 30 * ly);
    __syncthreads();
    if (COMM && ly == 0) pst_out(0);
    gemm2(1);
    __syncthreads();
    STAMP(24 + 30 * ly);
    g2_out(1, xres1);
    STAMP(25 + 30 * ly);
    if (ly == 0) {
      cin_load(0);
      __syncthreads();
      if constexpr (COMM) { pst_out(1); fin_wait(1); }
      else if (!fin0) { STAMP(28); fin_wait(0); }
      STAMP(26 + 30 * ly);
      xr0 = cin_ln(0);
      STAMP(27 + 30 * ly);
    }
  }
}

__global__ __launch_bounds__(512, 2) void k_main(const u16* __restrict__ dskip, const u16* __restrict__ b1,
    const u16* __restrict__ b2, char* __restrict__ ws, u16* __restrict__ out) {
  extern __shared__ __attribute__((aligned(16))) char smem[];
  LAS char* sm = (LAS char*)smem;
  const int b0 = blockIdx.x, w0 = __builtin_amdgcn_readfirstlane(threadIdx.x >> 6);
  const __amdgpu_buffer_rsrc_t rs = __builtin_amdgcn_make_buffer_rsrc(ws, 0, WS_BYTES, 0x00020000);
  STAMP(0);
  if (w0 == 7) main_body<true>(sm, rs, ws, out, dskip, b1, b2, b0, w0);
  else main_body<false>(sm, rs, ws, out, dskip, b1, b2, b0, w0);
  STAMP_FLUSH(1);
}

extern "C" void kernel_launch(void* const* d_in, const int* in_sizes, int n_in, void* d_out, int out_size, void* d_ws,
                              size_t ws_size, hipStream_t stream) {
  (void)hipFuncSetAttribute((const void*)k_main, hipFuncAttributeMaxDynamicSharedMemorySize, MAIN_LDS);
  k_prep<<<256, 512, 0, stream>>>((const u16*)d_in[0], (const u16*)d_in[1], (const u16*)d_in[2], (const u16*)d_in[3],
                                  (const u16*)d_in[4], (const u16*)d_in[5], (const u16*)d_in[8], (const u16*)d_in[10], (char*)d_ws);
  k_main<<<256, 512, MAIN_LDS, stream>>>((const u16*)d_in[6], (const u16*)d_in[9], (const u16*)d_in[11], (char*)d_ws, (u16*)d_out);
}
```

```cpp
#define WARM_CODE 1
#define WARM_TLB 1
#include <hip/hip_runtime.h>
#include <hip/hip_bf16.h>

typedef unsigned int u32;
typedef unsigned short u16;
typedef __attribute__((ext_vector_type(4))) u32 v4u;
typedef __attribute__((ext_vector_type(2))) u32 v2u;
typedef __attribute__((ext_vector_type(4))) float f32x4;
typedef __attribute__((ext_vector_type(2))) float f32x2;
typedef __attribute__((ext_vector_type(16))) float f32x16;
typedef __attribute__((ext_vector_type(8))) __bf16 bf16x8;
typedef __attribute__((ext_vector_type(2))) __bf16 bf16x2;
typedef __attribute__((ext_vector_type(8))) _Float16 f16x8;
typedef __attribute__((ext_vector_type(2))) _Float16 f16x2;
typedef __attribute__((ext_vector_type(4))) short s16x4;
#define LAS __attribute__((address_space(3)))
#define DI __device__ __forceinline__
#define STAMP(i) __builtin_amdgcn_sched_barrier(0)
#define STAMP_FLUSH(kid)

constexpr int SL = 2048, DM = 512, NS = 128, FF = 1792;
constexpr int XC_OFF = 0;
constexpr int XM_OFF = 4 << 20;
constexpr int PRM_OFF = 8 << 20;
constexpr int KSC_OFF = PRM_OFF + DM * 2560;
constexpr int CS_OFF = KSC_OFF + 2048;
constexpr int IDX_OFF = CS_OFF + 16384;
constexpr int W1F_OFF = 11 << 20;
constexpr int W2F_OFF = 13 << 20;
constexpr int FST_OFF = 15 << 20;
constexpr int CTL_OFF = FST_OFF + 32768;
constexpr int CNT_OFF = CTL_OFF;
constexpr int HF_OFF = CNT_OFF + 8192;
constexpr int PST_OFF = HF_OFF + 8192;
constexpr int CTL_BYTES = 8192 + 8192 + 262144;
constexpr int H_OFF = 16 << 20;
constexpr int H_LAYER = 32 * 8 * 2 * 14 * 1024;
constexpr int WS_BYTES = 32 << 20;
constexpr int SC1 = 16;
constexpr int SPIN_CAP = 1 << 16;
#define K0_AUX 0

DI u32 umin(u32 a, u32 b) { return a < b ? a : b; }
DI u32 umax(u32 a, u32 b) { return a > b ? a : b; }
DI float bf2f(u32 h) { return __builtin_bit_cast(float, h << 16); }
DI float bflo(u32 p) { return __builtin_bit_cast(float, p << 16); }
DI float bfhi(u32 p) { return __builtin_bit_cast(float, p & 0xffff0000u); }
DI u32 pack_bf16(float a, float b) { bf16x2 v = {(__bf16)a, (__bf16)b}; return __builtin_bit_cast(u32, v); }
DI float bfr(float a) { return bflo(pack_bf16(a, a)); }
DI u32 pack_f16(float a, float b) { f16x2 v = {(_Float16)a, (_Float16)b}; return __builtin_bit_cast(u32, v); }
DI float dot2bf(u32 a, u32 b, float c) {
  return __builtin_amdgcn_fdot2_f32_bf16(__builtin_bit_cast(bf16x2, a), __builtin_bit_cast(bf16x2, b), c, false);
}
DI f32x16 mfma32(v4u a, v4u b, f32x16 c) {
  return __builtin_amdgcn_mfma_f32_32x32x16_bf16(__builtin_bit_cast(bf16x8, a), __builtin_bit_cast(bf16x8, b), c, 0, 0, 0);
}
DI f32x4 mfma16h(v4u a, v4u b, f32x4 c) {
  return __builtin_amdgcn_mfma_f32_16x16x32_f16(__builtin_bit_cast(f16x8, a), __builtin_bit_cast(f16x8, b), c, 0, 0, 0);
}
DI f32x4 mfma16b(v4u a, v4u b, f32x4 c) {
  return __builtin_amdgcn_mfma_f32_16x16x32_bf16(__builtin_bit_cast(bf16x8, a), __builtin_bit_cast(bf16x8, b), c, 0, 0, 0);
}
DI v4u tr_frag(const LAS char* p) {
  const s16x4 a = __builtin_amdgcn_ds_read_tr16_b64_v4i16((LAS s16x4*)p);
  const s16x4 c = __builtin_amdgcn_ds_read_tr16_b64_v4i16((LAS s16x4*)(p + 256));
  return __builtin_bit_cast(v4u, __builtin_shufflevector(a, c, 0, 1, 2, 3, 4, 5, 6, 7));
}
template <int CTRL> DI float dpp_add(float v) {
  const int o = __builtin_amdgcn_update_dpp(0, __builtin_bit_cast(int, v), CTRL, 0xf, 0xf, false);
  return v + __builtin_bit_cast(float, o);
}
DI float row_sum16(float v) { v = dpp_add<0x128>(v); v = dpp_add<0x124>(v); v = dpp_add<0x122>(v); return dpp_add<0x121>(v); }
DI float swap32_add(float a, float c) {
  const auto r = __builtin_amdgcn_permlane32_swap(__builtin_bit_cast(u32, a), __builtin_bit_cast(u32, c), false, false);
  const u32 r0 = r[0], r1 = r[1];
  return __builtin_bit_cast(float, r0) + __builtin_bit_cast(float, r1);
}
DI float swap16_add(float a, float c) {
  const auto r = __builtin_amdgcn_permlane16_swap(__builtin_bit_cast(u32, a), __builtin_bit_cast(u32, c), false, false);
  const u32 r0 = r[0], r1 = r[1];
  return __builtin_bit_cast(float, r0) + __builtin_bit_cast(float, r1);
}
DI float wave_sum(float v) { v = row_sum16(v); v = swap16_add(v, v); return swap32_add(v, v); }
DI void ln_stats(float s, float q, float& m, float& v, float& mb, float& rs) {
  m = s * (1.0f / DM);
  v = fmaxf(q * (1.0f / DM) - m * m, 0.f);
  const u32 pk = pack_bf16(m, __builtin_amdgcn_sqrtf(bfr(bfr(v) + 1e-5f)));
  mb = bflo(pk); rs = __builtin_amdgcn_rcpf(bfhi(pk));
}
DI u32 npz(float a) { return umin(__builtin_bit_cast(u32, a), 0xfffffffeu); }
#define LD128(vo, so, aux) __builtin_amdgcn_raw_buffer_load_b128(rs, (vo), (so), (aux))
#define LD64(vo, so, aux) __builtin_amdgcn_raw_buffer_load_b64(rs, (vo), (so), (aux))
#define LD32(vo, so, aux) __builtin_amdgcn_raw_buffer_load_b32(rs, (vo), (so), (aux))
#define ST128(v, vo, so, aux) __builtin_amdgcn_raw_buffer_store_b128((v), rs, (vo) + (so), 0, (aux))
#define ST64(v, vo, so, aux) __builtin_amdgcn_raw_buffer_store_b64((v), rs, (vo), (so), (aux))
#define ST32(v, vo, so, aux) __builtin_amdgcn_raw_buffer_store_b32((v), rs, (vo), (so), (aux))
#define DRAIN() asm volatile("s_waitcnt vmcnt(0)" ::: "memory")
#define CBAR() asm volatile("" ::: "memory")
#define SBAR() __builtin_amdgcn_sched_barrier(0)
DI int opaque_v(int x) { asm volatile("" : "+v"(x)); return x; }
template <typename T, int N> DI void undef_regs(T (&a)[N]) {
#pragma unroll
  for (int i = 0; i < N; ++i) asm volatile("" : "=v"(a[i]));
}
DI int opaque_s(int x) { asm volatile("" : "+s"(x)); return x; }

constexpr int KT_REL = 24576 + 16;
constexpr int KT_CH = 1312;
DI void cexp_p(float arl, float rh, float rl, float p, float& re, float& im) {
  const float e = __builtin_amdgcn_exp2f(arl * p);
  const float r = __builtin_fmaf(rl, p, __builtin_amdgcn_fractf(rh * p));
  re = e * __builtin_amdgcn_cosf(r);
  im = e * __builtin_amdgcn_sinf(r);
}
DI float k_scale(float bs, int sign) {
  const int ex = (int)((__builtin_bit_cast(u32, bs) >> 23) & 0xff) - 126;
  int e = 15 - ex; e = e > 100 ? 100 : (e < -100 ? -100 : e);
  return __builtin_bit_cast(float, (u32)(127 + sign * e) << 23);
}
__global__ __launch_bounds__(512, 2) void k_prep(const u16* __restrict__ x0, const u16* __restrict__ logA,
    const u16* __restrict__ Aim, const u16* __restrict__ Cre, const u16* __restrict__ Cim,
    const u16* __restrict__ logdt, const u16* __restrict__ w1, const u16* __restrict__ w2, char* __restrict__ ws, const u16* outp) {
  __shared__ float bnd[4];
  __shared__ float csp[8][32];
  __shared__ __attribute__((aligned(16))) char wtile[8 * 2048];
  __shared__ u32 trt[64 * 33];
  const int b = blockIdx.x, tid = threadIdx.x, l = tid & 63, w = __builtin_amdgcn_readfirstlane(tid >> 6);
  const __amdgpu_buffer_rsrc_t rs = __builtin_amdgcn_make_buffer_rsrc(ws, 0, WS_BYTES, 0x00020000);
  STAMP(0);
  u32 rla = 0, rai = 0, rcr = 0, rci = 0, rdt = 0;
  if (w < 4) {
    const int o = 2 * b * NS + tid;
    rla = logA[o]; rai = Aim[o]; rcr = Cre[o]; rci = Cim[o]; rdt = logdt[2 * b + (w >> 1)];
  }
  const v4u xr = *(const v4u*)(x0 + (8 * b + w) * DM + 8 * l);
  const v4u xt = *(const v4u*)(x0 + (64 * (b >> 3) + (tid >> 3)) * DM + 64 * (b & 7) + 8 * (tid & 7));
  v4u wr0 = {0u, 0u, 0u, 0u}, wr1 = wr0;
  const int fr = (b < 112 ? b : b - 112) * 16 + 2 * w;
  if (b < 112) {
    const u16* p = w1 + (16 * (fr & 31) + (l >> 2)) * FF + 32 * (fr >> 5) + 8 * (l & 3);
    wr0 = *(const v4u*)p; wr1 = *(const v4u*)(p + 16 * FF);
  } else if (b < 224) {
    const int r1 = fr >> 1, ks2 = r1 % 14, r2 = r1 / 14;
    const u16* p = w2 + (224 * (r2 & 7) + 16 * ks2 + (l >> 2)) * DM + 64 * (r2 >> 3) + 8 * (l & 3);
    wr0 = *(const v4u*)p; wr1 = *(const v4u*)(p + 32);
  }
  u32 wv = 0;
  {
    unsigned long long pc;
    asm volatile("s_getpc_b64 s[90:91]\n\ts_add_u32 s90, s90, _Z6k_mainPKtS0_S0_PcPt@rel32@lo+4\n\ts_addc_u32 s91, s91, _Z6k_mainPKtS0_S0_PcPt@rel32@hi+12\n\ts_mov_b64 %0, s[90:91]"
                 : "=s"(pc) :: "s90", "s91", "scc");
    if (WARM_CODE && w == 7) {
      const v4u c0 = *(const v4u*)(pc + (b >> 3) * 1024 + l * 16);
      wv = c0[0] ^ c0[3];
      if ((b >> 3) == 0) { const v4u c1 = *(const v4u*)(pc + 32768 + l * 16); wv ^= c1[1]; }
    }
    if (WARM_TLB && w == 6 && l < 17) wv = l < 16 ? LD32(l * (2 << 20) + 4096 + (b >> 3) * 128, 0, 0) : *(const u32*)(outp + (b >> 3) * 32768);
  }
  ST128((v4u{~0u, ~0u, ~0u, ~0u}), tid * 16, XM_OFF + b * 8192, K0_AUX);
  if (tid >= 128 && tid < 132) ST128((v4u{~0u, ~0u, ~0u, ~0u}), (tid - 128) * 16, FST_OFF + 16384 + b * 64, K0_AUX);
  if (tid < CTL_BYTES / 16 / 256) ST128((v4u{0u, 0u, 0u, 0u}), tid * 16, CTL_OFF + b * (CTL_BYTES / 256), K0_AUX);
  if (b == 255) {
    const int c = tid >> 8, x = tid & 255, col = x >> 4, row = x & 15, i16 = l & 15;
    const int p0 = 128 * (row >> 3) + (row & 7) + 8 * col;
    const int p1 = (row < 8 ? 256 + 128 * (row >> 2) + 2 * (row & 3) : 512 + 128 * ((row - 8) >> 1) + 4 * (row & 1)) + 8 * col;
    const int c1 = row < 8 ? 128 + (p1 >> 1) : 256 + (p1 >> 2);
    const int c2 = row < 8 ? 512 + 16 * row + col : (x == 8 ? 640 : 641);
    const int kb = KT_REL + c * KT_CH;
    u32 gx[8];
#pragma unroll
    for (int j = 0; j < 8; ++j) {
      const int la = 8 * x + 7 - 2 * j, lb = la - 1;
      const u32 hb = pack_bf16((float)la, (float)lb);
      const int ia = la < 128 ? la : (int)(hb & 0xffffu) - 17024, ib = lb < 128 ? lb : (int)(hb >> 16) - 17024;
      gx[j] = (u32)(kb + 2 * ia) | ((u32)(kb + 2 * ib) << 16);
    }
    const f32x4 pp = {(float)(8 * i16), (float)(128 * (i16 >> 3) + (i16 & 7)),
                      (float)(i16 < 8 ? 256 + 128 * (i16 >> 2) + 2 * (i16 & 3) : 512 + 128 * ((i16 - 8) >> 1) + 4 * (i16 & 1)),
                      (float)(i16 < 8 ? 1024 + 128 * i16 : 2048)};
    ST128(__builtin_bit_cast(v4u, pp), tid * 16, IDX_OFF, K0_AUX);
    ST128((v4u{(u32)(kb + 2 * p0), (u32)(kb + 2 * c1), (u32)(kb + 2 * c2), 0u}), tid * 16, IDX_OFF + 8192, K0_AUX);
    ST128((v4u{gx[0], gx[1], gx[2], gx[3]}), tid * 16, IDX_OFF + 16384, K0_AUX);
    ST128((v4u{gx[4], gx[5], gx[6], gx[7]}), tid * 16, IDX_OFF + 24576, K0_AUX);
  }
  STAMP(1);
  float arl = 0.f, rh = 0.f, rl = 0.f, cr = 0.f, ci = 0.f;
  if (w < 4) {
    const float dt = bfr(expf(bf2f(rdt)));
    arl = -bfr(expf(bf2f(rla))) * dt * 1.4426950408889634f;
    double t = (double)(bf2f(rai) * dt) * 0.15915494309189533577;
    t -= __builtin_rint(t);
    rh = __builtin_bit_cast(float, __builtin_bit_cast(u32, (float)t) & 0xfffff000u);
    rl = (float)(t - (double)rh);
    cr = bf2f(rcr) * dt * 2.0f; ci = bf2f(rci) * dt * 2.0f;
    const float bs = wave_sum(fabsf(cr) + fabsf(ci));
    if (l == 0) bnd[w] = bs;
  }
  STAMP(2);
  {
    float s = 0.f, q = 0.f;
#pragma unroll
    for (int j = 0; j < 4; ++j) {
      const u32 p = xr[j];
      s = dot2bf(p, 0x3f803f80u, s); q = dot2bf(p, p, q);
      trt[(tid >> 3) * 33 + 4 * (tid & 7) + j] = xt[j];
    }
    s = wave_sum(s); q = wave_sum(q);
    float m, v, mb, rcs;
    ln_stats(s, q, m, v, mb, rcs);
    if (l < 2) ST32(__builtin_bit_cast(u32, l ? rcs : mb), l * 16, FST_OFF + (2 * b + (w >> 2)) * 32 + (w & 3) * 4, K0_AUX);
  }
  if (b < 224) {
    LAS char* wl = (LAS char*)wtile + w * 2048;
    *(LAS v4u*)(wl + l * 16) = wr0; *(LAS v4u*)(wl + 1024 + l * 16) = wr1;
    const int hs = b < 112 ? 512 : 256, ts = b < 112 ? 256 : 512;
    const LAS char* rp = wl + (l >> 5) * hs + ((l & 15) >> 2) * 64 + 32 * ((l >> 4) & 1) + 8 * (l & 3);
    const s16x4 t0 = __builtin_amdgcn_ds_read_tr16_b64_v4i16((LAS s16x4*)rp), t1 = __builtin_amdgcn_ds_read_tr16_b64_v4i16((LAS s16x4*)(rp + ts));
    const s16x4 t2 = __builtin_amdgcn_ds_read_tr16_b64_v4i16((LAS s16x4*)(rp + 1024)), t3 = __builtin_amdgcn_ds_read_tr16_b64_v4i16((LAS s16x4*)(rp + 1024 + ts));
    const v4u o1 = __builtin_bit_cast(v4u, __builtin_shufflevector(t0, t1, 0, 1, 2, 3, 4, 5, 6, 7));
    const v4u o2 = __builtin_bit_cast(v4u, __builtin_shufflevector(t2, t3, 0, 1, 2, 3, 4, 5, 6, 7));
    float cs = 0.f;
#pragma unroll
    for (int j = 0; j < 4; ++j) { const u32 p1 = o1[j], p2 = o2[j]; cs = dot2bf(p1, 0x3f803f80u, cs); cs = dot2bf(p2, 0x3f803f80u, cs); }
    ST128(o1, l * 16, (b < 112 ? W1F_OFF : W2F_OFF) + fr * 1024, K0_AUX);
    ST128(o2, l * 16 + 1024, (b < 112 ? W1F_OFF : W2F_OFF) + fr * 1024, K0_AUX);
    cs = swap32_add(cs, cs);
    if (l < 32) csp[w][l] = cs;
  }
  STAMP(5);
  __syncthreads();
  STAMP(6);
  {
    if (w < 4) {
      const float bs = bnd[w & 2] + bnd[(w & 2) + 1], sc = k_scale(bs, 1);
      const int vo = (tid >> 2) * 80 + (tid & 3) * 4;
      ST32(__builtin_bit_cast(u32, arl), vo, PRM_OFF + b * 5120, K0_AUX);
      ST32(__builtin_bit_cast(u32, rh), vo + 16, PRM_OFF + b * 5120, K0_AUX);
      ST32(__builtin_bit_cast(u32, rl), vo + 32, PRM_OFF + b * 5120, K0_AUX);
      ST32(__builtin_bit_cast(u32, cr * sc), vo + 48, PRM_OFF + b * 5120, K0_AUX);
      ST32(__builtin_bit_cast(u32, ci * sc), vo + 64, PRM_OFF + b * 5120, K0_AUX);
      if ((tid & 127) == 0) ST32(__builtin_bit_cast(u32, k_scale(bs, -1)), (tid >> 7) * 4, KSC_OFF + 8 * b, K0_AUX);
    }
    if (b < 112 && tid < 32) {
      const float cs = ((csp[0][tid] + csp[1][tid]) + (csp[2][tid] + csp[3][tid])) + ((csp[4][tid] + csp[5][tid]) + (csp[6][tid] + csp[7][tid]));
      ST32(__builtin_bit_cast(u32, cs), tid * 4, CS_OFF + (b & 1) * (FF * 4) + (b >> 1) * 128, K0_AUX);
    }
    const int cq = tid >> 3, oct = tid & 7;
    const u16* t16 = (const u16*)trt;
    v4u o;
#pragma unroll
    for (int j = 0; j < 4; ++j)
      o[j] = (u32)t16[(8 * oct + 2 * j) * 66 + cq] | ((u32)t16[(8 * oct + 2 * j + 1) * 66 + cq] << 16);
    ST128(o, cq * 4096 + oct * 16, XC_OFF + (b & 7) * (64 * 4096) + (b >> 3) * 128, K0_AUX);
  }
  asm volatile("" :: "v"(wv));
  STAMP(8);
  STAMP_FLUSH(0);
}

constexpr int KC_CH = 8 * 260 * 16;
constexpr int L_U = 2 * KC_CH;
constexpr int U_CH = 4672;
constexpr int L_R = L_U + 2 * U_CH;
constexpr int L_XB = L_R + 32768;
constexpr int L_PB = L_XB + 256;
constexpr int L_KT = L_R + KT_REL;
constexpr int RB_ROW = 144;
constexpr int L_S = L_R + 8 * 64 * RB_ROW;
constexpr int L_B1 = L_S;
constexpr int L_CS = L_B1 + 896;
constexpr int L_SP = L_CS + 896;
constexpr int L_AC = L_SP + 4096;
constexpr int L_B2 = L_AC + 64;
constexpr int L_DK = L_B2 + 256;
constexpr int L_AB = L_DK + 16;
constexpr int L_END = L_AB + 512;
constexpr int MAIN_LDS = L_END;
#define MAPV 1
#define MAP_T(b) ((((b) >> 1) & 3) * 8 + ((b) >> 5))
#define MAP_S(b) (((b) & 1) * 4 + (((b) >> 3) & 3))
#define W1_SPLIT 0
constexpr int W2A = 12;

DI f32x2 gelu2(f32x2 x) {
  const f32x2 t = x * (x * x * -0.10294324f + -2.3022082f);
  f32x2 e; e.x = __builtin_amdgcn_exp2f(t.x); e.y = __builtin_amdgcn_exp2f(t.y);
  e = e + 1.0f;
  f32x2 r; r.x = __builtin_amdgcn_rcpf(e.x); r.y = __builtin_amdgcn_rcpf(e.y);
  return x * r;
}
DI u32 max4(v4u a) { return umax(umax(a[0], a[1]), umax(a[2], a[3])); }
DI f32x4 unpack4(v2u p) { const u32 a = p[0], c = p[1]; return f32x4{bflo(a), bfhi(a), bflo(c), bfhi(c)}; }
DI f32x4 conv_ln(LAS char* dst, v2u xp, v4u mm, v4u rr, float dk) {
  const f32x4 x = unpack4(xp);
  const f32x4 u = (x - __builtin_bit_cast(f32x4, mm)) * __builtin_bit_cast(f32x4, rr);
  *(LAS v2u*)dst = v2u{pack_f16(u[0], u[1]), pack_f16(u[2], u[3])};
  return x + u * dk;
}

template <bool COMM>
DI void main_body(LAS char* sm, const __amdgpu_buffer_rsrc_t rs, char* ws, u16* out, const u16* dskip, const u16* b1,
                  const u16* b2, int b0, int w0) {
  v4u wf[32], wa[W2A];
  f32x4 xr0, xr1;
#define LOAD_W(lo, hi) do { _Pragma("unroll") for (int ks = (lo); ks < (hi); ++ks) { \
      if constexpr (!COMM) wf[ks] = LD128(l16 + (ks & 3) * 1024, W1F_OFF + (7 * s + w) * 32768 + (ks >> 2) * 4096, 0); \
      else if (ks < 28) wf[ks] = LD128(l16 + (ks & 3) * 1024, W2F_OFF + (s * 8 + 7) * 28672 + (ks >> 2) * 4096, 0); } \
      SBAR(); } while (0)
#define CW(k) do { if (!COMM || ly == 0) LOAD_W(k, (k) + 1); } while (0)
  {
    const int b = opaque_s(b0), w = opaque_s(w0), tid = opaque_v(threadIdx.x), l = tid & 63, l16 = l * 16, s = MAP_S(b);
    const int cc = (tid >> 3) & 1, q8 = (tid >> 4) * 8 + (tid & 7);
    const int kk = l >> 4, i16 = l & 15;
    {
      const v4u pr = LD128(tid * 16, PRM_OFF + b * 5120, 0);
      if (tid < 16) *(LAS v4u*)(sm + (tid >> 3) * KC_CH + (tid & 7) * 4160) = v4u{0u, 0u, 0u, 0u};
      if (tid >= 64 && tid < 128) *(LAS v4u*)(sm + L_U + ((tid >> 5) & 1) * U_CH + (tid & 31) * 16) = v4u{0u, 0u, 0u, 0u};
      if (tid >= 128 && tid < 130) *(LAS v4u*)(sm + L_KT - 16 + (tid - 128) * KT_CH) = v4u{0u, 0u, 0u, 0u};
      *(LAS v4u*)(sm + L_XB + tid * 16) = pr;
    }
    const f32x4 pp4 = __builtin_bit_cast(f32x4, LD128(l16, IDX_OFF, 0));
    {
      SBAR();
      const u32 on = (u32)opaque_v(-1);
      const int t = MAP_T(b);
      ST64((v2u{on, on}), (tid >> 4) * 32768 + (tid & 15) * 8, XM_OFF + (2 << 20) + 128 * b, SC1);
      ST64((v2u{on, on}), (tid >> 4) * 32768 + (tid & 15) * 8, XM_OFF + (3 << 20) + 128 * b, SC1);
      ST64((v2u{on, on}), (tid >> 3) * 4096 + (tid & 7) * 8, XC_OFF + (2 << 20) + s * (64 * 4096) + t * 64, SC1);
      ST64((v2u{on, on}), (tid >> 3) * 4096 + (tid & 7) * 8, XC_OFF + (2 << 20) + s * (64 * 4096) + t * 64 + 2048, SC1);
      SBAR();
    }
    v4u tix, gx0, gx1, m0, r0, m1, r1;
    v2u xa, xb;
    float dk, ksc, bias2, bb1, cs0, cs1;
    __syncthreads();
    const int c = tid >> 8, x = tid & 255;
    const float pB = pp4[0], pA0 = pp4[1], pA1 = pp4[2], pA2 = pp4[3];
    f32x4 acc0 = {0.f, 0.f, 0.f, 0.f}, acc1 = acc0, acc2 = acc0;
    STAMP(1);
#pragma unroll
    for (int g = 0; g < 2; ++g) {
      v4u fa0, fa1, fa2, fbh, fbl;
#pragma unroll
      for (int j = 0; j < 4; ++j) {
        const LAS float* pq = (const LAS float*)(sm + L_XB + w * 640 + g * 320 + kk * 80) + j;
        const float arl = pq[0], rh = pq[4], rl = pq[8], cr = pq[12], ci = pq[16];
        float br, bi, er, ei;
        cexp_p(arl, rh, rl, pB, br, bi);
        bi = -bi;
        const u32 hi = pack_f16(br, bi);
        const f16x2 hv = __builtin_bit_cast(f16x2, hi);
        fbh[j] = hi;
        fbl[j] = pack_f16(br - (float)hv.x, bi - (float)hv.y);
        cexp_p(arl, rh, rl, pA0, er, ei);
        fa0[j] = pack_f16(cr * er - ci * ei, cr * ei + ci * er);
        cexp_p(arl, rh, rl, pA1, er, ei);
        fa1[j] = pack_f16(cr * er - ci * ei, cr * ei + ci * er);
        cexp_p(arl, rh, rl, pA2, er, ei);
        fa2[j] = pack_f16(cr * er - ci * ei, cr * ei + ci * er);
        SBAR();
        LOAD_W(8 * g + 2 * j, 8 * g + 2 * j + 2);
        if (g == 0 && j == 0) { xa = LD64(cc * 4096 + q8 * 8, XC_OFF + b * 8192, 0); xb = LD64(cc * 4096 + q8 * 8 + 2048, XC_OFF + b * 8192, 0); }
        if (g == 0 && j == 1) { m0 = LD128(q8 * 32, FST_OFF, 0); r0 = LD128(q8 * 32 + 16, FST_OFF, 0); }
        if (g == 0 && j == 2) { m1 = LD128(q8 * 32, FST_OFF + 8192, 0); r1 = LD128(q8 * 32 + 16, FST_OFF + 8192, 0); }
        if (g == 0 && j == 3) { dk = bf2f(dskip[2 * b + cc]); ksc = __builtin_bit_cast(float, LD32(cc * 4, KSC_OFF + 8 * b, 0)); }
        if (g == 1 && j == 0) { bias2 = bf2f(b2[64 * s + l]); bb1 = bf2f(b1[224 * s + (tid < 224 ? tid : 0)]); }
        if (g == 1 && j == 1) { cs0 = __builtin_bit_cast(float, LD32(tid * 4, CS_OFF + s * 896, 0)); cs1 = __builtin_bit_cast(float, LD32(tid * 4, CS_OFF + FF * 4 + s * 896, 0)); }
        if (g == 1 && j == 2) { tix = LD128(tid * 16, IDX_OFF + 8192, 0); }
        if (g == 1 && j == 3) { gx0 = LD128(tid * 16, IDX_OFF + 16384, 0); gx1 = LD128(tid * 16, IDX_OFF + 24576, 0); }
        SBAR();
      }
      acc0 = mfma16h(fa0, fbh, acc0); acc1 = mfma16h(fa1, fbh, acc1); acc2 = mfma16h(fa2, fbh, acc2);
      acc0 = mfma16h(fa0, fbl, acc0); acc1 = mfma16h(fa1, fbl, acc1); acc2 = mfma16h(fa2, fbl, acc2);
    }
    *(LAS f32x4*)(sm + L_R + w * 3072 + i16 * 64 + kk * 16) = acc0;
    *(LAS f32x4*)(sm + L_R + w * 3072 + 1024 + i16 * 64 + kk * 16) = acc1;
    *(LAS f32x4*)(sm + L_R + w * 3072 + 2048 + i16 * 64 + kk * 16) = acc2;
    STAMP(60);
    xr0 = conv_ln(sm + L_U + cc * U_CH + 512 + q8 * 8, xa, m0, r0, dk);
    xr1 = conv_ln(sm + L_U + cc * U_CH + 2560 + q8 * 8, xb, m1, r1, dk);
    if (tid < 224) { *(LAS float*)(sm + L_B1 + tid * 4) = bb1; *(LAS float*)(sm + L_CS + tid * 4) = cs0 + cs1; }
    if (tid < 64) *(LAS float*)(sm + L_B2 + tid * 4) = bias2;
    if ((tid & ~8) == 0) *(LAS f32x2*)(sm + L_DK + cc * 8) = f32x2{dk, ksc};
    STAMP(61);
    __syncthreads();
    {
      const LAS float* pp = (const LAS float*)(sm + L_R + c * 12288) + x;
      *(LAS _Float16*)(sm + L_R + tix[0]) = (_Float16)((pp[0] + pp[768]) + (pp[1536] + pp[2304]));
      *(LAS _Float16*)(sm + L_R + tix[1]) = (_Float16)((pp[256] + pp[1024]) + (pp[1792] + pp[2560]));
      *(LAS _Float16*)(sm + L_R + tix[2]) = (_Float16)((pp[512] + pp[1280]) + (pp[2048] + pp[2816]));
    }
    __syncthreads();
    STAMP(62);
    {
      u32 wd[8];
#pragma unroll
      for (int j = 0; j < 8; ++j) {
        const u32 g = j < 4 ? gx0[j & 3] : gx1[j & 3];
        wd[j] = (u32)*(const LAS u16*)(sm + L_R + (g & 0xffffu)) | ((u32)*(const LAS u16*)(sm + L_R + (g >> 16)) << 16);
      }
#pragma unroll
      for (int cp = 0; cp < 8; ++cp) {
        const int sg = 7 - cp;
        v4u o;
#pragma unroll
        for (int k = 0; k < 4; ++k)
          o[k] = (sg & 1) ? __builtin_amdgcn_alignbit(wd[(sg + 1) / 2 + k], wd[(sg - 1) / 2 + k], 16) : wd[sg / 2 + k];
        *(LAS v4u*)(sm + c * KC_CH + cp * 4160 + (x + 1) * 16) = o;
      }
    }
    if constexpr (COMM) {
#pragma unroll
      for (int i = 16; i < 28; ++i) asm volatile("" : "=v"(wf[i]));
    }
  }

#pragma unroll 1
  for (int ly = 0; ly < 2; ++ly) {
    const int b = opaque_s(b0), w = opaque_s(w0);
    const int t = MAP_T(b), s = MAP_S(b);
    int tid = opaque_v(threadIdx.x), l = tid & 63, l16 = l * 16;
    const int xmo = XM_OFF + ly * (2 << 20) + t * 32768 + w * 1024;
    const int w2off = W2F_OFF + ((s * 8 + w) * 28) * 1024;
    const int hoff = H_OFF + ly * H_LAYER + (t * 8 + s) * (28 * 1024) + (2 * w) * 1024;
    v4u xs[4];
    auto x_load = [&](int half) {
#pragma unroll
      for (int k = 0; k < 4; ++k) xs[k] = LD128(l16, xmo + half * (1 << 20) + k * 8192, SC1);
    };
    auto x_ok = [&]() { return __all(umax(umax(max4(xs[0]), max4(xs[1])), umax(max4(xs[2]), max4(xs[3]))) != 0xffffffffu); };
    auto x_stage = [&](int half) {
      if (!x_ok()) {
        int sp = 0;
#pragma unroll 1
        do {
          STAMP(56 + half + 2 * ly);
          __builtin_amdgcn_s_sleep(4);
          CBAR();
          x_load(half);
        } while (!x_ok() && ++sp < SPIN_CAP);
      }
#pragma unroll
      for (int k = 0; k < 4; ++k) *(LAS v4u*)(sm + L_R + half * 32768 + k * 8192 + tid * 16) = xs[k];
    };
    v2u pg;
    bool fin0 = true;
    auto fin_load = [&](int hf) { pg = LD64(l * 8, PST_OFF + ((t * 2 + hf) * 8 + s) * 512, SC1); };
    auto fin_try = [&](int hf) -> bool {
      if (!__all(pg[1] == 1u)) return false;
      const u32 e0 = pg[0];
      float v = __builtin_bit_cast(float, e0);
      v = swap32_add(v, v); v = swap16_add(v, v); v = dpp_add<0x128>(v);
      const float o = __builtin_bit_cast(float, __builtin_amdgcn_update_dpp(0, __builtin_bit_cast(int, v), 0xb1, 0xf, 0xf, false));
      float mm, vr, mb, rcs;
      ln_stats((l & 1) ? o : v, (l & 1) ? v : o, mm, vr, mb, rcs);
      if (l < 8) ST32(npz((l & 1) ? rcs : mb), (l & 1) * 16 + (l >> 1) * 4, FST_OFF + 16384 + hf * 8192 + (8 * t + s) * 32, SC1);
      return true;
    };
    auto fin_wait = [&](int hf) {
#pragma unroll 1
      for (int sp = 0; sp < SPIN_CAP; ++sp) {
        CBAR();
        fin_load(hf);
        if (fin_try(hf)) break;
        __builtin_amdgcn_s_sleep(1);
      }
    };
    v2u ix; v4u im, ir;
    auto cin_load = [&](int hf) {
      const int cc = (tid >> 3) & 1, q8 = (tid >> 4) * 8 + (tid & 7);
      ix = LD64(cc * 4096 + q8 * 8, XC_OFF + (2 << 20) + b * 8192 + hf * 2048, SC1);
      im = LD128(q8 * 32, FST_OFF + 16384 + hf * 8192, SC1); ir = LD128(q8 * 32 + 16, FST_OFF + 16384 + hf * 8192, SC1);
    };
    auto cin_ok = [&]() { return __all(umax(umax(ix[0], ix[1]), umax(max4(im), max4(ir))) != 0xffffffffu); };
    auto cin_ln = [&](int hf) -> f32x4 {
      if (!cin_ok()) {
        int sp = 0;
#pragma unroll 1
        do {
          STAMP(29 + 2 * hf);
          __builtin_amdgcn_s_sleep(4);
          CBAR();
          cin_load(hf);
        } while (!cin_ok() && ++sp < SPIN_CAP);
      }
      const int cc = (tid >> 3) & 1, q8 = (tid >> 4) * 8 + (tid & 7);
      return conv_ln(sm + L_U + cc * U_CH + 512 + hf * 2048 + q8 * 8, ix, im, ir, *(const LAS float*)(sm + L_DK + cc * 8));
    };
    CW(16); CW(17); CW(18); CW(19);
    STAMP(2 + 30 * ly);
    __syncthreads();
    STAMP(3 + 30 * ly);
    {
      const int c16 = l & 15, g4 = l >> 4;
      const LAS char* pa = sm + (w >> 2) * KC_CH + (c16 & 7) * 4160 + (3 + (c16 >> 3) - g4) * 16 + (w & 3) * 128;
      const LAS char* pb = sm + L_U + (w >> 2) * U_CH + (30 + 2 * c16 + g4) * 16 - (w & 3) * 128;
      LAS char* pp = sm + L_R + w * 4096 + (w >> 2) * 128 + ((4 * c16 + g4) ^ ((c16 >> 1) & 3)) * 16;
      const int xmv = (tid >> 4) * 32768 + (tid & 15) * 8;
      const LAS char* pr = sm + L_R + ((tid >> 3) & 1) * 16512 + ((((tid >> 4) << 3) | (tid & 7)) ^ ((tid >> 4) & 3)) * 16;
      auto conv_out = [&](int half, f32x4 xr) {
        const LAS char* p = pr + (half ? L_PB - L_R : 0);
        const float ksc = *(const LAS float*)(sm + L_DK + ((tid >> 3) & 1) * 8 + 4);
        const f32x4 y = (*(const LAS f32x4*)p + *(const LAS f32x4*)(p + 4096)) + (*(const LAS f32x4*)(p + 8192) + *(const LAS f32x4*)(p + 12288));
        const f32x4 o = y * ksc + xr;
        ST64((v2u{umin(pack_bf16(o[0], o[1]), 0xfffffffeu), umin(pack_bf16(o[2], o[3]), 0xfffffffeu)}), xmv,
             XM_OFF + ly * (2 << 20) + half * (1 << 20) + 128 * b, SC1);
      };
      {
        f32x4 acc[4];
#pragma unroll
        for (int i = 0; i < 4; ++i) acc[i] = f32x4{0.f, 0.f, 0.f, 0.f};
        v4u fa[2][4], fb[2][4];
#pragma unroll
        for (int tau = 0; tau < 2; ++tau)
#pragma unroll
          for (int i = 0; i < 4; ++i) { fa[tau][i] = *(const LAS v4u*)(pa + i * 512 + tau * 64); fb[tau][i] = *(const LAS v4u*)(pb + i * 512 - tau * 64); }
#pragma unroll
        for (int tau = 0; tau < 2; ++tau)
#pragma unroll
          for (int sx = 0; sx < 4; ++sx) {
#pragma unroll
            for (int e = 0; e + sx < 4; ++e) acc[sx + e] = mfma16h(fa[tau][sx], fb[tau][e], acc[sx + e]);
            if (sx == 0) { SBAR(); CW(20 + tau); }
          }
#pragma unroll
        for (int i = 0; i < 4; ++i) *(LAS f32x4*)(pp + i * 1024) = acc[i];
      }
      v4u fa[8], fb[4];
#pragma unroll
      for (int i = 0; i < 4; ++i) { fb[i] = *(const LAS v4u*)(pb + i * 512); fa[7 - i] = *(const LAS v4u*)(pa + (7 - i) * 512); }
#pragma unroll
      for (int i = 1; i < 4; ++i) fa[i] = *(const LAS v4u*)(pa + i * 512);
      SBAR();
      CW(22); CW(23);
      STAMP(4 + 30 * ly);
      __syncthreads();
      conv_out(0, xr0);
      STAMP(5 + 30 * ly);
      {
        f32x4 acc[4];
#pragma unroll
        for (int i = 0; i < 4; ++i) acc[i] = f32x4{0.f, 0.f, 0.f, 0.f};
#pragma unroll
        for (int e = 0; e < 4; ++e) {
#pragma unroll
          for (int i = 0; i < 4; ++i) acc[i] = mfma16h(fa[4 + i - e], fb[e], acc[i]);
          fb[e] = *(const LAS v4u*)(pb + e * 512 - 64); fa[7 - e] = *(const LAS v4u*)(pa + (7 - e) * 512 + 64);
          SBAR();
          if (e & 1) CW(24 + (e >> 1));
        }
#pragma unroll
        for (int i = 3; i >= 1; --i) fa[i] = *(const LAS v4u*)(pa + i * 512 + 64);
        if (ly) cin_load(1);
        else { asm volatile("" : "=v"(ix), "=v"(im), "=v"(ir)); }
        SBAR();
#pragma unroll
        for (int e = 0; e < 4; ++e) {
#pragma unroll
          for (int i = 0; i < 4; ++i) acc[i] = mfma16h(fa[4 + i - e], fb[e], acc[i]);
          SBAR();
          if (e & 1) CW(26 + (e >> 1));
        }
#pragma unroll
        for (int i = 0; i < 4; ++i) { fa[i] = *(const LAS v4u*)(pa + i * 512); fa[4 + i] = *(const LAS v4u*)(pa + i * 512 + 64); }
        STAMP(8 + 30 * ly);
        if (ly) xr1 = cin_ln(1);
        SBAR();
        CW(28); CW(29);
        STAMP(9 + 30 * ly);
        __syncthreads();
        v4u fc[8];
#pragma unroll
        for (int i = 0; i < 4; ++i) { fc[i] = *(const LAS v4u*)(pb + (4 + i) * 512); fc[4 + i] = *(const LAS v4u*)(pb + (4 + i) * 512 - 64); }
#pragma unroll
        for (int tau = 0; tau < 2; ++tau)
#pragma unroll
          for (int e = 0; e < 4; ++e) {
#pragma unroll
            for (int i = e; i < 4; ++i) acc[i] = mfma16h(fa[4 * tau + i - e], fc[4 * tau + e], acc[i]);
            if (e == 0) { SBAR(); CW(30 + tau); }
          }
#pragma unroll
        for (int i = 0; i < 4; ++i) *(LAS f32x4*)(pp + (L_PB - L_R) + i * 1024) = acc[i];
      }
      STAMP(6 + 30 * ly);
      __syncthreads();
      x_load(0);
      SBAR();
      conv_out(1, xr1);
      STAMP(7 + 30 * ly);
      x_stage(0);
      SBAR();
      if constexpr (!COMM) {
        const u32 on = (u32)opaque_v(-1);
#pragma unroll
        for (int k = 0; k < 4; ++k) ST128((v4u{on, on, on, on}), l16 + (k & 1) * 1024, hoff + (k >> 1) * 14336, SC1);
        SBAR();
      }
    }
    STAMP(11 + 30 * ly);
    __syncthreads();
    tid = opaque_v(threadIdx.x); l = tid & 63; l16 = l * 16;
    const int h = l >> 5;
    const LAS char* xb = sm + L_R + (8 * h + ((l & 15) >> 2)) * 64 + 32 * ((l >> 4) & 1) + 8 * (l & 3);
    auto stats = [&](int half) {
      const LAS char* xq = sm + L_R + half * 32768 + (8 * (l >> 4) + ((l & 15) >> 2)) * 64 + 8 * (l & 3);
      const u32 one = (u32)opaque_v(0x3f803f80);
      const v4u ones = {one, one, one, one};
      f32x4 s0 = {0.f, 0.f, 0.f, 0.f}, q0 = s0, s1 = s0, q1 = s0;
      v4u f0[4], f1[4];
#pragma unroll
      for (int k = 0; k < 4; ++k) { f0[k] = tr_frag(xq + k * 2048); f1[k] = tr_frag(xq + k * 2048 + 32); }
#pragma unroll
      for (int k = 0; k < 16; ++k) {
        s0 = mfma16b(ones, f0[k & 3], s0); q0 = mfma16b(f0[k & 3], f0[k & 3], q0);
        s1 = mfma16b(ones, f1[k & 3], s1); q1 = mfma16b(f1[k & 3], f1[k & 3], q1);
        if (k + 4 < 16) { f0[k & 3] = tr_frag(xq + (k + 4) * 2048); f1[k & 3] = tr_frag(xq + (k + 4) * 2048 + 32); }
        if (half == 0 && k == 9) x_load(1);
        SBAR();
      }
      const float d0 = (l & 2) ? ((l & 1) ? q0[3] : q0[2]) : ((l & 1) ? q0[1] : q0[0]);
      const float d1 = (l & 2) ? ((l & 1) ? q1[3] : q1[2]) : ((l & 1) ? q1[1] : q1[0]);
#pragma unroll
      for (int g = 0; g < 2; ++g) {
        float m, v, mb, r1;
        ln_stats(g ? s1[0] : s0[0], g ? d1 : d0, m, v, mb, r1);
        const u32 p2 = pack_bf16((m - mb) * r1, v * r1 * r1);
        const float r2 = __builtin_amdgcn_rcpf(bfr(__builtin_amdgcn_sqrtf(bfr(bfhi(p2) + 1e-5f))));
        if ((l >> 4) == ((l >> 2) & 3)) *(LAS f32x2*)(sm + L_AB + (half * 32 + 16 * g + (l & 15)) * 8) = f32x2{r1 * r2, (mb * r1 + bflo(p2)) * r2};
      }
    };
    auto epi_quad = [&](const f32x16& acc, int q, f32x2 ab, u32& o0, u32& o1) {
      const f32x4 c4 = *(const LAS f32x4*)(sm + L_CS + (32 * w + 8 * q + 4 * h) * 4);
      const f32x4 b4 = *(const LAS f32x4*)(sm + L_B1 + (32 * w + 8 * q + 4 * h) * 4);
      const f32x4 x = f32x4{acc[4 * q], acc[4 * q + 1], acc[4 * q + 2], acc[4 * q + 3]} * ab.x + (b4 - c4 * ab.y);
      const f32x2 g0 = gelu2(f32x2{x[0], x[1]}), g1 = gelu2(f32x2{x[2], x[3]});
      o0 = umin(pack_bf16(g0.x, g0.y), 0xfffffffeu); o1 = umin(pack_bf16(g1.x, g1.y), 0xfffffffeu);
    };
    f32x16 acc0, acc1;
    constexpr int XD = 3;
    if constexpr (!COMM) {
      v4u xf[XD];
#pragma unroll
      for (int k = 0; k < XD; ++k) xf[k] = tr_frag(xb + k * 1024);
      acc0 = f32x16{};
#pragma unroll
      for (int ks = 0; ks < 32; ++ks) {
        acc0 = mfma32(wf[ks], xf[ks % XD], acc0);
        if (ks + XD < 32) xf[ks % XD] = tr_frag(xb + (ks + XD) * 1024);
        if (ks < 2 * W2A && !(ks & 1)) { const int k = ks >> 1; wa[k] = LD128(l16 + (k & 3) * 1024, w2off + (k >> 2) * 4096, 0); }
        if (ks == 15) x_load(1);
        SBAR();
      }
    } else {
      __builtin_amdgcn_s_setprio(2);
      stats(0);
      __builtin_amdgcn_s_setprio(0);
    }
    STAMP(12 + 30 * ly);
    x_stage(1);
    STAMP(13 + 30 * ly);
    __syncthreads();
    STAMP(14 + 30 * ly);
    const int orow = ly ? l : 8 * w + (l >> 3), oq = ly ? w : (l & 7);
    const v2u xres0 = *(const LAS v2u*)(sm + L_R + (64 * s + orow) * 64 + 8 * oq);
    const v2u xres1 = *(const LAS v2u*)(sm + L_XB + (64 * s + orow) * 64 + 8 * oq);
    const int hfo = HF_OFF + ly * 4096 + t * 128;
    if constexpr (!COMM) {
      v4u xf[XD];
#pragma unroll
      for (int k = 0; k < XD; ++k) xf[k] = tr_frag(xb + 32768 + k * 1024);
      const f32x2 ab = *(const LAS f32x2*)(sm + L_AB + (l & 31) * 8);
      u32 o[4];
      acc1 = f32x16{};
#pragma unroll
      for (int ks = 0; ks < 32; ++ks) {
        acc1 = mfma32(wf[ks], xf[ks % XD], acc1);
        if (ks + XD < 32) xf[ks % XD] = tr_frag(xb + 32768 + (ks + XD) * 1024);
        if (ks & 1) { const int j = ks >> 1, k = W2A + j; wf[j] = LD128(l16 + (k & 3) * 1024, w2off + (k >> 2) * 4096, 0); }
        if (ks % 6 == 1 && ks < 24) { const int q = ks / 6; epi_quad(acc0, q, ab, o[2 * (q & 1)], o[2 * (q & 1) + 1]); }
        if (ks == 11 || ks == 23) ST128((v4u{o[0], o[1], o[2], o[3]}), l16 + (ks / 12) * 1024, hoff, SC1);
        if (ks % 6 == 5 || ks >= 24) SBAR();
      }
    } else {
      if (l == 0) ST32(1u, 0, hfo + s * 4, SC1);
      __builtin_amdgcn_s_setprio(2);
      stats(1);
      __builtin_amdgcn_s_setprio(0);
#pragma unroll 1
      for (int sp = 0; sp < SPIN_CAP; ++sp) {
        CBAR();
        const u32 f = LD32((l & 7) * 4, hfo, SC1);
        if (__all(f == 1u)) break;
        __builtin_amdgcn_s_sleep(1);
      }
      CBAR();
    }
    STAMP(15 + 30 * ly);
    __syncthreads();
    v4u hf[14];
    const int hin = H_OFF + ly * H_LAYER + (t * 8 + w) * (28 * 1024);
    auto h_load = [&](int half, int lo, int hi) {
#pragma unroll
      for (int k = lo; k < hi; ++k) { hf[k] = LD128(l16, hin + (14 * half + k) * 1024, SC1); SBAR(); }
    };
    h_load(0, 0, 7);
    STAMP(16 + 30 * ly);
    if constexpr (!COMM) {
      const f32x2 ab = *(const LAS f32x2*)(sm + L_AB + (32 + (l & 31)) * 8);
#pragma unroll
      for (int sp = 0; sp < 2; ++sp) {
        u32 o[4];
        epi_quad(acc1, 2 * sp, ab, o[0], o[1]);
        epi_quad(acc1, 2 * sp + 1, ab, o[2], o[3]);
        ST128((v4u{o[0], o[1], o[2], o[3]}), l16 + sp * 1024, hoff + 14336, SC1);
      }
      SBAR();
    }
    h_load(0, 7, 14);
    STAMP(17 + 30 * ly);
    LAS char* rbw = sm + L_R + (w * 64 + (l & 31)) * RB_ROW + h * 16;
    auto g2_out = [&](int half, v2u xres) {
      const LAS char* rp = sm + L_R + orow * RB_ROW + oq * 16;
      f32x4 o4 = unpack4(xres) + *(const LAS float*)(sm + L_B2 + orow * 4);
      f32x4 p4 = *(const LAS f32x4*)rp;
#pragma unroll
      for (int ww = 1; ww < 8; ++ww) { const f32x4 v = *(const LAS f32x4*)(rp + ww * (64 * RB_ROW)); if (ww & 1) o4 += v; else p4 += v; }
      o4 += p4;
      u32 o0 = pack_bf16(o4[0], o4[1]), o1 = pack_bf16(o4[2], o4[3]);
      if (ly) {
        u16* op = out + (1024 * half + 32 * t + 4 * w) * DM + 64 * s + l;
        op[0] = (u16)(o0 & 0xffffu); op[DM] = (u16)(o0 >> 16); op[2 * DM] = (u16)(o1 & 0xffffu); op[3 * DM] = (u16)(o1 >> 16);
      } else {
        o0 = umin(o0, 0xfffffffeu); o1 = umin(o1, 0xfffffffeu);
        ST64((v2u{o0, o1}), (tid >> 3) * 4096 + (tid & 7) * 8, XC_OFF + (2 << 20) + s * (64 * 4096) + t * 64 + half * 2048, SC1);
        const float x0 = bflo(o0), x1 = bfhi(o0), x2 = bflo(o1), x3 = bfhi(o1);
        const float a0 = swap32_add(x0, x2), a1 = swap32_add(x0 * x0, x2 * x2), a2 = swap32_add(x1, x3), a3 = swap32_add(x1 * x1, x3 * x3);
        const float sv = dpp_add<0x128>(swap16_add(a0, a2)), qv = dpp_add<0x128>(swap16_add(a1, a3));
        if ((l & 8) == 0) *(LAS f32x2*)(sm + L_SP + ((half * 8 + w) * 32 + 4 * (l & 7) + (l >> 4)) * 8) = f32x2{sv, qv};
      }
    };
    auto pst_out = [&](int half) {
      float v = 0.f;
#pragma unroll
      for (int k = 0; k < 8; ++k) v += *(const LAS float*)(sm + L_SP + (half * 8 + k) * 256 + l * 4);
      ST64((v2u{__builtin_bit_cast(u32, v), (u32)opaque_v(1)}), (l >> 3) * 512 + (l & 7) * 8, PST_OFF + (t * 2 + half) * 4096 + s * 64, SC1);
    };
#define W2FRAG(k) (COMM ? wf[k] : (k) < W2A ? wa[(k) < W2A ? (k) : 0] : wf[(k) < W2A ? 0 : (k) - W2A])
    auto gemm2 = [&](int half) {
      f32x16 a0, a1;
#pragma unroll 1
      for (int sp = 0; sp < SPIN_CAP; ++sp) {
        a0 = f32x16{}; a1 = f32x16{};
        u32 mx = 0;
#pragma unroll
        for (int k = 0; k < 14; ++k) {
          a0 = mfma32(hf[k], W2FRAG(2 * k), a0); a1 = mfma32(hf[k], W2FRAG(2 * k + 1), a1);
          mx = umax(mx, max4(hf[k]));
        }
        if (__all(mx != 0xffffffffu)) break;
        STAMP(10 + 8 * half + 30 * ly);
        __builtin_amdgcn_s_sleep(2);
        CBAR();
        h_load(half, 0, 14);
      }
      STAMP(19 + 4 * half + 30 * ly);
      if (!COMM && half == 0) h_load(1, 0, 14);
      if (half == 0) STAMP(20 + 30 * ly);
#pragma unroll
      for (int q = 0; q < 4; ++q) {
        *(LAS f32x4*)(rbw + q * 32) = f32x4{a0[4 * q], a0[4 * q + 1], a0[4 * q + 2], a0[4 * q + 3]};
        *(LAS f32x4*)(rbw + 32 * RB_ROW + q * 32) = f32x4{a1[4 * q], a1[4 * q + 1], a1[4 * q + 2], a1[4 * q + 3]};
      }
      if constexpr (!COMM) {
        if (half == 1) {
          SBAR();
          if (ly == 0) {
            if (w == 0) {
#pragma unroll 1
              for (int sp = 0; sp < 3; ++sp) { CBAR(); fin_load(0); fin0 = fin_try(0); if (fin0) break; }
            }
            LOAD_W(0, 16);
          } else {
#pragma unroll
            for (int i = 0; i < 16; ++i) asm volatile("" : "=v"(wf[i]));
          }
        }
      }
    };
    gemm2(0);
    __syncthreads();
    if constexpr (COMM) h_load(1, 0, 14);
    STAMP(21 + 30 * ly);
    g2_out(0, xres0);
    STAMP(22 + 30 * ly);
    __syncthreads();
    if (COMM && ly == 0) pst_out(0);
    gemm2(1);
    __syncthreads();
    STAMP(24 + 30 * ly);
    g2_out(1, xres1);
    STAMP(25 + 30 * ly);
    if (ly == 0) {
      cin_load(0);
      __syncthreads();
      if constexpr (COMM) { pst_out(1); fin_wait(1); }
      else if (!fin0) { STAMP(28); fin_wait(0); }
      STAMP(26 + 30 * ly);
      xr0 = cin_ln(0);
      STAMP(27 + 30 * ly);
    }
  }
}

__global__ __launch_bounds__(512, 2) void k_main(const u16* __restrict__ dskip, const u16* __restrict__ b1,
    const u16* __restrict__ b2, char* __restrict__ ws, u16* __restrict__ out) {
  extern __shared__ __attribute__((aligned(16))) char smem[];
  LAS char* sm = (LAS char*)smem;
  const int b0 = blockIdx.x, w0 = __builtin_amdgcn_readfirstlane(threadIdx.x >> 6);
  const __amdgpu_buffer_rsrc_t rs = __builtin_amdgcn_make_buffer_rsrc(ws, 0, WS_BYTES, 0x00020000);
  STAMP(0);
  if (w0 == 7) main_body<true>(sm, rs, ws, out, dskip, b1, b2, b0, w0);
  else main_body<false>(sm, rs, ws, out, dskip, b1, b2, b0, w0);
  STAMP_FLUSH(1);
}

extern "C" void kernel_launch(void* const* d_in, const int* in_sizes, int n_in, void* d_out, int out_size, void* d_ws,
                              size_t ws_size, hipStream_t stream) {
  (void)hipFuncSetAttribute((const void*)k_main, hipFuncAttributeMaxDynamicSharedMemorySize, MAIN_LDS);
  k_prep<<<256, 512, 0, stream>>>((const u16*)d_in[0], (const u16*)d_in[1], (const u16*)d_in[2], (const u16*)d_in[3],
                                  (const u16*)d_in[4], (const u16*)d_in[5], (const u16*)d_in[8], (const u16*)d_in[10], (char*)d_ws, (const u16*)d_out);
  k_main<<<256, 512, MAIN_LDS, stream>>>((const u16*)d_in[6], (const u16*)d_in[9], (const u16*)d_in[11], (char*)d_ws, (u16*)d_out);
}
```

```cpp
#define WARM_SELF 0
#define WARM_IN 1
#include <hip/hip_runtime.h>
#include <hip/hip_bf16.h>

typedef unsigned int u32;
typedef unsigned short u16;
typedef __attribute__((ext_vector_type(4))) u32 v4u;
typedef __attribute__((ext_vector_type(2))) u32 v2u;
typedef __attribute__((ext_vector_type(4))) float f32x4;
typedef __attribute__((ext_vector_type(2))) float f32x2;
typedef __attribute__((ext_vector_type(16))) float f32x16;
typedef __attribute__((ext_vector_type(8))) __bf16 bf16x8;
typedef __attribute__((ext_vector_type(2))) __bf16 bf16x2;
typedef __attribute__((ext_vector_type(8))) _Float16 f16x8;
typedef __attribute__((ext_vector_type(2))) _Float16 f16x2;
typedef __attribute__((ext_vector_type(4))) short s16x4;
#define LAS __attribute__((address_space(3)))
#define DI __device__ __forceinline__
#define STAMP(i) __builtin_amdgcn_sched_barrier(0)
#define STAMP_FLUSH(kid)

constexpr int SL = 2048, DM = 512, NS = 128, FF = 1792;
constexpr int XC_OFF = 0;
constexpr int XM_OFF = 4 << 20;
constexpr int PRM_OFF = 8 << 20;
constexpr int KSC_OFF = PRM_OFF + DM * 2560;
constexpr int CS_OFF = KSC_OFF + 2048;
constexpr int IDX_OFF = CS_OFF + 16384;
constexpr int W1F_OFF = 11 << 20;
constexpr int W2F_OFF = 13 << 20;
constexpr int FST_OFF = 15 << 20;
constexpr int CTL_OFF = FST_OFF + 32768;
constexpr int CNT_OFF = CTL_OFF;
constexpr int HF_OFF = CNT_OFF + 8192;
constexpr int PST_OFF = HF_OFF + 8192;
constexpr int CTL_BYTES = 8192 + 8192 + 262144;
constexpr int H_OFF = 16 << 20;
constexpr int H_LAYER = 32 * 8 * 2 * 14 * 1024;
constexpr int WS_BYTES = 32 << 20;
constexpr int SC1 = 16;
constexpr int SPIN_CAP = 1 << 16;
#define K0_AUX 0

DI u32 umin(u32 a, u32 b) { return a < b ? a : b; }
DI u32 umax(u32 a, u32 b) { return a > b ? a : b; }
DI float bf2f(u32 h) { return __builtin_bit_cast(float, h << 16); }
DI float bflo(u32 p) { return __builtin_bit_cast(float, p << 16); }
DI float bfhi(u32 p) { return __builtin_bit_cast(float, p & 0xffff0000u); }
DI u32 pack_bf16(float a, float b) { bf16x2 v = {(__bf16)a, (__bf16)b}; return __builtin_bit_cast(u32, v); }
DI float bfr(float a) { return bflo(pack_bf16(a, a)); }
DI u32 pack_f16(float a, float b) { f16x2 v = {(_Float16)a, (_Float16)b}; return __builtin_bit_cast(u32, v); }
DI float dot2bf(u32 a, u32 b, float c) {
  return __builtin_amdgcn_fdot2_f32_bf16(__builtin_bit_cast(bf16x2, a), __builtin_bit_cast(bf16x2, b), c, false);
}
DI f32x16 mfma32(v4u a, v4u b, f32x16 c) {
  return __builtin_amdgcn_mfma_f32_32x32x16_bf16(__builtin_bit_cast(bf16x8, a), __builtin_bit_cast(bf16x8, b), c, 0, 0, 0);
}
DI f32x4 mfma16h(v4u a, v4u b, f32x4 c) {
  return __builtin_amdgcn_mfma_f32_16x16x32_f16(__builtin_bit_cast(f16x8, a), __builtin_bit_cast(f16x8, b), c, 0, 0, 0);
}
DI f32x4 mfma16b(v4u a, v4u b, f32x4 c) {
  return __builtin_amdgcn_mfma_f32_16x16x32_bf16(__builtin_bit_cast(bf16x8, a), __builtin_bit_cast(bf16x8, b), c, 0, 0, 0);
}
DI v4u tr_frag(const LAS char* p) {
  const s16x4 a = __builtin_amdgcn_ds_read_tr16_b64_v4i16((LAS s16x4*)p);
  const s16x4 c = __builtin_amdgcn_ds_read_tr16_b64_v4i16((LAS s16x4*)(p + 256));
  return __builtin_bit_cast(v4u, __builtin_shufflevector(a, c, 0, 1, 2, 3, 4, 5, 6, 7));
}
template <int CTRL> DI float dpp_add(float v) {
  const int o = __builtin_amdgcn_update_dpp(0, __builtin_bit_cast(int, v), CTRL, 0xf, 0xf, false);
  return v + __builtin_bit_cast(float, o);
}
DI float row_sum16(float v) { v = dpp_add<0x128>(v); v = dpp_add<0x124>(v); v = dpp_add<0x122>(v); return dpp_add<0x121>(v); }
DI float swap32_add(float a, float c) {
  const auto r = __builtin_amdgcn_permlane32_swap(__builtin_bit_cast(u32, a), __builtin_bit_cast(u32, c), false, false);
  const u32 r0 = r[0], r1 = r[1];
  return __builtin_bit_cast(float, r0) + __builtin_bit_cast(float, r1);
}
DI float swap16_add(float a, float c) {
  const auto r = __builtin_amdgcn_permlane16_swap(__builtin_bit_cast(u32, a), __builtin_bit_cast(u32, c), false, false);
  const u32 r0 = r[0], r1 = r[1];
  return __builtin_bit_cast(float, r0) + __builtin_bit_cast(float, r1);
}
DI float wave_sum(float v) { v = row_sum16(v); v = swap16_add(v, v); return swap32_add(v, v); }
DI void ln_stats(float s, float q, float& m, float& v, float& mb, float& rs) {
  m = s * (1.0f / DM);
  v = fmaxf(q * (1.0f / DM) - m * m, 0.f);
  const u32 pk = pack_bf16(m, __builtin_amdgcn_sqrtf(bfr(bfr(v) + 1e-5f)));
  mb = bflo(pk); rs = __builtin_amdgcn_rcpf(bfhi(pk));
}
DI u32 npz(float a) { return umin(__builtin_bit_cast(u32, a), 0xfffffffeu); }
#define LD128(vo, so, aux) __builtin_amdgcn_raw_buffer_load_b128(rs, (vo), (so), (aux))
#define LD64(vo, so, aux) __builtin_amdgcn_raw_buffer_load_b64(rs, (vo), (so), (aux))
#define LD32(vo, so, aux) __builtin_amdgcn_raw_buffer_load_b32(rs, (vo), (so), (aux))
#define ST128(v, vo, so, aux) __builtin_amdgcn_raw_buffer_store_b128((v), rs, (vo) + (so), 0, (aux))
#define ST64(v, vo, so, aux) __builtin_amdgcn_raw_buffer_store_b64((v), rs, (vo), (so), (aux))
#define ST32(v, vo, so, aux) __builtin_amdgcn_raw_buffer_store_b32((v), rs, (vo), (so), (aux))
#define DRAIN() asm volatile("s_waitcnt vmcnt(0)" ::: "memory")
#define CBAR() asm volatile("" ::: "memory")
#define SBAR() __builtin_amdgcn_sched_barrier(0)
DI int opaque_v(int x) { asm volatile("" : "+v"(x)); return x; }
template <typename T, int N> DI void undef_regs(T (&a)[N]) {
#pragma unroll
  for (int i = 0; i < N; ++i) asm volatile("" : "=v"(a[i]));
}
DI int opaque_s(int x) { asm volatile("" : "+s"(x)); return x; }

constexpr int KT_REL = 24576 + 16;
constexpr int KT_CH = 1312;
DI void cexp_p(float arl, float rh, float rl, float p, float& re, float& im) {
  const float e = __builtin_amdgcn_exp2f(arl * p);
  const float r = __builtin_fmaf(rl, p, __builtin_amdgcn_fractf(rh * p));
  re = e * __builtin_amdgcn_cosf(r);
  im = e * __builtin_amdgcn_sinf(r);
}
DI float k_scale(float bs, int sign) {
  const int ex = (int)((__builtin_bit_cast(u32, bs) >> 23) & 0xff) - 126;
  int e = 15 - ex; e = e > 100 ? 100 : (e < -100 ? -100 : e);
  return __builtin_bit_cast(float, (u32)(127 + sign * e) << 23);
}
__global__ __launch_bounds__(512, 2) void k_prep(const u16* __restrict__ x0, const u16* __restrict__ logA,
    const u16* __restrict__ Aim, const u16* __restrict__ Cre, const u16* __restrict__ Cim,
    const u16* __restrict__ logdt, const u16* __restrict__ w1, const u16* __restrict__ w2, char* __restrict__ ws,
    const u16* __restrict__ b1p, const u16* __restrict__ b2p, const u16* __restrict__ dsp) {
  __shared__ float bnd[4];
  __shared__ float csp[8][32];
  __shared__ __attribute__((aligned(16))) char wtile[8 * 2048];
  __shared__ u32 trt[64 * 33];
  const int b = blockIdx.x, tid = threadIdx.x, l = tid & 63, w = __builtin_amdgcn_readfirstlane(tid >> 6);
  const __amdgpu_buffer_rsrc_t rs = __builtin_amdgcn_make_buffer_rsrc(ws, 0, WS_BYTES, 0x00020000);
  STAMP(0);
  u32 wv = 0;
  {
    unsigned long long pc0;
    asm volatile("s_getpc_b64 %0" : "=s"(pc0));
    if (WARM_SELF && l < 34) wv = *(const __attribute__((address_space(1))) u32*)((pc0 & ~127ull) + (w & 1) * 64 + l * 128);
  }
  u32 rla = 0, rai = 0, rcr = 0, rci = 0, rdt = 0;
  if (w < 4) {
    const int o = 2 * b * NS + tid;
    rla = logA[o]; rai = Aim[o]; rcr = Cre[o]; rci = Cim[o]; rdt = logdt[2 * b + (w >> 1)];
  }
  const v4u xr = *(const v4u*)(x0 + (8 * b + w) * DM + 8 * l);
  const v4u xt = *(const v4u*)(x0 + (64 * (b >> 3) + (tid >> 3)) * DM + 64 * (b & 7) + 8 * (tid & 7));
  v4u wr0 = {0u, 0u, 0u, 0u}, wr1 = wr0;
  const int fr = (b < 112 ? b : b - 112) * 16 + 2 * w;
  if (b < 112) {
    const u16* p = w1 + (16 * (fr & 31) + (l >> 2)) * FF + 32 * (fr >> 5) + 8 * (l & 3);
    wr0 = *(const v4u*)p; wr1 = *(const v4u*)(p + 16 * FF);
  } else if (b < 224) {
    const int r1 = fr >> 1, ks2 = r1 % 14, r2 = r1 / 14;
    const u16* p = w2 + (224 * (r2 & 7) + 16 * ks2 + (l >> 2)) * DM + 64 * (r2 >> 3) + 8 * (l & 3);
    wr0 = *(const v4u*)p; wr1 = *(const v4u*)(p + 32);
  }
  {
    unsigned long long pc;
    asm volatile("s_getpc_b64 s[90:91]\n\ts_add_u32 s90, s90, _Z6k_mainPKtS0_S0_PcPt@rel32@lo+4\n\ts_addc_u32 s91, s91, _Z6k_mainPKtS0_S0_PcPt@rel32@hi+12\n\ts_mov_b64 %0, s[90:91]"
                 : "=s"(pc) :: "s90", "s91", "scc");
    if (w == 7) {
      const v4u c0 = *(const __attribute__((address_space(1))) v4u*)(pc + (b >> 3) * 1024 + l * 16);
      wv ^= c0[0] ^ c0[3];
      if ((b >> 3) == 0) { const v4u c1 = *(const __attribute__((address_space(1))) v4u*)(pc + 32768 + l * 16); wv ^= c1[1]; }
    }
    if (WARM_IN && w == 6 && l < 44) wv ^= *(const u32*)(l < 28 ? b1p + l * 64 : (l < 36 ? b2p + (l - 28) * 64 : dsp + (l - 36) * 64));
  }
  ST128((v4u{~0u, ~0u, ~0u, ~0u}), tid * 16, XM_OFF + b * 8192, K0_AUX);
  if (tid >= 128 && tid < 132) ST128((v4u{~0u, ~0u, ~0u, ~0u}), (tid - 128) * 16, FST_OFF + 16384 + b * 64, K0_AUX);
  if (tid < CTL_BYTES / 16 / 256) ST128((v4u{0u, 0u, 0u, 0u}), tid * 16, CTL_OFF + b * (CTL_BYTES / 256), K0_AUX);
  if (b == 255) {
    const int c = tid >> 8, x = tid & 255, col = x >> 4, row = x & 15, i16 = l & 15;
    const int p0 = 128 * (row >> 3) + (row & 7) + 8 * col;
    const int p1 = (row < 8 ? 256 + 128 * (row >> 2) + 2 * (row & 3) : 512 + 128 * ((row - 8) >> 1) + 4 * (row & 1)) + 8 * col;
    const int c1 = row < 8 ? 128 + (p1 >> 1) : 256 + (p1 >> 2);
    const int c2 = row < 8 ? 512 + 16 * row + col : (x == 8 ? 640 : 641);
    const int kb = KT_REL + c * KT_CH;
    u32 gx[8];
#pragma unroll
    for (int j = 0; j < 8; ++j) {
      const int la = 8 * x + 7 - 2 * j, lb = la - 1;
      const u32 hb = pack_bf16((float)la, (float)lb);
      const int ia = la < 128 ? la : (int)(hb & 0xffffu) - 17024, ib = lb < 128 ? lb : (int)(hb >> 16) - 17024;
      gx[j] = (u32)(kb + 2 * ia) | ((u32)(kb + 2 * ib) << 16);
    }
    const f32x4 pp = {(float)(8 * i16), (float)(128 * (i16 >> 3) + (i16 & 7)),
                      (float)(i16 < 8 ? 256 + 128 * (i16 >> 2) + 2 * (i16 & 3) : 512 + 128 * ((i16 - 8) >> 1) + 4 * (i16 & 1)),
                      (float)(i16 < 8 ? 1024 + 128 * i16 : 2048)};
    ST128(__builtin_bit_cast(v4u, pp), tid * 16, IDX_OFF, K0_AUX);
    ST128((v4u{(u32)(kb + 2 * p0), (u32)(kb + 2 * c1), (u32)(kb + 2 * c2), 0u}), tid * 16, IDX_OFF + 8192, K0_AUX);
    ST128((v4u{gx[0], gx[1], gx[2], gx[3]}), tid * 16, IDX_OFF + 16384, K0_AUX);
    ST128((v4u{gx[4], gx[5], gx[6], gx[7]}), tid * 16, IDX_OFF + 24576, K0_AUX);
  }
  STAMP(1);
  float arl = 0.f, rh = 0.f, rl = 0.f, cr = 0.f, ci = 0.f;
  if (w < 4) {
    const float dt = bfr(expf(bf2f(rdt)));
    arl = -bfr(expf(bf2f(rla))) * dt * 1.4426950408889634f;
    double t = (double)(bf2f(rai) * dt) * 0.15915494309189533577;
    t -= __builtin_rint(t);
    rh = __builtin_bit_cast(float, __builtin_bit_cast(u32, (float)t) & 0xfffff000u);
    rl = (float)(t - (double)rh);
    cr = bf2f(rcr) * dt * 2.0f; ci = bf2f(rci) * dt * 2.0f;
    const float bs = wave_sum(fabsf(cr) + fabsf(ci));
    if (l == 0) bnd[w] = bs;
  }
  STAMP(2);
  {
    float s = 0.f, q = 0.f;
#pragma unroll
    for (int j = 0; j < 4; ++j) {
      const u32 p = xr[j];
      s = dot2bf(p, 0x3f803f80u, s); q = dot2bf(p, p, q);
      trt[(tid >> 3) * 33 + 4 * (tid & 7) + j] = xt[j];
    }
    s = wave_sum(s); q = wave_sum(q);
    float m, v, mb, rcs;
    ln_stats(s, q, m, v, mb, rcs);
    if (l < 2) ST32(__builtin_bit_cast(u32, l ? rcs : mb), l * 16, FST_OFF + (2 * b + (w >> 2)) * 32 + (w & 3) * 4, K0_AUX);
  }
  if (b < 224) {
    LAS char* wl = (LAS char*)wtile + w * 2048;
    *(LAS v4u*)(wl + l * 16) = wr0; *(LAS v4u*)(wl + 1024 + l * 16) = wr1;
    const int hs = b < 112 ? 512 : 256, ts = b < 112 ? 256 : 512;
    const LAS char* rp = wl + (l >> 5) * hs + ((l & 15) >> 2) * 64 + 32 * ((l >> 4) & 1) + 8 * (l & 3);
    const s16x4 t0 = __builtin_amdgcn_ds_read_tr16_b64_v4i16((LAS s16x4*)rp), t1 = __builtin_amdgcn_ds_read_tr16_b64_v4i16((LAS s16x4*)(rp + ts));
    const s16x4 t2 = __builtin_amdgcn_ds_read_tr16_b64_v4i16((LAS s16x4*)(rp + 1024)), t3 = __builtin_amdgcn_ds_read_tr16_b64_v4i16((LAS s16x4*)(rp + 1024 + ts));
    const v4u o1 = __builtin_bit_cast(v4u, __builtin_shufflevector(t0, t1, 0, 1, 2, 3, 4, 5, 6, 7));
    const v4u o2 = __builtin_bit_cast(v4u, __builtin_shufflevector(t2, t3, 0, 1, 2, 3, 4, 5, 6, 7));
    float cs = 0.f;
#pragma unroll
    for (int j = 0; j < 4; ++j) { const u32 p1 = o1[j], p2 = o2[j]; cs = dot2bf(p1, 0x3f803f80u, cs); cs = dot2bf(p2, 0x3f803f80u, cs); }
    ST128(o1, l * 16, (b < 112 ? W1F_OFF : W2F_OFF) + fr * 1024, K0_AUX);
    ST128(o2, l * 16 + 1024, (b < 112 ? W1F_OFF : W2F_OFF) + fr * 1024, K0_AUX);
    cs = swap32_add(cs, cs);
    if (l < 32) csp[w][l] = cs;
  }
  STAMP(5);
  __syncthreads();
  STAMP(6);
  {
    if (w < 4) {
      const float bs = bnd[w & 2] + bnd[(w & 2) + 1], sc = k_scale(bs, 1);
      const int vo = (tid >> 2) * 80 + (tid & 3) * 4;
      ST32(__builtin_bit_cast(u32, arl), vo, PRM_OFF + b * 5120, K0_AUX);
      ST32(__builtin_bit_cast(u32, rh), vo + 16, PRM_OFF + b * 5120, K0_AUX);
      ST32(__builtin_bit_cast(u32, rl), vo + 32, PRM_OFF + b * 5120, K0_AUX);
      ST32(__builtin_bit_cast(u32, cr * sc), vo + 48, PRM_OFF + b * 5120, K0_AUX);
      ST32(__builtin_bit_cast(u32, ci * sc), vo + 64, PRM_OFF + b * 5120, K0_AUX);
      if ((tid & 127) == 0) ST32(__builtin_bit_cast(u32, k_scale(bs, -1)), (tid >> 7) * 4, KSC_OFF + 8 * b, K0_AUX);
    }
    if (b < 112 && tid < 32) {
      const float cs = ((csp[0][tid] + csp[1][tid]) + (csp[2][tid] + csp[3][tid])) + ((csp[4][tid] + csp[5][tid]) + (csp[6][tid] + csp[7][tid]));
      ST32(__builtin_bit_cast(u32, cs), tid * 4, CS_OFF + (b & 1) * (FF * 4) + (b >> 1) * 128, K0_AUX);
    }
    const int cq = tid >> 3, oct = tid & 7;
    const u16* t16 = (const u16*)trt;
    v4u o;
#pragma unroll
    for (int j = 0; j < 4; ++j)
      o[j] = (u32)t16[(8 * oct + 2 * j) * 66 + cq] | ((u32)t16[(8 * oct + 2 * j + 1) * 66 + cq] << 16);
    ST128(o, cq * 4096 + oct * 16, XC_OFF + (b & 7) * (64 * 4096) + (b >> 3) * 128, K0_AUX);
  }
  asm volatile("" :: "v"(wv));
  STAMP(8);
  STAMP_FLUSH(0);
}

constexpr int KC_CH = 8 * 260 * 16;
constexpr int L_U = 2 * KC_CH;
constexpr int U_CH = 4672;
constexpr int L_R = L_U + 2 * U_CH;
constexpr int L_XB = L_R + 32768;
constexpr int L_PB = L_XB + 256;
constexpr int L_KT = L_R + KT_REL;
constexpr int RB_ROW = 144;
constexpr int L_S = L_R + 8 * 64 * RB_ROW;
constexpr int L_B1 = L_S;
constexpr int L_CS = L_B1 + 896;
constexpr int L_SP = L_CS + 896;
constexpr int L_AC = L_SP + 4096;
constexpr int L_B2 = L_AC + 64;
constexpr int L_DK = L_B2 + 256;
constexpr int L_AB = L_DK + 16;
constexpr int L_END = L_AB + 512;
constexpr int MAIN_LDS = L_END;
#define MAPV 1
#define MAP_T(b) ((((b) >> 1) & 3) * 8 + ((b) >> 5))
#define MAP_S(b) (((b) & 1) * 4 + (((b) >> 3) & 3))
#define W1_SPLIT 0
constexpr int W2A = 12;

DI f32x2 gelu2(f32x2 x) {
  const f32x2 t = x * (x * x * -0.10294324f + -2.3022082f);
  f32x2 e; e.x = __builtin_amdgcn_exp2f(t.x); e.y = __builtin_amdgcn_exp2f(t.y);
  e = e + 1.0f;
  f32x2 r; r.x = __builtin_amdgcn_rcpf(e.x); r.y = __builtin_amdgcn_rcpf(e.y);
  return x * r;
}
DI u32 max4(v4u a) { return umax(umax(a[0], a[1]), umax(a[2], a[3])); }
DI f32x4 unpack4(v2u p) { const u32 a = p[0], c = p[1]; return f32x4{bflo(a), bfhi(a), bflo(c), bfhi(c)}; }
DI f32x4 conv_ln(LAS char* dst, v2u xp, v4u mm, v4u rr, float dk) {
  const f32x4 x = unpack4(xp);
  const f32x4 u = (x - __builtin_bit_cast(f32x4, mm)) * __builtin_bit_cast(f32x4, rr);
  *(LAS v2u*)dst = v2u{pack_f16(u[0], u[1]), pack_f16(u[2], u[3])};
  return x + u * dk;
}

template <bool COMM>
DI void main_body(LAS char* sm, const __amdgpu_buffer_rsrc_t rs, char* ws, u16* out, const u16* dskip, const u16* b1,
                  const u16* b2, int b0, int w0) {
  v4u wf[32], wa[W2A];
  f32x4 xr0, xr1;
#define LOAD_W(lo, hi) do { _Pragma("unroll") for (int ks = (lo); ks < (hi); ++ks) { \
      if constexpr (!COMM) wf[ks] = LD128(l16 + (ks & 3) * 1024, W1F_OFF + (7 * s + w) * 32768 + (ks >> 2) * 4096, 0); \
      else if (ks < 28) wf[ks] = LD128(l16 + (ks & 3) * 1024, W2F_OFF + (s * 8 + 7) * 28672 + (ks >> 2) * 4096, 0); } \
      SBAR(); } while (0)
#define CW(k) do { if (!COMM || ly == 0) LOAD_W(k, (k) + 1); } while (0)
  {
    const int b = opaque_s(b0), w = opaque_s(w0), tid = opaque_v(threadIdx.x), l = tid & 63, l16 = l * 16, s = MAP_S(b);
    const int cc = (tid >> 3) & 1, q8 = (tid >> 4) * 8 + (tid & 7);
    const int kk = l >> 4, i16 = l & 15;
    {
      const v4u pr = LD128(tid * 16, PRM_OFF + b * 5120, 0);
      if (tid < 16) *(LAS v4u*)(sm + (tid >> 3) * KC_CH + (tid & 7) * 4160) = v4u{0u, 0u, 0u, 0u};
      if (tid >= 64 && tid < 128) *(LAS v4u*)(sm + L_U + ((tid >> 5) & 1) * U_CH + (tid & 31) * 16) = v4u{0u, 0u, 0u, 0u};
      if (tid >= 128 && tid < 130) *(LAS v4u*)(sm + L_KT - 16 + (tid - 128) * KT_CH) = v4u{0u, 0u, 0u, 0u};
      *(LAS v4u*)(sm + L_XB + tid * 16) = pr;
    }
    const f32x4 pp4 = __builtin_bit_cast(f32x4, LD128(l16, IDX_OFF, 0));
    {
      SBAR();
      const u32 on = (u32)opaque_v(-1);
      const int t = MAP_T(b);
      ST64((v2u{on, on}), (tid >> 4) * 32768 + (tid & 15) * 8, XM_OFF + (2 << 20) + 128 * b, SC1);
      ST64((v2u{on, on}), (tid >> 4) * 32768 + (tid & 15) * 8, XM_OFF + (3 << 20) + 128 * b, SC1);
      ST64((v2u{on, on}), (tid >> 3) * 4096 + (tid & 7) * 8, XC_OFF + (2 << 20) + s * (64 * 4096) + t * 64, SC1);
      ST64((v2u{on, on}), (tid >> 3) * 4096 + (tid & 7) * 8, XC_OFF + (2 << 20) + s * (64 * 4096) + t * 64 + 2048, SC1);
      SBAR();
    }
    v4u tix, gx0, gx1, m0, r0, m1, r1;
    v2u xa, xb;
    float dk, ksc, bias2, bb1, cs0, cs1;
    __syncthreads();
    const int c = tid >> 8, x = tid & 255;
    const float pB = pp4[0], pA0 = pp4[1], pA1 = pp4[2], pA2 = pp4[3];
    f32x4 acc0 = {0.f, 0.f, 0.f, 0.f}, acc1 = acc0, acc2 = acc0;
    STAMP(1);
#pragma unroll
    for (int g = 0; g < 2; ++g) {
      v4u fa0, fa1, fa2, fbh, fbl;
#pragma unroll
      for (int j = 0; j < 4; ++j) {
        const LAS float* pq = (const LAS float*)(sm + L_XB + w * 640 + g * 320 + kk * 80) + j;
        const float arl = pq[0], rh = pq[4], rl = pq[8], cr = pq[12], ci = pq[16];
        float br, bi, er, ei;
        cexp_p(arl, rh, rl, pB, br, bi);
        bi = -bi;
        const u32 hi = pack_f16(br, bi);
        const f16x2 hv = __builtin_bit_cast(f16x2, hi);
        fbh[j] = hi;
        fbl[j] = pack_f16(br - (float)hv.x, bi - (float)hv.y);
        cexp_p(arl, rh, rl, pA0, er, ei);
        fa0[j] = pack_f16(cr * er - ci * ei, cr * ei + ci * er);
        cexp_p(arl, rh, rl, pA1, er, ei);
        fa1[j] = pack_f16(cr * er - ci * ei, cr * ei + ci * er);
        cexp_p(arl, rh, rl, pA2, er, ei);
        fa2[j] = pack_f16(cr * er - ci * ei, cr * ei + ci * er);
        SBAR();
        LOAD_W(8 * g + 2 * j, 8 * g + 2 * j + 2);
        if (g == 0 && j == 0) { xa = LD64(cc * 4096 + q8 * 8, XC_OFF + b * 8192, 0); xb = LD64(cc * 4096 + q8 * 8 + 2048, XC_OFF + b * 8192, 0); }
        if (g == 0 && j == 1) { m0 = LD128(q8 * 32, FST_OFF, 0); r0 = LD128(q8 * 32 + 16, FST_OFF, 0); }
        if (g == 0 && j == 2) { m1 = LD128(q8 * 32, FST_OFF + 8192, 0); r1 = LD128(q8 * 32 + 16, FST_OFF + 8192, 0); }
        if (g == 0 && j == 3) { dk = bf2f(dskip[2 * b + cc]); ksc = __builtin_bit_cast(float, LD32(cc * 4, KSC_OFF + 8 * b, 0)); }
        if (g == 1 && j == 0) { bias2 = bf2f(b2[64 * s + l]); bb1 = bf2f(b1[224 * s + (tid < 224 ? tid : 0)]); }
        if (g == 1 && j == 1) { cs0 = __builtin_bit_cast(float, LD32(tid * 4, CS_OFF + s * 896, 0)); cs1 = __builtin_bit_cast(float, LD32(tid * 4, CS_OFF + FF * 4 + s * 896, 0)); }
        if (g == 1 && j == 2) { tix = LD128(tid * 16, IDX_OFF + 8192, 0); }
        if (g == 1 && j == 3) { gx0 = LD128(tid * 16, IDX_OFF + 16384, 0); gx1 = LD128(tid * 16, IDX_OFF + 24576, 0); }
        SBAR();
      }
      acc0 = mfma16h(fa0, fbh, acc0); acc1 = mfma16h(fa1, fbh, acc1); acc2 = mfma16h(fa2, fbh, acc2);
      acc0 = mfma16h(fa0, fbl, acc0); acc1 = mfma16h(fa1, fbl, acc1); acc2 = mfma16h(fa2, fbl, acc2);
    }
    *(LAS f32x4*)(sm + L_R + w * 3072 + i16 * 64 + kk * 16) = acc0;
    *(LAS f32x4*)(sm + L_R + w * 3072 + 1024 + i16 * 64 + kk * 16) = acc1;
    *(LAS f32x4*)(sm + L_R + w * 3072 + 2048 + i16 * 64 + kk * 16) = acc2;
    STAMP(60);
    xr0 = conv_ln(sm + L_U + cc * U_CH + 512 + q8 * 8, xa, m0, r0, dk);
    xr1 = conv_ln(sm + L_U + cc * U_CH + 2560 + q8 * 8, xb, m1, r1, dk);
    if (tid < 224) { *(LAS float*)(sm + L_B1 + tid * 4) = bb1; *(LAS float*)(sm + L_CS + tid * 4) = cs0 + cs1; }
    if (tid < 64) *(LAS float*)(sm + L_B2 + tid * 4) = bias2;
    if ((tid & ~8) == 0) *(LAS f32x2*)(sm + L_DK + cc * 8) = f32x2{dk, ksc};
    STAMP(61);
    __syncthreads();
    {
      const LAS float* pp = (const LAS float*)(sm + L_R + c * 12288) + x;
      *(LAS _Float16*)(sm + L_R + tix[0]) = (_Float16)((pp[0] + pp[768]) + (pp[1536] + pp[2304]));
      *(LAS _Float16*)(sm + L_R + tix[1]) = (_Float16)((pp[256] + pp[1024]) + (pp[1792] + pp[2560]));
      *(LAS _Float16*)(sm + L_R + tix[2]) = (_Float16)((pp[512] + pp[1280]) + (pp[2048] + pp[2816]));
    }
    __syncthreads();
    STAMP(62);
    {
      u32 wd[8];
#pragma unroll
      for (int j = 0; j < 8; ++j) {
        const u32 g = j < 4 ? gx0[j & 3] : gx1[j & 3];
        wd[j] = (u32)*(const LAS u16*)(sm + L_R + (g & 0xffffu)) | ((u32)*(const LAS u16*)(sm + L_R + (g >> 16)) << 16);
      }
#pragma unroll
      for (int cp = 0; cp < 8; ++cp) {
        const int sg = 7 - cp;
        v4u o;
#pragma unroll
        for (int k = 0; k < 4; ++k)
          o[k] = (sg & 1) ? __builtin_amdgcn_alignbit(wd[(sg + 1) / 2 + k], wd[(sg - 1) / 2 + k], 16) : wd[sg / 2 + k];
        *(LAS v4u*)(sm + c * KC_CH + cp * 4160 + (x + 1) * 16) = o;
      }
    }
    if constexpr (COMM) {
#pragma unroll
      for (int i = 16; i < 28; ++i) asm volatile("" : "=v"(wf[i]));
    }
  }

#pragma unroll 1
  for (int ly = 0; ly < 2; ++ly) {
    const int b = opaque_s(b0), w = opaque_s(w0);
    const int t = MAP_T(b), s = MAP_S(b);
    int tid = opaque_v(threadIdx.x), l = tid & 63, l16 = l * 16;
    const int xmo = XM_OFF + ly * (2 << 20) + t * 32768 + w * 1024;
    const int w2off = W2F_OFF + ((s * 8 + w) * 28) * 1024;
    const int hoff = H_OFF + ly * H_LAYER + (t * 8 + s) * (28 * 1024) + (2 * w) * 1024;
    v4u xs[4];
    auto x_load = [&](int half) {
#pragma unroll
      for (int k = 0; k < 4; ++k) xs[k] = LD128(l16, xmo + half * (1 << 20) + k * 8192, SC1);
    };
    auto x_ok = [&]() { return __all(umax(umax(max4(xs[0]), max4(xs[1])), umax(max4(xs[2]), max4(xs[3]))) != 0xffffffffu); };
    auto x_stage = [&](int half) {
      if (!x_ok()) {
        int sp = 0;
#pragma unroll 1
        do {
          STAMP(56 + half + 2 * ly);
          __builtin_amdgcn_s_sleep(4);
          CBAR();
          x_load(half);
        } while (!x_ok() && ++sp < SPIN_CAP);
      }
#pragma unroll
      for (int k = 0; k < 4; ++k) *(LAS v4u*)(sm + L_R + half * 32768 + k * 8192 + tid * 16) = xs[k];
    };
    v2u pg;
    bool fin0 = true;
    auto fin_load = [&](int hf) { pg = LD64(l * 8, PST_OFF + ((t * 2 + hf) * 8 + s) * 512, SC1); };
    auto fin_try = [&](int hf) -> bool {
      if (!__all(pg[1] == 1u)) return false;
      const u32 e0 = pg[0];
      float v = __builtin_bit_cast(float, e0);
      v = swap32_add(v, v); v = swap16_add(v, v); v = dpp_add<0x128>(v);
      const float o = __builtin_bit_cast(float, __builtin_amdgcn_update_dpp(0, __builtin_bit_cast(int, v), 0xb1, 0xf, 0xf, false));
      float mm, vr, mb, rcs;
      ln_stats((l & 1) ? o : v, (l & 1) ? v : o, mm, vr, mb, rcs);
      if (l < 8) ST32(npz((l & 1) ? rcs : mb), (l & 1) * 16 + (l >> 1) * 4, FST_OFF + 16384 + hf * 8192 + (8 * t + s) * 32, SC1);
      return true;
    };
    auto fin_wait = [&](int hf) {
#pragma unroll 1
      for (int sp = 0; sp < SPIN_CAP; ++sp) {
        CBAR();
        fin_load(hf);
        if (fin_try(hf)) break;
        __builtin_amdgcn_s_sleep(1);
      }
    };
    v2u ix; v4u im, ir;
    auto cin_load = [&](int hf) {
      const int cc = (tid >> 3) & 1, q8 = (tid >> 4) * 8 + (tid & 7);
      ix = LD64(cc * 4096 + q8 * 8, XC_OFF + (2 << 20) + b * 8192 + hf * 2048, SC1);
      im = LD128(q8 * 32, FST_OFF + 16384 + hf * 8192, SC1); ir = LD128(q8 * 32 + 16, FST_OFF + 16384 + hf * 8192, SC1);
    };
    auto cin_ok = [&]() { return __all(umax(umax(ix[0], ix[1]), umax(max4(im), max4(ir))) != 0xffffffffu); };
    auto cin_ln = [&](int hf) -> f32x4 {
      if (!cin_ok()) {
        int sp = 0;
#pragma unroll 1
        do {
          STAMP(29 + 2 * hf);
          __builtin_amdgcn_s_sleep(4);
          CBAR();
          cin_load(hf);
        } while (!cin_ok() && ++sp < SPIN_CAP);
      }
      const int cc = (tid >> 3) & 1, q8 = (tid >> 4) * 8 + (tid & 7);
      return conv_ln(sm + L_U + cc * U_CH + 512 + hf * 2048 + q8 * 8, ix, im, ir, *(const LAS float*)(sm + L_DK + cc * 8));
    };
    CW(16); CW(17); CW(18); CW(19);
    STAMP(2 + 30 * ly);
    __syncthreads();
    STAMP(3 + 30 * ly);
    {
      const int c16 = l & 15, g4 = l >> 4;
      const LAS char* pa = sm + (w >> 2) * KC_CH + (c16 & 7) * 4160 + (3 + (c16 >> 3) - g4) * 16 + (w & 3) * 128;
      const LAS char* pb = sm + L_U + (w >> 2) * U_CH + (30 + 2 * c16 + g4) * 16 - (w & 3) * 128;
      LAS char* pp = sm + L_R + w * 4096 + (w >> 2) * 128 + ((4 * c16 + g4) ^ ((c16 >> 1) & 3)) * 16;
      const int xmv = (tid >> 4) * 32768 + (tid & 15) * 8;
      const LAS char* pr = sm + L_R + ((tid >> 3) & 1) * 16512 + ((((tid >> 4) << 3) | (tid & 7)) ^ ((tid >> 4) & 3)) * 16;
      auto conv_out = [&](int half, f32x4 xr) {
        const LAS char* p = pr + (half ? L_PB - L_R : 0);
        const float ksc = *(const LAS float*)(sm + L_DK + ((tid >> 3) & 1) * 8 + 4);
        const f32x4 y = (*(const LAS f32x4*)p + *(const LAS f32x4*)(p + 4096)) + (*(const LAS f32x4*)(p + 8192) + *(const LAS f32x4*)(p + 12288));
        const f32x4 o = y * ksc + xr;
        ST64((v2u{umin(pack_bf16(o[0], o[1]), 0xfffffffeu), umin(pack_bf16(o[2], o[3]), 0xfffffffeu)}), xmv,
             XM_OFF + ly * (2 << 20) + half * (1 << 20) + 128 * b, SC1);
      };
      {
        f32x4 acc[4];
#pragma unroll
        for (int i = 0; i < 4; ++i) acc[i] = f32x4{0.f, 0.f, 0.f, 0.f};
        v4u fa[2][4], fb[2][4];
#pragma unroll
        for (int tau = 0; tau < 2; ++tau)
#pragma unroll
          for (int i = 0; i < 4; ++i) { fa[tau][i] = *(const LAS v4u*)(pa + i * 512 + tau * 64); fb[tau][i] = *(const LAS v4u*)(pb + i * 512 - tau * 64); }
#pragma unroll
        for (int tau = 0; tau < 2; ++tau)
#pragma unroll
          for (int sx = 0; sx < 4; ++sx) {
#pragma unroll
            for (int e = 0; e + sx < 4; ++e) acc[sx + e] = mfma16h(fa[tau][sx], fb[tau][e], acc[sx + e]);
            if (sx == 0) { SBAR(); CW(20 + tau); }
          }
#pragma unroll
        for (int i = 0; i < 4; ++i) *(LAS f32x4*)(pp + i * 1024) = acc[i];
      }
      v4u fa[8], fb[4];
#pragma unroll
      for (int i = 0; i < 4; ++i) { fb[i] = *(const LAS v4u*)(pb + i * 512); fa[7 - i] = *(const LAS v4u*)(pa + (7 - i) * 512); }
#pragma unroll
      for (int i = 1; i < 4; ++i) fa[i] = *(const LAS v4u*)(pa + i * 512);
      SBAR();
      CW(22); CW(23);
      STAMP(4 + 30 * ly);
      __syncthreads();
      conv_out(0, xr0);
      STAMP(5 + 30 * ly);
      {
        f32x4 acc[4];
#pragma unroll
        for (int i = 0; i < 4; ++i) acc[i] = f32x4{0.f, 0.f, 0.f, 0.f};
#pragma unroll
        for (int e = 0; e < 4; ++e) {
#pragma unroll
          for (int i = 0; i < 4; ++i) acc[i] = mfma16h(fa[4 + i - e], fb[e], acc[i]);
          fb[e] = *(const LAS v4u*)(pb + e * 512 - 64); fa[7 - e] = *(const LAS v4u*)(pa + (7 - e) * 512 + 64);
          SBAR();
          if (e & 1) CW(24 + (e >> 1));
        }
#pragma unroll
        for (int i = 3; i >= 1; --i) fa[i] = *(const LAS v4u*)(pa + i * 512 + 64);
        if (ly) cin_load(1);
        else { asm volatile("" : "=v"(ix), "=v"(im), "=v"(ir)); }
        SBAR();
#pragma unroll
        for (int e = 0; e < 4; ++e) {
#pragma unroll
          for (int i = 0; i < 4; ++i) acc[i] = mfma16h(fa[4 + i - e], fb[e], acc[i]);
          SBAR();
          if (e & 1) CW(26 + (e >> 1));
        }
#pragma unroll
        for (int i = 0; i < 4; ++i) { fa[i] = *(const LAS v4u*)(pa + i * 512); fa[4 + i] = *(const LAS v4u*)(pa + i * 512 + 64); }
        STAMP(8 + 30 * ly);
        if (ly) xr1 = cin_ln(1);
        SBAR();
        CW(28); CW(29);
        STAMP(9 + 30 * ly);
        __syncthreads();
        v4u fc[8];
#pragma unroll
        for (int i = 0; i < 4; ++i) { fc[i] = *(const LAS v4u*)(pb + (4 + i) * 512); fc[4 + i] = *(const LAS v4u*)(pb + (4 + i) * 512 - 64); }
#pragma unroll
        for (int tau = 0; tau < 2; ++tau)
#pragma unroll
          for (int e = 0; e < 4; ++e) {
#pragma unroll
            for (int i = e; i < 4; ++i) acc[i] = mfma16h(fa[4 * tau + i - e], fc[4 * tau + e], acc[i]);
            if (e == 0) { SBAR(); CW(30 + tau); }
          }
#pragma unroll
        for (int i = 0; i < 4; ++i) *(LAS f32x4*)(pp + (L_PB - L_R) + i * 1024) = acc[i];
      }
      STAMP(6 + 30 * ly);
      __syncthreads();
      x_load(0);
      SBAR();
      conv_out(1, xr1);
      STAMP(7 + 30 * ly);
      x_stage(0);
      SBAR();
      if constexpr (!COMM) {
        const u32 on = (u32)opaque_v(-1);
#pragma unroll
        for (int k = 0; k < 4; ++k) ST128((v4u{on, on, on, on}), l16 + (k & 1) * 1024, hoff + (k >> 1) * 14336, SC1);
        SBAR();
      }
    }
    STAMP(11 + 30 * ly);
    __syncthreads();
    tid = opaque_v(threadIdx.x); l = tid & 63; l16 = l * 16;
    const int h = l >> 5;
    const LAS char* xb = sm + L_R + (8 * h + ((l & 15) >> 2)) * 64 + 32 * ((l >> 4) & 1) + 8 * (l & 3);
    auto stats = [&](int half) {
      const LAS char* xq = sm + L_R + half * 32768 + (8 * (l >> 4) + ((l & 15) >> 2)) * 64 + 8 * (l & 3);
      const u32 one = (u32)opaque_v(0x3f803f80);
      const v4u ones = {one, one, one, one};
      f32x4 s0 = {0.f, 0.f, 0.f, 0.f}, q0 = s0, s1 = s0, q1 = s0;
      v4u f0[4], f1[4];
#pragma unroll
      for (int k = 0; k < 4; ++k) { f0[k] = tr_frag(xq + k * 2048); f1[k] = tr_frag(xq + k * 2048 + 32); }
#pragma unroll
      for (int k = 0; k < 16; ++k) {
        s0 = mfma16b(ones, f0[k & 3], s0); q0 = mfma16b(f0[k & 3], f0[k & 3], q0);
        s1 = mfma16b(ones, f1[k & 3], s1); q1 = mfma16b(f1[k & 3], f1[k & 3], q1);
        if (k + 4 < 16) { f0[k & 3] = tr_frag(xq + (k + 4) * 2048); f1[k & 3] = tr_frag(xq + (k + 4) * 2048 + 32); }
        if (half == 0 && k == 9) x_load(1);
        SBAR();
      }
      const float d0 = (l & 2) ? ((l & 1) ? q0[3] : q0[2]) : ((l & 1) ? q0[1] : q0[0]);
      const float d1 = (l & 2) ? ((l & 1) ? q1[3] : q1[2]) : ((l & 1) ? q1[1] : q1[0]);
#pragma unroll
      for (int g = 0; g < 2; ++g) {
        float m, v, mb, r1;
        ln_stats(g ? s1[0] : s0[0], g ? d1 : d0, m, v, mb, r1);
        const u32 p2 = pack_bf16((m - mb) * r1, v * r1 * r1);
        const float r2 = __builtin_amdgcn_rcpf(bfr(__builtin_amdgcn_sqrtf(bfr(bfhi(p2) + 1e-5f))));
        if ((l >> 4) == ((l >> 2) & 3)) *(LAS f32x2*)(sm + L_AB + (half * 32 + 16 * g + (l & 15)) * 8) = f32x2{r1 * r2, (mb * r1 + bflo(p2)) * r2};
      }
    };
    auto epi_quad = [&](const f32x16& acc, int q, f32x2 ab, u32& o0, u32& o1) {
      const f32x4 c4 = *(const LAS f32x4*)(sm + L_CS + (32 * w + 8 * q + 4 * h) * 4);
      const f32x4 b4 = *(const LAS f32x4*)(sm + L_B1 + (32 * w + 8 * q + 4 * h) * 4);
      const f32x4 x = f32x4{acc[4 * q], acc[4 * q + 1], acc[4 * q + 2], acc[4 * q + 3]} * ab.x + (b4 - c4 * ab.y);
      const f32x2 g0 = gelu2(f32x2{x[0], x[1]}), g1 = gelu2(f32x2{x[2], x[3]});
      o0 = umin(pack_bf16(g0.x, g0.y), 0xfffffffeu); o1 = umin(pack_bf16(g1.x, g1.y), 0xfffffffeu);
    };
    f32x16 acc0, acc1;
    constexpr int XD = 3;
    if constexpr (!COMM) {
      v4u xf[XD];
#pragma unroll
      for (int k = 0; k < XD; ++k) xf[k] = tr_frag(xb + k * 1024);
      acc0 = f32x16{};
#pragma unroll
      for (int ks = 0; ks < 32; ++ks) {
        acc0 = mfma32(wf[ks], xf[ks % XD], acc0);
        if (ks + XD < 32) xf[ks % XD] = tr_frag(xb + (ks + XD) * 1024);
        if (ks < 2 * W2A && !(ks & 1)) { const int k = ks >> 1; wa[k] = LD128(l16 + (k & 3) * 1024, w2off + (k >> 2) * 4096, 0); }
        if (ks == 15) x_load(1);
        SBAR();
      }
    } else {
      __builtin_amdgcn_s_setprio(2);
      stats(0);
      __builtin_amdgcn_s_setprio(0);
    }
    STAMP(12 + 30 * ly);
    x_stage(1);
    STAMP(13 + 30 * ly);
    __syncthreads();
    STAMP(14 + 30 * ly);
    const int orow = ly ? l : 8 * w + (l >> 3), oq = ly ? w : (l & 7);
    const v2u xres0 = *(const LAS v2u*)(sm + L_R + (64 * s + orow) * 64 + 8 * oq);
    const v2u xres1 = *(const LAS v2u*)(sm + L_XB + (64 * s + orow) * 64 + 8 * oq);
    const int hfo = HF_OFF + ly * 4096 + t * 128;
    if constexpr (!COMM) {
      v4u xf[XD];
#pragma unroll
      for (int k = 0; k < XD; ++k) xf[k] = tr_frag(xb + 32768 + k * 1024);
      const f32x2 ab = *(const LAS f32x2*)(sm + L_AB + (l & 31) * 8);
      u32 o[4];
      acc1 = f32x16{};
#pragma unroll
      for (int ks = 0; ks < 32; ++ks) {
        acc1 = mfma32(wf[ks], xf[ks % XD], acc1);
        if (ks + XD < 32) xf[ks % XD] = tr_frag(xb + 32768 + (ks + XD) * 1024);
        if (ks & 1) { const int j = ks >> 1, k = W2A + j; wf[j] = LD128(l16 + (k & 3) * 1024, w2off + (k >> 2) * 4096, 0); }
        if (ks % 6 == 1 && ks < 24) { const int q = ks / 6; epi_quad(acc0, q, ab, o[2 * (q & 1)], o[2 * (q & 1) + 1]); }
        if (ks == 11 || ks == 23) ST128((v4u{o[0], o[1], o[2], o[3]}), l16 + (ks / 12) * 1024, hoff, SC1);
        if (ks % 6 == 5 || ks >= 24) SBAR();
      }
    } else {
      if (l == 0) ST32(1u, 0, hfo + s * 4, SC1);
      __builtin_amdgcn_s_setprio(2);
      stats(1);
      __builtin_amdgcn_s_setprio(0);
#pragma unroll 1
      for (int sp = 0; sp < SPIN_CAP; ++sp) {
        CBAR();
        const u32 f = LD32((l & 7) * 4, hfo, SC1);
        if (__all(f == 1u)) break;
        __builtin_amdgcn_s_sleep(1);
      }
      CBAR();
    }
    STAMP(15 + 30 * ly);
    __syncthreads();
    v4u hf[14];
    const int hin = H_OFF + ly * H_LAYER + (t * 8 + w) * (28 * 1024);
    auto h_load = [&](int half, int lo, int hi) {
#pragma unroll
      for (int k = lo; k < hi; ++k) { hf[k] = LD128(l16, hin + (14 * half + k) * 1024, SC1); SBAR(); }
    };
    h_load(0, 0, 7);
    STAMP(16 + 30 * ly);
    if constexpr (!COMM) {
      const f32x2 ab = *(const LAS f32x2*)(sm + L_AB + (32 + (l & 31)) * 8);
#pragma unroll
      for (int sp = 0; sp < 2; ++sp) {
        u32 o[4];
        epi_quad(acc1, 2 * sp, ab, o[0], o[1]);
        epi_quad(acc1, 2 * sp + 1, ab, o[2], o[3]);
        ST128((v4u{o[0], o[1], o[2], o[3]}), l16 + sp * 1024, hoff + 14336, SC1);
      }
      SBAR();
    }
    h_load(0, 7, 14);
    STAMP(17 + 30 * ly);
    LAS char* rbw = sm + L_R + (w * 64 + (l & 31)) * RB_ROW + h * 16;
    auto g2_out = [&](int half, v2u xres) {
      const LAS char* rp = sm + L_R + orow * RB_ROW + oq * 16;
      f32x4 o4 = unpack4(xres) + *(const LAS float*)(sm + L_B2 + orow * 4);
      f32x4 p4 = *(const LAS f32x4*)rp;
#pragma unroll
      for (int ww = 1; ww < 8; ++ww) { const f32x4 v = *(const LAS f32x4*)(rp + ww * (64 * RB_ROW)); if (ww & 1) o4 += v; else p4 += v; }
      o4 += p4;
      u32 o0 = pack_bf16(o4[0], o4[1]), o1 = pack_bf16(o4[2], o4[3]);
      if (ly) {
        u16* op = out + (1024 * half + 32 * t + 4 * w) * DM + 64 * s + l;
        op[0] = (u16)(o0 & 0xffffu); op[DM] = (u16)(o0 >> 16); op[2 * DM] = (u16)(o1 & 0xffffu); op[3 * DM] = (u16)(o1 >> 16);
      } else {
        o0 = umin(o0, 0xfffffffeu); o1 = umin(o1, 0xfffffffeu);
        ST64((v2u{o0, o1}), (tid >> 3) * 4096 + (tid & 7) * 8, XC_OFF + (2 << 20) + s * (64 * 4096) + t * 64 + half * 2048, SC1);
        const float x0 = bflo(o0), x1 = bfhi(o0), x2 = bflo(o1), x3 = bfhi(o1);
        const float a0 = swap32_add(x0, x2), a1 = swap32_add(x0 * x0, x2 * x2), a2 = swap32_add(x1, x3), a3 = swap32_add(x1 * x1, x3 * x3);
        const float sv = dpp_add<0x128>(swap16_add(a0, a2)), qv = dpp_add<0x128>(swap16_add(a1, a3));
        if ((l & 8) == 0) *(LAS f32x2*)(sm + L_SP + ((half * 8 + w) * 32 + 4 * (l & 7) + (l >> 4)) * 8) = f32x2{sv, qv};
      }
    };
    auto pst_out = [&](int half) {
      float v = 0.f;
#pragma unroll
      for (int k = 0; k < 8; ++k) v += *(const LAS float*)(sm + L_SP + (half * 8 + k) * 256 + l * 4);
      ST64((v2u{__builtin_bit_cast(u32, v), (u32)opaque_v(1)}), (l >> 3) * 512 + (l & 7) * 8, PST_OFF + (t * 2 + half) * 4096 + s * 64, SC1);
    };
#define W2FRAG(k) (COMM ? wf[k] : (k) < W2A ? wa[(k) < W2A ? (k) : 0] : wf[(k) < W2A ? 0 : (k) - W2A])
    auto gemm2 = [&](int half) {
      f32x16 a0, a1;
#pragma unroll 1
      for (int sp = 0; sp < SPIN_CAP; ++sp) {
        a0 = f32x16{}; a1 = f32x16{};
        u32 mx = 0;
#pragma unroll
        for (int k = 0; k < 14; ++k) {
          a0 = mfma32(hf[k], W2FRAG(2 * k), a0); a1 = mfma32(hf[k], W2FRAG(2 * k + 1), a1);
          mx = umax(mx, max4(hf[k]));
        }
        if (__all(mx != 0xffffffffu)) break;
        STAMP(10 + 8 * half + 30 * ly);
        __builtin_amdgcn_s_sleep(2);
        CBAR();
        h_load(half, 0, 14);
      }
      STAMP(19 + 4 * half + 30 * ly);
      if (!COMM && half == 0) h_load(1, 0, 14);
      if (half == 0) STAMP(20 + 30 * ly);
#pragma unroll
      for (int q = 0; q < 4; ++q) {
        *(LAS f32x4*)(rbw + q * 32) = f32x4{a0[4 * q], a0[4 * q + 1], a0[4 * q + 2], a0[4 * q + 3]};
        *(LAS f32x4*)(rbw + 32 * RB_ROW + q * 32) = f32x4{a1[4 * q], a1[4 * q + 1], a1[4 * q + 2], a1[4 * q + 3]};
      }
      if constexpr (!COMM) {
        if (half == 1) {
          SBAR();
          if (ly == 0) {
            if (w == 0) {
#pragma unroll 1
              for (int sp = 0; sp < 3; ++sp) { CBAR(); fin_load(0); fin0 = fin_try(0); if (fin0) break; }
            }
            LOAD_W(0, 16);
          } else {
#pragma unroll
            for (int i = 0; i < 16; ++i) asm volatile("" : "=v"(wf[i]));
          }
        }
      }
    };
    gemm2(0);
    __syncthreads();
    if constexpr (COMM) h_load(1, 0, 14);
    STAMP(21 + 30 * ly);
    g2_out(0, xres0);
    STAMP(22 + 30 * ly);
    __syncthreads();
    if (COMM && ly == 0) pst_out(0);
    gemm2(1);
    __syncthreads();
    STAMP(24 + 30 * ly);
    g2_out(1, xres1);
    STAMP(25 + 30 * ly);
    if (ly == 0) {
      cin_load(0);
      __syncthreads();
      if constexpr (COMM) { pst_out(1); fin_wait(1); }
      else if (!fin0) { STAMP(28); fin_wait(0); }
      STAMP(26 + 30 * ly);
      xr0 = cin_ln(0);
      STAMP(27 + 30 * ly);
    }
  }
}

__global__ __launch_bounds__(512, 2) void k_main(const u16* __restrict__ dskip, const u16* __restrict__ b1,
    const u16* __restrict__ b2, char* __restrict__ ws, u16* __restrict__ out) {
  extern __shared__ __attribute__((aligned(16))) char smem[];
  LAS char* sm = (LAS char*)smem;
  const int b0 = blockIdx.x, w0 = __builtin_amdgcn_readfirstlane(threadIdx.x >> 6);
  const __amdgpu_buffer_rsrc_t rs = __builtin_amdgcn_make_buffer_rsrc(ws, 0, WS_BYTES, 0x00020000);
  STAMP(0);
  if (w0 == 7) main_body<true>(sm, rs, ws, out, dskip, b1, b2, b0, w0);
  else main_body<false>(sm, rs, ws, out, dskip, b1, b2, b0, w0);
  STAMP_FLUSH(1);
}

extern "C" void kernel_launch(void* const* d_in, const int* in_sizes, int n_in, void* d_out, int out_size, void* d_ws,
                              size_t ws_size, hipStream_t stream) {
  (void)hipFuncSetAttribute((const void*)k_main, hipFuncAttributeMaxDynamicSharedMemorySize, MAIN_LDS);
  k_prep<<<256, 512, 0, stream>>>((const u16*)d_in[0], (const u16*)d_in[1], (const u16*)d_in[2], (const u16*)d_in[3],
                                  (const u16*)d_in[4], (const u16*)d_in[5], (const u16*)d_in[8], (const u16*)d_in[10], (char*)d_ws,
                                  (const u16*)d_in[9], (const u16*)d_in[11], (const u16*)d_in[6]);
  k_main<<<256, 512, MAIN_LDS, stream>>>((const u16*)d_in[6], (const u16*)d_in[9], (const u16*)d_in[11], (char*)d_ws, (u16*)d_out);
}
```

```cpp
#include <hip/hip_runtime.h>
#include <hip/hip_bf16.h>

typedef unsigned int u32;
typedef unsigned short u16;
typedef __attribute__((ext_vector_type(4))) u32 v4u;
typedef __attribute__((ext_vector_type(2))) u32 v2u;
typedef __attribute__((ext_vector_type(4))) float f32x4;
typedef __attribute__((ext_vector_type(2))) float f32x2;
typedef __attribute__((ext_vector_type(16))) float f32x16;
typedef __attribute__((ext_vector_type(8))) __bf16 bf16x8;
typedef __attribute__((ext_vector_type(2))) __bf16 bf16x2;
typedef __attribute__((ext_vector_type(8))) _Float16 f16x8;
typedef __attribute__((ext_vector_type(2))) _Float16 f16x2;
typedef __attribute__((ext_vector_type(4))) short s16x4;
#define LAS __attribute__((address_space(3)))
#define DI __device__ __forceinline__
#define STAMP(i) __builtin_amdgcn_sched_barrier(0)
#define STAMP_INIT(p)
#define STAMP_FLUSH(kid)

constexpr int SL = 2048, DM = 512, NS = 128, FF = 1792;
constexpr int CNT_OFF = 0;
constexpr int HF_OFF = CNT_OFF + 16384;
constexpr int FST_OFF = 65536;
constexpr int CS_OFF = FST_OFF + 32768;
constexpr int PST_OFF = CS_OFF + 65536;
constexpr int XC_OFF = 1 << 20;
constexpr int XM_OFF = 5 << 20;
constexpr int W1F_OFF = 9 << 20;
constexpr int W2F_OFF = 11 << 20;
constexpr int H_OFF = 13 << 20;
constexpr int H_LAYER = 32 * 8 * 2 * 14 * 1024;
constexpr int WS_BYTES = 28 << 20;
#define HANDSHAKE 1
constexpr int SC1 = 16;
constexpr int SPIN_CAP = 1 << 16;

DI u32 umin(u32 a, u32 b) { return a < b ? a : b; }
DI u32 umax(u32 a, u32 b) { return a > b ? a : b; }
DI float bf2f(u32 h) { return __builtin_bit_cast(float, h << 16); }
DI float bflo(u32 p) { return __builtin_bit_cast(float, p << 16); }
DI float bfhi(u32 p) { return __builtin_bit_cast(float, p & 0xffff0000u); }
DI u32 pack_bf16(float a, float b) { bf16x2 v = {(__bf16)a, (__bf16)b}; return __builtin_bit_cast(u32, v); }
DI float bfr(float a) { return bflo(pack_bf16(a, a)); }
DI u32 pack_f16(float a, float b) { f16x2 v = {(_Float16)a, (_Float16)b}; return __builtin_bit_cast(u32, v); }
DI float dot2bf(u32 a, u32 b, float c) {
  return __builtin_amdgcn_fdot2_f32_bf16(__builtin_bit_cast(bf16x2, a), __builtin_bit_cast(bf16x2, b), c, false);
}
DI f32x16 mfma32(v4u a, v4u b, f32x16 c) {
  return __builtin_amdgcn_mfma_f32_32x32x16_bf16(__builtin_bit_cast(bf16x8, a), __builtin_bit_cast(bf16x8, b), c, 0, 0, 0);
}
DI f32x4 mfma16h(v4u a, v4u b, f32x4 c) {
  return __builtin_amdgcn_mfma_f32_16x16x32_f16(__builtin_bit_cast(f16x8, a), __builtin_bit_cast(f16x8, b), c, 0, 0, 0);
}
DI f32x4 mfma16b(v4u a, v4u b, f32x4 c) {
  return __builtin_amdgcn_mfma_f32_16x16x32_bf16(__builtin_bit_cast(bf16x8, a), __builtin_bit_cast(bf16x8, b), c, 0, 0, 0);
}
DI v4u tr_frag(const LAS char* p) {
  const s16x4 a = __builtin_amdgcn_ds_read_tr16_b64_v4i16((LAS s16x4*)p);
  const s16x4 c = __builtin_amdgcn_ds_read_tr16_b64_v4i16((LAS s16x4*)(p + 256));
  return __builtin_bit_cast(v4u, __builtin_shufflevector(a, c, 0, 1, 2, 3, 4, 5, 6, 7));
}
template <int CTRL> DI float dpp_add(float v) {
  const int o = __builtin_amdgcn_update_dpp(0, __builtin_bit_cast(int, v), CTRL, 0xf, 0xf, false);
  return v + __builtin_bit_cast(float, o);
}
DI float row_sum16(float v) { v = dpp_add<0x128>(v); v = dpp_add<0x124>(v); v = dpp_add<0x122>(v); return dpp_add<0x121>(v); }
DI float swap32_add(float a, float c) {
  const auto r = __builtin_amdgcn_permlane32_swap(__builtin_bit_cast(u32, a), __builtin_bit_cast(u32, c), false, false);
  const u32 r0 = r[0], r1 = r[1];
  return __builtin_bit_cast(float, r0) + __builtin_bit_cast(float, r1);
}
DI float swap16_add(float a, float c) {
  const auto r = __builtin_amdgcn_permlane16_swap(__builtin_bit_cast(u32, a), __builtin_bit_cast(u32, c), false, false);
  const u32 r0 = r[0], r1 = r[1];
  return __builtin_bit_cast(float, r0) + __builtin_bit_cast(float, r1);
}
DI float wave_sum(float v) { v = row_sum16(v); v = swap16_add(v, v); return swap32_add(v, v); }
DI void ln_stats(float s, float q, float& m, float& v, float& mb, float& rs) {
  m = s * (1.0f / DM);
  v = fmaxf(q * (1.0f / DM) - m * m, 0.f);
  const u32 pk = pack_bf16(m, __builtin_amdgcn_sqrtf(bfr(bfr(v) + 1e-5f)));
  mb = bflo(pk); rs = __builtin_amdgcn_rcpf(bfhi(pk));
}
DI u32 npz(float a) { return umin(__builtin_bit_cast(u32, a), 0xfffffffeu); }
#define LD128(vo, so, aux) __builtin_amdgcn_raw_buffer_load_b128(rs, (vo), (so), (aux))
#define LD64(vo, so, aux) __builtin_amdgcn_raw_buffer_load_b64(rs, (vo), (so), (aux))
#define LD32(vo, so, aux) __builtin_amdgcn_raw_buffer_load_b32(rs, (vo), (so), (aux))
#define ST128(v, vo, so, aux) __builtin_amdgcn_raw_buffer_store_b128((v), rs, (vo) + (so), 0, (aux))
#define ST64(v, vo, so, aux) __builtin_amdgcn_raw_buffer_store_b64((v), rs, (vo), (so), (aux))
#define ST32(v, vo, so, aux) __builtin_amdgcn_raw_buffer_store_b32((v), rs, (vo), (so), (aux))
#define DRAIN() asm volatile("s_waitcnt vmcnt(0)" ::: "memory")
#define CBAR() asm volatile("" ::: "memory")
#define SBAR() __builtin_amdgcn_sched_barrier(0)
DI int opaque_v(int x) { asm volatile("" : "+v"(x)); return x; }
template <typename T, int N> DI void undef_regs(T (&a)[N]) {
#pragma unroll
  for (int i = 0; i < N; ++i) asm volatile("" : "=v"(a[i]));
}
DI int opaque_s(int x) { asm volatile("" : "+s"(x)); return x; }

constexpr int KT_REL = 24576 + 16;
constexpr int KT_CH = 1312;
DI void cexp_p(float arl, float rh, float rl, float p, float& re, float& im) {
  const float e = __builtin_amdgcn_exp2f(arl * p);
  const float r = __builtin_fmaf(rl, p, __builtin_amdgcn_fractf(rh * p));
  re = e * __builtin_amdgcn_cosf(r);
  im = e * __builtin_amdgcn_sinf(r);
}
DI float k_scale(float bs, int sign) {
  const int ex = (int)((__builtin_bit_cast(u32, bs) >> 23) & 0xff) - 126;
  int e = 15 - ex; e = e > 100 ? 100 : (e < -100 ? -100 : e);
  return __builtin_bit_cast(float, (u32)(127 + sign * e) << 23);
}
constexpr int KC_CH = 8 * 260 * 16;
constexpr int L_U = 2 * KC_CH;
constexpr int U_CH = 4672;
constexpr int L_R = L_U + 2 * U_CH;
constexpr int L_XB = L_R + 32768;
constexpr int L_PB = L_XB + 256;
constexpr int L_KT = L_R + KT_REL;
constexpr int L_PRM = L_XB + 16384;
constexpr int L_BND = L_PRM + 5120;
constexpr int RB_ROW = 144;
constexpr int L_S = L_R + 8 * 64 * RB_ROW;
constexpr int L_B1 = L_S;
constexpr int L_CS = L_B1 + 896;
constexpr int L_SP = L_CS + 896;
constexpr int L_AC = L_SP + 4096;
constexpr int L_B2 = L_AC + 64;
constexpr int L_DK = L_B2 + 256;
constexpr int L_AB = L_DK + 16;
constexpr int L_END = L_AB + 512;
constexpr int MAIN_LDS = L_END;
#define MAPV 1
#define MAP_T(b) ((((b) >> 1) & 3) * 8 + ((b) >> 5))
#define MAP_S(b) (((b) & 1) * 4 + (((b) >> 3) & 3))
#define W1_SPLIT 0
constexpr int W2A = 12;

DI f32x2 gelu2(f32x2 x) {
  const f32x2 t = x * (x * x * -0.10294324f + -2.3022082f);
  f32x2 e; e.x = __builtin_amdgcn_exp2f(t.x); e.y = __builtin_amdgcn_exp2f(t.y);
  e = e + 1.0f;
  f32x2 r; r.x = __builtin_amdgcn_rcpf(e.x); r.y = __builtin_amdgcn_rcpf(e.y);
  return x * r;
}
DI u32 max4(v4u a) { return umax(umax(a[0], a[1]), umax(a[2], a[3])); }
DI f32x4 unpack4(v2u p) { const u32 a = p[0], c = p[1]; return f32x4{bflo(a), bfhi(a), bflo(c), bfhi(c)}; }
DI f32x4 conv_ln(LAS char* dst, v2u xp, v4u mm, v4u rr, float dk) {
  const f32x4 x = unpack4(xp);
  const f32x4 u = (x - __builtin_bit_cast(f32x4, mm)) * __builtin_bit_cast(f32x4, rr);
  *(LAS v2u*)dst = v2u{pack_f16(u[0], u[1]), pack_f16(u[2], u[3])};
  return x + u * dk;
}

struct Args {
  const u16 *x0, *logA, *Aim, *Cre, *Cim, *logdt, *dskip, *w1, *b1, *w2, *b2;
  char* ws;
  u16* out;
};
template <bool COMM>
DI void main_body(LAS char* sm, const __amdgpu_buffer_rsrc_t rs, const Args& a, int b0, int w0) {
  char* const ws = a.ws;
  u16* const out = a.out;
  STAMP_INIT(sm + L_END);
  STAMP(0);
  v4u wf[32], wa[W2A];
  f32x4 xr0, xr1;
#define LOAD_W(lo, hi) do { _Pragma("unroll") for (int ks = (lo); ks < (hi); ++ks) { \
      if constexpr (!COMM) wf[ks] = LD128(l16 + (ks & 3) * 1024, W1F_OFF + (7 * s + w) * 32768 + (ks >> 2) * 4096, SC1); \
      else if (ks < 28) wf[ks] = LD128(l16 + (ks & 3) * 1024, W2F_OFF + (s * 8 + 7) * 28672 + (ks >> 2) * 4096, SC1); } \
      SBAR(); } while (0)
#define CW(k) do { if (!COMM || ly == 0) LOAD_W(k, (k) + 1); } while (0)
  {
    const int b = opaque_s(b0), w = opaque_s(w0), tid = opaque_v(threadIdx.x), l = tid & 63, l16 = l * 16, s = MAP_S(b);
    const int cc = (tid >> 3) & 1, q8 = (tid >> 4) * 8 + (tid & 7);
    const int kk = l >> 4, i16 = l & 15, t = MAP_T(b);
    u32 rla, rai, rcr, rci, rdt, hs_c = 0, warm = 0;
    v4u xrow[2], xtile[2], wr[4];
    const int fr = (b < 112 ? b : b - 112) * 16 + 4 * (w & 3);
    if (!COMM && w < 4) {
      const int o = 2 * b * NS + tid;
      rla = a.logA[o]; rai = a.Aim[o]; rcr = a.Cre[o]; rci = a.Cim[o]; rdt = a.logdt[2 * b + (w >> 1)];
      undef_regs(wr); undef_regs(xrow); undef_regs(xtile);
      if (w == 3) {
        unsigned long long pc;
        asm volatile("s_getpc_b64 s[90:91]\n\ts_add_u32 s90, s90, _Z6k_main4Args@rel32@lo+4\n\ts_addc_u32 s91, s91, _Z6k_main4Args@rel32@hi+12\n\ts_mov_b64 %0, s[90:91]"
                     : "=s"(pc) :: "s90", "s91", "scc");
        warm = *(const __attribute__((address_space(1))) u32*)(pc + (b >> 3) * 1280 + (l < 10 ? l : 0) * 128);
      }
    } else {
      asm volatile("" : "=v"(rla), "=v"(rai), "=v"(rcr), "=v"(rci), "=v"(rdt));
      if (HANDSHAKE && !COMM && w == 4) hs_c = LD32(0, CNT_OFF + 8192 + b * 4, SC1);
#pragma unroll
      for (int i = 0; i < 2; ++i) {
        xrow[i] = *(const v4u*)(a.x0 + (8 * b + 2 * (w & 3) + i) * DM + 8 * l);
        xtile[i] = *(const v4u*)(a.x0 + (64 * (b >> 3) + 32 * i + (l >> 1)) * DM + 64 * (b & 7) + 16 * (w & 3) + 8 * (l & 1));
      }
      if (b < 112) {
        const u16* p = a.w1 + (16 * (fr & 31) + (l >> 2)) * FF + 32 * (fr >> 5) + 8 * (l & 3);
#pragma unroll
        for (int f = 0; f < 4; ++f) wr[f] = *(const v4u*)(p + 16 * f * FF);
      } else {
        const int r1 = (b < 224 ? fr : 0) >> 1, ks2 = r1 % 14, r2 = r1 / 14;
        const u16* p = a.w2 + (224 * (r2 & 7) + 16 * ks2 + (l >> 2)) * DM + 64 * (r2 >> 3) + 8 * (l & 3);
#pragma unroll
        for (int f = 0; f < 4; ++f) wr[f] = *(const v4u*)(p + 16 * (f >> 1) * DM + 32 * (f & 1));
      }
    }
    const float dk = bf2f(a.dskip[2 * b + cc]);
    const float bias2 = bf2f(a.b2[64 * s + l]);
    const float bb1 = bf2f(a.b1[224 * s + (tid < 224 ? tid : 0)]);
    SBAR();
    u32 hs_q = 0;
    if (HANDSHAKE && COMM && b == 255) {
      const v4u d = LD128(l16, CNT_OFF, SC1);
      const u32 qo = LD32(0, CNT_OFF + 4096, SC1);
      u32 q = qo + 1u;
#pragma unroll 1
      for (int k = 0; k < 300; ++k) { if (!__any(d[0] == q || d[1] == q || d[2] == q || d[3] == q)) break; ++q; }
      hs_q = __builtin_amdgcn_readfirstlane(q);
      if (l < 8) ST32(hs_q, l * 128, CNT_OFF + 4096, SC1);
    }
    if (tid < 16) { *(LAS v4u*)(sm + (tid >> 3) * KC_CH + (tid & 7) * 4160) = v4u{0u, 0u, 0u, 0u}; *(LAS u32*)(sm + L_AC + tid * 4) = 0u; }
    if (tid >= 64 && tid < 128) *(LAS v4u*)(sm + L_U + ((tid >> 5) & 1) * U_CH + (tid & 31) * 16) = v4u{0u, 0u, 0u, 0u};
    if (tid >= 128 && tid < 130) *(LAS v4u*)(sm + L_KT - 16 + (tid - 128) * KT_CH) = v4u{0u, 0u, 0u, 0u};
    const int c = tid >> 8, x = tid & 255;
    int tix, gix[8];
    {
      const int col = x >> 4, row = x & 15;
      const int p0 = 128 * (row >> 3) + (row & 7) + 8 * col;
      const int p1 = (row < 8 ? 256 + 128 * (row >> 2) + 2 * (row & 3) : 512 + 128 * ((row - 8) >> 1) + 4 * (row & 1)) + 8 * col;
      const int c1 = row < 8 ? 128 + (p1 >> 1) : 256 + (p1 >> 2);
      const int c2 = row < 8 ? 512 + 16 * row + col : (x == 8 ? 640 : 641);
      tix = opaque_v(p0 | (c1 << 10) | (c2 << 20));
#pragma unroll
      for (int j = 0; j < 8; ++j) {
        const int la = 8 * x + 7 - 2 * j, lb = la - 1;
        const u32 hb = pack_bf16((float)la, (float)lb);
        const int ia = la < 128 ? la : (int)(hb & 0xffffu) - 17024, ib = lb < 128 ? lb : (int)(hb >> 16) - 17024;
        gix[j] = opaque_v((KT_REL + c * KT_CH + 2 * ia) | ((KT_REL + c * KT_CH + 2 * ib) << 16));
      }
    }
    const float pB = (float)(8 * i16);
    const float pA0 = (float)(128 * (i16 >> 3) + (i16 & 7));
    const float pA1 = (float)(i16 < 8 ? 256 + 128 * (i16 >> 2) + 2 * (i16 & 3) : 512 + 128 * ((i16 - 8) >> 1) + 4 * (i16 & 1));
    const float pA2 = (float)(i16 < 8 ? 1024 + 128 * i16 : 2048);
    f32x4 acc0 = {0.f, 0.f, 0.f, 0.f}, acc1 = acc0, acc2 = acc0;
    STAMP(1);
    if (!COMM && w < 4) {
      const float dt = bfr(expf(bf2f(rdt)));
      const float arl = -bfr(expf(bf2f(rla))) * dt * 1.4426950408889634f;
      double tt = (double)(bf2f(rai) * dt) * 0.15915494309189533577;
      tt -= __builtin_rint(tt);
      const float rh = __builtin_bit_cast(float, __builtin_bit_cast(u32, (float)tt) & 0xfffff000u);
      const float rl = (float)(tt - (double)rh);
      const float cr = bf2f(rcr) * dt * 2.0f, ci = bf2f(rci) * dt * 2.0f;
      LAS float* q = (LAS float*)(sm + L_PRM + (tid >> 2) * 80) + (tid & 3);
      q[0] = arl; q[4] = rh; q[8] = rl; q[12] = cr; q[16] = ci;
      const float bs = wave_sum(fabsf(cr) + fabsf(ci));
      if (l == 0) *(LAS float*)(sm + L_BND + w * 4) = bs;
    }
    STAMP(56);
    __syncthreads();
    STAMP(57);
    if (COMM || w >= 4) {
      __builtin_amdgcn_s_setprio(3);
      if (HANDSHAKE && !COMM && w == 4 && l == 0) { ST32(hs_c + 1u, 0, CNT_OFF + 12288 + b * 4, SC1); *(LAS u32*)(sm + L_AC + 12) = hs_c + 1u; }
      {
        const u32 on = (u32)opaque_v(-1);
        const v4u on4 = {on, on, on, on};
        const int i8 = tid & 255;
#pragma unroll
        for (int k = 0; k < 2; ++k) ST128(on4, (i8 >> 3) * 32768 + (i8 & 7) * 16, XM_OFF + k * (1 << 20) + 128 * b, SC1);
        if (i8 < 64) ST128(on4, (i8 >> 2) * 512 + (i8 & 3) * 16, PST_OFF + t * 8192 + s * 64, SC1);
        else if (i8 < 68) ST128(on4, ((i8 >> 1) & 1) * 8192 + (i8 & 1) * 16, FST_OFF + 16384 + (8 * t + s) * 32, SC1);
        else if (i8 < 70) ST32(on, (i8 & 1) * 4096, HF_OFF + t * 128 + s * 4, SC1);
        SBAR();
      }
#pragma unroll
      for (int i = 0; i < 2; ++i) {
        float sx = 0.f, qx = 0.f;
#pragma unroll
        for (int j = 0; j < 4; ++j) { const u32 p = xrow[i][j]; sx = dot2bf(p, 0x3f803f80u, sx); qx = dot2bf(p, p, qx); }
        *(LAS v4u*)(sm + L_XB + (w & 3) * 4096 + i * 1024 + l16) = xtile[i];
        const float r = row_sum16(swap16_add(swap32_add(sx, qx), swap32_add(sx, qx)));
        const auto rr = __builtin_amdgcn_permlane32_swap(__builtin_bit_cast(u32, r), __builtin_bit_cast(u32, r), false, false);
        const u32 rs0 = rr[0], rs1 = rr[1];
        float mm, vv, mb, rcs;
        ln_stats(__builtin_bit_cast(float, rs0), __builtin_bit_cast(float, rs1), mm, vv, mb, rcs);
        if (l < 2) ST32(__builtin_bit_cast(u32, l ? rcs : mb), l * 16, FST_OFF + (2 * b + ((w >> 1) & 1)) * 32 + (2 * (w & 1) + i) * 4, SC1);
      }
      {
        const LAS char* tp = sm + L_XB + (w & 3) * 4096 + (16 * kk + (i16 >> 2)) * 32 + (l & 3) * 8;
        const s16x4 t0 = __builtin_amdgcn_ds_read_tr16_b64_v4i16((LAS s16x4*)tp), t1 = __builtin_amdgcn_ds_read_tr16_b64_v4i16((LAS s16x4*)(tp + 128));
        const s16x4 t2 = __builtin_amdgcn_ds_read_tr16_b64_v4i16((LAS s16x4*)(tp + 256)), t3 = __builtin_amdgcn_ds_read_tr16_b64_v4i16((LAS s16x4*)(tp + 384));
        const int so = XC_OFF + (64 * (b & 7) + 16 * (w & 3)) * 4096 + (b >> 3) * 128;
        ST128(__builtin_bit_cast(v4u, __builtin_shufflevector(t0, t1, 0, 1, 2, 3, 4, 5, 6, 7)), i16 * 4096 + kk * 32, so, SC1);
        ST128(__builtin_bit_cast(v4u, __builtin_shufflevector(t2, t3, 0, 1, 2, 3, 4, 5, 6, 7)), i16 * 4096 + kk * 32 + 16, so, SC1);
      }
      if (b < 224) {
        LAS char* wl = sm + L_XB + (w & 3) * 4096;
#pragma unroll
        for (int f = 0; f < 4; ++f) *(LAS v4u*)(wl + f * 1024 + l16) = wr[f];
        const int hs = b < 112 ? 512 : 256, ts = b < 112 ? 256 : 512;
        const LAS char* rp = wl + (l >> 5) * hs + ((l & 15) >> 2) * 64 + 32 * ((l >> 4) & 1) + 8 * (l & 3);
        float cs = 0.f;
#pragma unroll
        for (int f = 0; f < 4; ++f) {
          const s16x4 t0 = __builtin_amdgcn_ds_read_tr16_b64_v4i16((LAS s16x4*)(rp + f * 1024)), t1 = __builtin_amdgcn_ds_read_tr16_b64_v4i16((LAS s16x4*)(rp + f * 1024 + ts));
          const v4u o = __builtin_bit_cast(v4u, __builtin_shufflevector(t0, t1, 0, 1, 2, 3, 4, 5, 6, 7));
#pragma unroll
          for (int j = 0; j < 4; ++j) { const u32 p = o[j]; cs = dot2bf(p, 0x3f803f80u, cs); }
          ST128(o, l16 + f * 1024, (b < 112 ? W1F_OFF : W2F_OFF) + fr * 1024, SC1);
        }
        cs = swap32_add(cs, cs);
        if (b < 112 && l < 32) ST32(__builtin_bit_cast(u32, cs), l * 4, CS_OFF + ((b & 1) * 4 + (w & 3)) * (FF * 4) + (b >> 1) * 128, SC1);
      }
      if (!(HANDSHAKE && COMM && b == 255)) __builtin_amdgcn_s_setprio(1);
    }
    STAMP(63);
    const f32x2 bn = *(const LAS f32x2*)(sm + L_BND + (w >> 2) * 8);
    const float sc = k_scale(bn.x + bn.y, 1);
#pragma unroll
    for (int g = 0; g < 2; ++g) {
      v4u fa0, fa1, fa2, fbh, fbl;
#pragma unroll
      for (int j = 0; j < 4; ++j) {
        const LAS float* pq = (const LAS float*)(sm + L_PRM + w * 640 + g * 320 + kk * 80) + j;
        const float arl = pq[0], rh = pq[4], rl = pq[8], cr = pq[12] * sc, ci = pq[16] * sc;
        float br, bi, er, ei;
        cexp_p(arl, rh, rl, pB, br, bi);
        bi = -bi;
        const u32 hi = pack_f16(br, bi);
        const f16x2 hv = __builtin_bit_cast(f16x2, hi);
        fbh[j] = hi;
        fbl[j] = pack_f16(br - (float)hv.x, bi - (float)hv.y);
        cexp_p(arl, rh, rl, pA0, er, ei);
        fa0[j] = pack_f16(cr * er - ci * ei, cr * ei + ci * er);
        cexp_p(arl, rh, rl, pA1, er, ei);
        fa1[j] = pack_f16(cr * er - ci * ei, cr * ei + ci * er);
        cexp_p(arl, rh, rl, pA2, er, ei);
        fa2[j] = pack_f16(cr * er - ci * ei, cr * ei + ci * er);
        if (g == 0 && j == 2 && (COMM || w >= 4)) {
          SBAR();
          DRAIN();
          u32 old = 0;
          if (l == 0) old = __hip_atomic_fetch_add((LAS u32*)(sm + L_AC), 1u, __ATOMIC_RELAXED, __HIP_MEMORY_SCOPE_WORKGROUP);
          if (__builtin_amdgcn_readfirstlane(old) == 3u) {
            if constexpr (HANDSHAKE) {
              const u32 q = LD32(0, CNT_OFF + 4096 + (b & 7) * 128, SC1);
              if (l == 0) { ST32(q, 0, CNT_OFF + b * 4, SC1); *(LAS u32*)(sm + L_AC + 8) = q; }
            } else if (l < 8) {
              __hip_atomic_fetch_add((u32*)(ws + CNT_OFF + (l * 8 + (b & 7)) * 128), 1u, __ATOMIC_RELAXED, __HIP_MEMORY_SCOPE_AGENT);
            }
          }
          if (!(HANDSHAKE && COMM && b == 255)) __builtin_amdgcn_s_setprio(0);
          STAMP(29);
        }
      }
      acc0 = mfma16h(fa0, fbh, acc0); acc1 = mfma16h(fa1, fbh, acc1); acc2 = mfma16h(fa2, fbh, acc2);
      acc0 = mfma16h(fa0, fbl, acc0); acc1 = mfma16h(fa1, fbl, acc1); acc2 = mfma16h(fa2, fbl, acc2);
    }
    *(LAS f32x4*)(sm + L_R + w * 3072 + i16 * 64 + kk * 16) = acc0;
    *(LAS f32x4*)(sm + L_R + w * 3072 + 1024 + i16 * 64 + kk * 16) = acc1;
    *(LAS f32x4*)(sm + L_R + w * 3072 + 2048 + i16 * 64 + kk * 16) = acc2;
    { const int o = tid < 224 ? tid * 4 : L_AC + 32 - L_B1; *(LAS float*)(sm + L_B1 + o) = bb1; }
    *(LAS float*)(sm + L_B2 + l * 4) = bias2;
    asm volatile("" :: "v"(warm));
    if (HANDSHAKE && COMM && b == 255) {
#pragma unroll 1
      for (int sp = 0; sp < SPIN_CAP; ++sp) {
        CBAR();
        const v4u d = LD128(l16, CNT_OFF, SC1);
        const u32 own = __hip_atomic_load((LAS u32*)(sm + L_AC), __ATOMIC_RELAXED, __HIP_MEMORY_SCOPE_WORKGROUP);
        if (__all(d[0] == hs_q && d[1] == hs_q && d[2] == hs_q && (l == 63 ? own == 4u : d[3] == hs_q))) break;
      }
      CBAR();
      const v4u cw = LD128(l16, CNT_OFF + 12288, SC1);
      ST128(cw, l16, CNT_OFF + 8192, SC1);
      __builtin_amdgcn_s_setprio(0);
      STAMP(60);
    }
    STAMP(61);
    __syncthreads();
    {
      const LAS float* pp = (const LAS float*)(sm + L_R + c * 12288) + x;
      LAS _Float16* kt = (LAS _Float16*)(sm + L_KT + c * KT_CH);
      kt[tix & 1023] = (_Float16)((pp[0] + pp[768]) + (pp[1536] + pp[2304]));
      kt[(tix >> 10) & 1023] = (_Float16)((pp[256] + pp[1024]) + (pp[1792] + pp[2560]));
      kt[tix >> 20] = (_Float16)((pp[512] + pp[1280]) + (pp[2048] + pp[2816]));
    }
    __syncthreads();
    STAMP(62);
    {
      u32 wd[8];
#pragma unroll
      for (int j = 0; j < 8; ++j)
        wd[j] = (u32)*(const LAS u16*)(sm + L_R + (gix[j] & 0xffff)) | ((u32)*(const LAS u16*)(sm + L_R + ((u32)gix[j] >> 16)) << 16);
#pragma unroll
      for (int cp = 0; cp < 8; ++cp) {
        const int sg = 7 - cp;
        v4u o;
#pragma unroll
        for (int k = 0; k < 4; ++k)
          o[k] = (sg & 1) ? __builtin_amdgcn_alignbit(wd[(sg + 1) / 2 + k], wd[(sg - 1) / 2 + k], 16) : wd[sg / 2 + k];
        *(LAS v4u*)(sm + c * KC_CH + cp * 4160 + (x + 1) * 16) = o;
      }
    }
    {
      const u32 on = (u32)opaque_v(-1);
      const v4u on4 = {on, on, on, on};
      ST128(on4, (tid >> 3) * 32768 + (tid & 7) * 16, XM_OFF + (2 << 20) + 128 * b, SC1);
      ST128(on4, ((tid >> 2) & 63) * 4096 + (tid >> 8) * 2048 + (tid & 3) * 16, XC_OFF + (2 << 20) + s * (64 * 4096) + t * 64, SC1);
    }
    STAMP(28);
    if (!COMM && w == 0) {
      u32 qp = *(const LAS u32*)(sm + L_AC + 8);
      hs_c = *(const LAS u32*)(sm + L_AC + 12);
#pragma unroll 1
      for (int sp = 0; sp < SPIN_CAP; ++sp) {
        CBAR();
        if constexpr (HANDSHAKE) {
          const u32 r = LD32(0, CNT_OFF + 8192 + b * 4, SC1), q = LD32(0, CNT_OFF + 4096 + (b & 7) * 128, SC1);
          if (__builtin_amdgcn_readfirstlane(r) == __builtin_amdgcn_readfirstlane(hs_c)) break;
          if (__builtin_amdgcn_readfirstlane(q) != __builtin_amdgcn_readfirstlane(qp)) { qp = q; if (l == 0) ST32(q, 0, CNT_OFF + b * 4, SC1); }
        } else {
          const u32 f = LD32((l & 7) * 128, CNT_OFF + (b & 7) * 1024, SC1);
          if (__all(f == 31u)) break;
        }
        __builtin_amdgcn_s_sleep(2);
      }
      CBAR();
      if (l == 0) __hip_atomic_store((LAS u32*)(sm + L_AC + 4), 1u, __ATOMIC_RELAXED, __HIP_MEMORY_SCOPE_WORKGROUP);
    }
#pragma unroll 1
    for (int sp = 0; sp < (SPIN_CAP << 4); ++sp) {
      if (__hip_atomic_load((LAS u32*)(sm + L_AC + 4), __ATOMIC_RELAXED, __HIP_MEMORY_SCOPE_WORKGROUP) != 0u) break;
      __builtin_amdgcn_s_sleep(1);
    }
    CBAR();
    STAMP(59);
    {
      const v2u xa = LD64(cc * 4096 + q8 * 8, XC_OFF + b * 8192, SC1), xb = LD64(cc * 4096 + q8 * 8 + 2048, XC_OFF + b * 8192, SC1);
      const v4u m0 = LD128(q8 * 32, FST_OFF, SC1), r0 = LD128(q8 * 32 + 16, FST_OFF, SC1);
      const v4u m1 = LD128(q8 * 32, FST_OFF + 8192, SC1), r1 = LD128(q8 * 32 + 16, FST_OFF + 8192, SC1);
      SBAR();
      LOAD_W(0, 8);
      xr0 = conv_ln(sm + L_U + cc * U_CH + 512 + q8 * 8, xa, m0, r0, dk);
      xr1 = conv_ln(sm + L_U + cc * U_CH + 2560 + q8 * 8, xb, m1, r1, dk);
      if ((tid & ~8) == 0) { const f32x2 bc = *(const LAS f32x2*)(sm + L_BND + cc * 8); *(LAS f32x2*)(sm + L_DK + cc * 8) = f32x2{dk, k_scale(bc.x + bc.y, -1)}; }
      SBAR();
#pragma unroll
      for (int i = 8; i < (COMM ? 28 : 16); ++i) asm volatile("" : "=v"(wf[i]));
    }
  }

#pragma unroll 1
  for (int ly = 0; ly < 2; ++ly) {
    const int b = opaque_s(b0), w = opaque_s(w0);
    const int t = MAP_T(b), s = MAP_S(b);
    int tid = opaque_v(threadIdx.x), l = tid & 63, l16 = l * 16;
    const int xmo = XM_OFF + ly * (2 << 20) + t * 32768 + w * 1024;
    const int w2off = W2F_OFF + ((s * 8 + w) * 28) * 1024;
    const int hoff = H_OFF + ly * H_LAYER + (t * 8 + s) * (28 * 1024) + (2 * w) * 1024;
    v4u xs[4];
    auto x_load = [&](int half) {
#pragma unroll
      for (int k = 0; k < 4; ++k) xs[k] = LD128(l16, xmo + half * (1 << 20) + k * 8192, SC1);
    };
    auto x_ok = [&]() { return __all(umax(umax(max4(xs[0]), max4(xs[1])), umax(max4(xs[2]), max4(xs[3]))) != 0xffffffffu); };
    auto x_stage = [&](int half) {
      if (!x_ok()) {
        int sp = 0;
#pragma unroll 1
        do {
          STAMP(56 + half + 2 * ly);
          __builtin_amdgcn_s_sleep(4);
          CBAR();
          x_load(half);
        } while (!x_ok() && ++sp < SPIN_CAP);
      }
#pragma unroll
      for (int k = 0; k < 4; ++k) *(LAS v4u*)(sm + L_R + half * 32768 + k * 8192 + tid * 16) = xs[k];
    };
    v2u pg;
    bool fin0 = true;
    auto fin_load = [&](int hf) { pg = LD64(l * 8, PST_OFF + ((t * 2 + hf) * 8 + s) * 512, SC1); };
    auto fin_try = [&](int hf) -> bool {
      if (!__all(pg[1] == 1u)) return false;
      const u32 e0 = pg[0];
      float v = __builtin_bit_cast(float, e0);
      v = swap32_add(v, v); v = swap16_add(v, v); v = dpp_add<0x128>(v);
      const float o = __builtin_bit_cast(float, __builtin_amdgcn_update_dpp(0, __builtin_bit_cast(int, v), 0xb1, 0xf, 0xf, false));
      float mm, vr, mb, rcs;
      ln_stats((l & 1) ? o : v, (l & 1) ? v : o, mm, vr, mb, rcs);
      if (l < 8) ST32(npz((l & 1) ? rcs : mb), (l & 1) * 16 + (l >> 1) * 4, FST_OFF + 16384 + hf * 8192 + (8 * t + s) * 32, SC1);
      return true;
    };
    auto fin_wait = [&](int hf) {
#pragma unroll 1
      for (int sp = 0; sp < SPIN_CAP; ++sp) {
        CBAR();
        fin_load(hf);
        if (fin_try(hf)) break;
        __builtin_amdgcn_s_sleep(1);
      }
    };
    v2u ix; v4u im, ir;
    auto cin_load = [&](int hf) {
      const int cc = (tid >> 3) & 1, q8 = (tid >> 4) * 8 + (tid & 7);
      ix = LD64(cc * 4096 + q8 * 8, XC_OFF + (2 << 20) + b * 8192 + hf * 2048, SC1);
      im = LD128(q8 * 32, FST_OFF + 16384 + hf * 8192, SC1); ir = LD128(q8 * 32 + 16, FST_OFF + 16384 + hf * 8192, SC1);
    };
    auto cin_ok = [&]() { return __all(umax(umax(ix[0], ix[1]), umax(max4(im), max4(ir))) != 0xffffffffu); };
    auto cin_ln = [&](int hf) -> f32x4 {
      if (!cin_ok()) {
        int sp = 0;
#pragma unroll 1
        do {
          STAMP(29 + 2 * hf);
          __builtin_amdgcn_s_sleep(4);
          CBAR();
          cin_load(hf);
        } while (!cin_ok() && ++sp < SPIN_CAP);
      }
      const int cc = (tid >> 3) & 1, q8 = (tid >> 4) * 8 + (tid & 7);
      return conv_ln(sm + L_U + cc * U_CH + 512 + hf * 2048 + q8 * 8, ix, im, ir, *(const LAS float*)(sm + L_DK + cc * 8));
    };
    CW(16); CW(17); CW(18); CW(19);
    STAMP(2 + 30 * ly);
    __syncthreads();
    STAMP(3 + 30 * ly);
    {
      const int c16 = l & 15, g4 = l >> 4;
      const LAS char* pa = sm + (w >> 2) * KC_CH + (c16 & 7) * 4160 + (3 + (c16 >> 3) - g4) * 16 + (w & 3) * 128;
      const LAS char* pb = sm + L_U + (w >> 2) * U_CH + (30 + 2 * c16 + g4) * 16 - (w & 3) * 128;
      LAS char* pp = sm + L_R + w * 4096 + (w >> 2) * 128 + ((4 * c16 + g4) ^ ((c16 >> 1) & 3)) * 16;
      const int xmv = (tid >> 4) * 32768 + (tid & 15) * 8;
      const LAS char* pr = sm + L_R + ((tid >> 3) & 1) * 16512 + ((((tid >> 4) << 3) | (tid & 7)) ^ ((tid >> 4) & 3)) * 16;
      auto conv_out = [&](int half, f32x4 xr) {
        const LAS char* p = pr + (half ? L_PB - L_R : 0);
        const float ksc = *(const LAS float*)(sm + L_DK + ((tid >> 3) & 1) * 8 + 4);
        const f32x4 y = (*(const LAS f32x4*)p + *(const LAS f32x4*)(p + 4096)) + (*(const LAS f32x4*)(p + 8192) + *(const LAS f32x4*)(p + 12288));
        const f32x4 o = y * ksc + xr;
        ST64((v2u{umin(pack_bf16(o[0], o[1]), 0xfffffffeu), umin(pack_bf16(o[2], o[3]), 0xfffffffeu)}), xmv,
             XM_OFF + ly * (2 << 20) + half * (1 << 20) + 128 * b, SC1);
      };
      {
        f32x4 acc[4];
#pragma unroll
        for (int i = 0; i < 4; ++i) acc[i] = f32x4{0.f, 0.f, 0.f, 0.f};
        v4u fa[2][4], fb[2][4];
#pragma unroll
        for (int tau = 0; tau < 2; ++tau)
#pragma unroll
          for (int i = 0; i < 4; ++i) { fa[tau][i] = *(const LAS v4u*)(pa + i * 512 + tau * 64); fb[tau][i] = *(const LAS v4u*)(pb + i * 512 - tau * 64); }
#pragma unroll
        for (int tau = 0; tau < 2; ++tau)
#pragma unroll
          for (int sx = 0; sx < 4; ++sx) {
#pragma unroll
            for (int e = 0; e + sx < 4; ++e) acc[sx + e] = mfma16h(fa[tau][sx], fb[tau][e], acc[sx + e]);
            if (sx == 0) { SBAR(); CW(20 + tau); }
          }
#pragma unroll
        for (int i = 0; i < 4; ++i) *(LAS f32x4*)(pp + i * 1024) = acc[i];
      }
      v4u fa[8], fb[4];
#pragma unroll
      for (int i = 0; i < 4; ++i) { fb[i] = *(const LAS v4u*)(pb + i * 512); fa[7 - i] = *(const LAS v4u*)(pa + (7 - i) * 512); }
#pragma unroll
      for (int i = 1; i < 4; ++i) fa[i] = *(const LAS v4u*)(pa + i * 512);
      SBAR();
      CW(22); CW(23);
      STAMP(4 + 30 * ly);
      __syncthreads();
      conv_out(0, xr0);
      STAMP(5 + 30 * ly);
      if (ly == 0) {
        LOAD_W(8, 16);
        if constexpr (COMM) {
          f32x4 cs = {0.f, 0.f, 0.f, 0.f};
#pragma unroll
          for (int k = 0; k < 8; ++k) cs += __builtin_bit_cast(f32x4, LD128(l16, CS_OFF + k * (FF * 4) + s * 896, SC1));
          if (l < 56) *(LAS f32x4*)(sm + L_CS + l16) = cs;
        }
      }
      {
        f32x4 acc[4];
#pragma unroll
        for (int i = 0; i < 4; ++i) acc[i] = f32x4{0.f, 0.f, 0.f, 0.f};
#pragma unroll
        for (int e = 0; e < 4; ++e) {
#pragma unroll
          for (int i = 0; i < 4; ++i) acc[i] = mfma16h(fa[4 + i - e], fb[e], acc[i]);
          fb[e] = *(const LAS v4u*)(pb + e * 512 - 64); fa[7 - e] = *(const LAS v4u*)(pa + (7 - e) * 512 + 64);
          SBAR();
          if (e & 1) CW(24 + (e >> 1));
        }
#pragma unroll
        for (int i = 3; i >= 1; --i) fa[i] = *(const LAS v4u*)(pa + i * 512 + 64);
        if (ly) cin_load(1);
        else { asm volatile("" : "=v"(ix), "=v"(im), "=v"(ir)); }
        SBAR();
#pragma unroll
        for (int e = 0; e < 4; ++e) {
#pragma unroll
          for (int i = 0; i < 4; ++i) acc[i] = mfma16h(fa[4 + i - e], fb[e], acc[i]);
          SBAR();
          if (e & 1) CW(26 + (e >> 1));
        }
#pragma unroll
        for (int i = 0; i < 4; ++i) { fa[i] = *(const LAS v4u*)(pa + i * 512); fa[4 + i] = *(const LAS v4u*)(pa + i * 512 + 64); }
        STAMP(8 + 30 * ly);
        if (ly) xr1 = cin_ln(1);
        SBAR();
        CW(28); CW(29);
        STAMP(9 + 30 * ly);
        __syncthreads();
        v4u fc[8];
#pragma unroll
        for (int i = 0; i < 4; ++i) { fc[i] = *(const LAS v4u*)(pb + (4 + i) * 512); fc[4 + i] = *(const LAS v4u*)(pb + (4 + i) * 512 - 64); }
#pragma unroll
        for (int tau = 0; tau < 2; ++tau)
#pragma unroll
          for (int e = 0; e < 4; ++e) {
#pragma unroll
            for (int i = e; i < 4; ++i) acc[i] = mfma16h(fa[4 * tau + i - e], fc[4 * tau + e], acc[i]);
            if (e == 0) { SBAR(); CW(30 + tau); }
          }
#pragma unroll
        for (int i = 0; i < 4; ++i) *(LAS f32x4*)(pp + (L_PB - L_R) + i * 1024) = acc[i];
      }
      STAMP(6 + 30 * ly);
      __syncthreads();
      x_load(0);
      SBAR();
      conv_out(1, xr1);
      STAMP(7 + 30 * ly);
      x_stage(0);
      SBAR();
      if constexpr (!COMM) {
        const u32 on = (u32)opaque_v(-1);
#pragma unroll
        for (int k = 0; k < 4; ++k) ST128((v4u{on, on, on, on}), l16 + (k & 1) * 1024, hoff + (k >> 1) * 14336, SC1);
        SBAR();
      }
    }
    STAMP(11 + 30 * ly);
    __syncthreads();
    tid = opaque_v(threadIdx.x); l = tid & 63; l16 = l * 16;
    const int h = l >> 5;
    const LAS char* xb = sm + L_R + (8 * h + ((l & 15) >> 2)) * 64 + 32 * ((l >> 4) & 1) + 8 * (l & 3);
    auto stats = [&](int half) {
      const LAS char* xq = sm + L_R + half * 32768 + (8 * (l >> 4) + ((l & 15) >> 2)) * 64 + 8 * (l & 3);
      const u32 one = (u32)opaque_v(0x3f803f80);
      const v4u ones = {one, one, one, one};
      f32x4 s0 = {0.f, 0.f, 0.f, 0.f}, q0 = s0, s1 = s0, q1 = s0;
      v4u f0[4], f1[4];
#pragma unroll
      for (int k = 0; k < 4; ++k) { f0[k] = tr_frag(xq + k * 2048); f1[k] = tr_frag(xq + k * 2048 + 32); }
#pragma unroll
      for (int k = 0; k < 16; ++k) {
        s0 = mfma16b(ones, f0[k & 3], s0); q0 = mfma16b(f0[k & 3], f0[k & 3], q0);
        s1 = mfma16b(ones, f1[k & 3], s1); q1 = mfma16b(f1[k & 3], f1[k & 3], q1);
        if (k + 4 < 16) { f0[k & 3] = tr_frag(xq + (k + 4) * 2048); f1[k & 3] = tr_frag(xq + (k + 4) * 2048 + 32); }
        if (half == 0 && k == 9) x_load(1);
        SBAR();
      }
      const float d0 = (l & 2) ? ((l & 1) ? q0[3] : q0[2]) : ((l & 1) ? q0[1] : q0[0]);
      const float d1 = (l & 2) ? ((l & 1) ? q1[3] : q1[2]) : ((l & 1) ? q1[1] : q1[0]);
#pragma unroll
      for (int g = 0; g < 2; ++g) {
        float m, v, mb, r1;
        ln_stats(g ? s1[0] : s0[0], g ? d1 : d0, m, v, mb, r1);
        const u32 p2 = pack_bf16((m - mb) * r1, v * r1 * r1);
        const float r2 = __builtin_amdgcn_rcpf(bfr(__builtin_amdgcn_sqrtf(bfr(bfhi(p2) + 1e-5f))));
        if ((l >> 4) == ((l >> 2) & 3)) *(LAS f32x2*)(sm + L_AB + (half * 32 + 16 * g + (l & 15)) * 8) = f32x2{r1 * r2, (mb * r1 + bflo(p2)) * r2};
      }
    };
    auto epi_quad = [&](const f32x16& acc, int q, f32x2 ab, u32& o0, u32& o1) {
      const f32x4 c4 = *(const LAS f32x4*)(sm + L_CS + (32 * w + 8 * q + 4 * h) * 4);
      const f32x4 b4 = *(const LAS f32x4*)(sm + L_B1 + (32 * w + 8 * q + 4 * h) * 4);
      const f32x4 x = f32x4{acc[4 * q], acc[4 * q + 1], acc[4 * q + 2], acc[4 * q + 3]} * ab.x + (b4 - c4 * ab.y);
      const f32x2 g0 = gelu2(f32x2{x[0], x[1]}), g1 = gelu2(f32x2{x[2], x[3]});
      o0 = umin(pack_bf16(g0.x, g0.y), 0xfffffffeu); o1 = umin(pack_bf16(g1.x, g1.y), 0xfffffffeu);
    };
    f32x16 acc0, acc1;
    constexpr int XD = 3;
    if constexpr (!COMM) {
      v4u xf[XD];
#pragma unroll
      for (int k = 0; k < XD; ++k) xf[k] = tr_frag(xb + k * 1024);
      acc0 = f32x16{};
#pragma unroll
      for (int ks = 0; ks < 32; ++ks) {
        acc0 = mfma32(wf[ks], xf[ks % XD], acc0);
        if (ks + XD < 32) xf[ks % XD] = tr_frag(xb + (ks + XD) * 1024);
        if (ks < 2 * W2A && !(ks & 1)) { const int k = ks >> 1; wa[k] = LD128(l16 + (k & 3) * 1024, w2off + (k >> 2) * 4096, SC1); }
        if (ks == 15) x_load(1);
        SBAR();
      }
      asm volatile("" : "+v"(acc0));
    } else {
      __builtin_amdgcn_s_setprio(2);
      stats(0);
      __builtin_amdgcn_s_setprio(0);
    }
    STAMP(12 + 30 * ly);
    x_stage(1);
    STAMP(13 + 30 * ly);
    __syncthreads();
    STAMP(14 + 30 * ly);
    const int orow = ly ? l : 8 * w + (l >> 3), oq = ly ? w : (l & 7);
    const v2u xres0 = *(const LAS v2u*)(sm + L_R + (64 * s + orow) * 64 + 8 * oq);
    const v2u xres1 = *(const LAS v2u*)(sm + L_XB + (64 * s + orow) * 64 + 8 * oq);
    const int hfo = HF_OFF + ly * 4096 + t * 128;
    if constexpr (!COMM) {
      v4u xf[XD];
#pragma unroll
      for (int k = 0; k < XD; ++k) xf[k] = tr_frag(xb + 32768 + k * 1024);
      const f32x2 ab = *(const LAS f32x2*)(sm + L_AB + (l & 31) * 8);
      u32 o[4];
      acc1 = f32x16{};
#pragma unroll
      for (int ks = 0; ks < 32; ++ks) {
        acc1 = mfma32(wf[ks], xf[ks % XD], acc1);
        if (ks + XD < 32) xf[ks % XD] = tr_frag(xb + 32768 + (ks + XD) * 1024);
        if (ks & 1) { const int j = ks >> 1, k = W2A + j; wf[j] = LD128(l16 + (k & 3) * 1024, w2off + (k >> 2) * 4096, SC1); }
        if (ks % 6 == 1 && ks < 24) { const int q = ks / 6; epi_quad(acc0, q, ab, o[2 * (q & 1)], o[2 * (q & 1) + 1]); }
        if (ks == 11 || ks == 23) ST128((v4u{o[0], o[1], o[2], o[3]}), l16 + (ks / 12) * 1024, hoff, SC1);
        if (ks % 6 == 5 || ks >= 24) SBAR();
      }
      asm volatile("" : "+v"(acc1));
    } else {
      if (l == 0) ST32(1u, 0, hfo + s * 4, SC1);
      __builtin_amdgcn_s_setprio(2);
      stats(1);
      __builtin_amdgcn_s_setprio(0);
#pragma unroll 1
      for (int sp = 0; sp < SPIN_CAP; ++sp) {
        CBAR();
        const u32 f = LD32((l & 7) * 4, hfo, SC1);
        if (__all(f == 1u)) break;
        __builtin_amdgcn_s_sleep(1);
      }
      CBAR();
    }
    STAMP(15 + 30 * ly);
    __syncthreads();
    v4u hf[14];
    const int hin = H_OFF + ly * H_LAYER + (t * 8 + w) * (28 * 1024);
    auto h_load = [&](int half, int lo, int hi) {
#pragma unroll
      for (int k = lo; k < hi; ++k) { hf[k] = LD128(l16, hin + (14 * half + k) * 1024, SC1); SBAR(); }
    };
    h_load(0, 0, 7);
    STAMP(16 + 30 * ly);
    if constexpr (!COMM) {
      const f32x2 ab = *(const LAS f32x2*)(sm + L_AB + (32 + (l & 31)) * 8);
#pragma unroll
      for (int sp = 0; sp < 2; ++sp) {
        u32 o[4];
        epi_quad(acc1, 2 * sp, ab, o[0], o[1]);
        epi_quad(acc1, 2 * sp + 1, ab, o[2], o[3]);
        ST128((v4u{o[0], o[1], o[2], o[3]}), l16 + sp * 1024, hoff + 14336, SC1);
      }
      SBAR();
    }
    h_load(0, 7, 14);
    STAMP(17 + 30 * ly);
    LAS char* rbw = sm + L_R + (w * 64 + (l & 31)) * RB_ROW + h * 16;
    auto g2_out = [&](int half, v2u xres) {
      const LAS char* rp = sm + L_R + orow * RB_ROW + oq * 16;
      f32x4 o4 = unpack4(xres) + *(const LAS float*)(sm + L_B2 + orow * 4);
      f32x4 p4 = *(const LAS f32x4*)rp;
#pragma unroll
      for (int ww = 1; ww < 8; ++ww) { const f32x4 v = *(const LAS f32x4*)(rp + ww * (64 * RB_ROW)); if (ww & 1) o4 += v; else p4 += v; }
      o4 += p4;
      u32 o0 = pack_bf16(o4[0], o4[1]), o1 = pack_bf16(o4[2], o4[3]);
      if (ly) {
        u16* op = out + (1024 * half + 32 * t + 4 * w) * DM + 64 * s + l;
        op[0] = (u16)(o0 & 0xffffu); op[DM] = (u16)(o0 >> 16); op[2 * DM] = (u16)(o1 & 0xffffu); op[3 * DM] = (u16)(o1 >> 16);
      } else {
        o0 = umin(o0, 0xfffffffeu); o1 = umin(o1, 0xfffffffeu);
        ST64((v2u{o0, o1}), (tid >> 3) * 4096 + (tid & 7) * 8, XC_OFF + (2 << 20) + s * (64 * 4096) + t * 64 + half * 2048, SC1);
        const float x0 = bflo(o0), x1 = bfhi(o0), x2 = bflo(o1), x3 = bfhi(o1);
        const float a0 = swap32_add(x0, x2), a1 = swap32_add(x0 * x0, x2 * x2), a2 = swap32_add(x1, x3), a3 = swap32_add(x1 * x1, x3 * x3);
        const float sv = dpp_add<0x128>(swap16_add(a0, a2)), qv = dpp_add<0x128>(swap16_add(a1, a3));
        if ((l & 8) == 0) *(LAS f32x2*)(sm + L_SP + ((half * 8 + w) * 32 + 4 * (l & 7) + (l >> 4)) * 8) = f32x2{sv, qv};
      }
    };
    auto pst_out = [&](int half) {
      float v = 0.f;
#pragma unroll
      for (int k = 0; k < 8; ++k) v += *(const LAS float*)(sm + L_SP + (half * 8 + k) * 256 + l * 4);
      ST64((v2u{__builtin_bit_cast(u32, v), (u32)opaque_v(1)}), (l >> 3) * 512 + (l & 7) * 8, PST_OFF + (t * 2 + half) * 4096 + s * 64, SC1);
    };
#define W2FRAG(k) (COMM ? wf[k] : (k) < W2A ? wa[(k) < W2A ? (k) : 0] : wf[(k) < W2A ? 0 : (k) - W2A])
    auto gemm2 = [&](int half) {
      f32x16 a0, a1;
#pragma unroll 1
      for (int sp = 0; sp < SPIN_CAP; ++sp) {
        a0 = f32x16{}; a1 = f32x16{};
        u32 mx = 0;
#pragma unroll
        for (int k = 0; k < 14; ++k) {
          a0 = mfma32(hf[k], W2FRAG(2 * k), a0); a1 = mfma32(hf[k], W2FRAG(2 * k + 1), a1);
          mx = umax(mx, max4(hf[k]));
        }
        if (__all(mx != 0xffffffffu)) break;
        STAMP(10 + 8 * half + 30 * ly);
        __builtin_amdgcn_s_sleep(2);
        CBAR();
        h_load(half, 0, 14);
      }
      STAMP(19 + 4 * half + 30 * ly);
      if (!COMM && half == 0) h_load(1, 0, 14);
      if (half == 0) STAMP(20 + 30 * ly);
#pragma unroll
      for (int q = 0; q < 4; ++q) {
        *(LAS f32x4*)(rbw + q * 32) = f32x4{a0[4 * q], a0[4 * q + 1], a0[4 * q + 2], a0[4 * q + 3]};
        *(LAS f32x4*)(rbw + 32 * RB_ROW + q * 32) = f32x4{a1[4 * q], a1[4 * q + 1], a1[4 * q + 2], a1[4 * q + 3]};
      }
      if constexpr (!COMM) {
        if (half == 1) {
          SBAR();
          if (ly == 0) {
            if (w == 0) {
#pragma unroll 1
              for (int sp = 0; sp < 3; ++sp) { CBAR(); fin_load(0); fin0 = fin_try(0); if (fin0) break; }
            }
            LOAD_W(0, 16);
          } else {
#pragma unroll
            for (int i = 0; i < 16; ++i) asm volatile("" : "=v"(wf[i]));
          }
        }
      }
    };
    gemm2(0);
    __syncthreads();
    if constexpr (COMM) h_load(1, 0, 14);
    STAMP(21 + 30 * ly);
    g2_out(0, xres0);
    STAMP(22 + 30 * ly);
    __syncthreads();
    if (COMM && ly == 0) pst_out(0);
    gemm2(1);
    __syncthreads();
    STAMP(24 + 30 * ly);
    g2_out(1, xres1);
    STAMP(25 + 30 * ly);
    if (ly == 0) {
      cin_load(0);
      __syncthreads();
      if constexpr (COMM) { pst_out(1); fin_wait(1); }
      else if (!fin0) { STAMP(28); fin_wait(0); }
      STAMP(26 + 30 * ly);
      xr0 = cin_ln(0);
      STAMP(27 + 30 * ly);
    } else {
      asm volatile("" : "=v"(xr0));
    }
    asm volatile("" : "=v"(xr1));
  }
  STAMP_FLUSH(1);
}

__global__ __launch_bounds__(512, 2) void k_main(const Args a) {
  extern __shared__ __attribute__((aligned(16))) char smem[];
  LAS char* sm = (LAS char*)smem;
  const int b0 = blockIdx.x, w0 = __builtin_amdgcn_readfirstlane(threadIdx.x >> 6);
  const __amdgpu_buffer_rsrc_t rs = __builtin_amdgcn_make_buffer_rsrc(a.ws, 0, WS_BYTES, 0x00020000);
  if (w0 == 7) main_body<true>(sm, rs, a, b0, w0);
  else main_body<false>(sm, rs, a, b0, w0);
}

__global__ __launch_bounds__(512) void k_init(v4u* ws) {
  ws[threadIdx.x] = v4u{~0u, ~0u, ~0u, ~0u};
}

extern "C" void kernel_launch(void* const* d_in, const int* in_sizes, int n_in, void* d_out, int out_size, void* d_ws,
                              size_t ws_size, hipStream_t stream) {
  (void)hipFuncSetAttribute((const void*)k_main, hipFuncAttributeMaxDynamicSharedMemorySize, MAIN_LDS);
  if (!HANDSHAKE) k_init<<<1, 512, 0, stream>>>((v4u*)d_ws);
  Args a;
  a.x0 = (const u16*)d_in[0]; a.logA = (const u16*)d_in[1]; a.Aim = (const u16*)d_in[2]; a.Cre = (const u16*)d_in[3];
  a.Cim = (const u16*)d_in[4]; a.logdt = (const u16*)d_in[5]; a.dskip = (const u16*)d_in[6]; a.w1 = (const u16*)d_in[8];
  a.b1 = (const u16*)d_in[9]; a.w2 = (const u16*)d_in[10]; a.b2 = (const u16*)d_in[11];
  a.ws = (char*)d_ws; a.out = (u16*)d_out;
  k_main<<<256, 512, MAIN_LDS, stream>>>(a);
}
```

```cpp
#include <hip/hip_runtime.h>
#include <hip/hip_bf16.h>

typedef unsigned int u32;
typedef unsigned short u16;
typedef __attribute__((ext_vector_type(4))) u32 v4u;
typedef __attribute__((ext_vector_type(2))) u32 v2u;
typedef __attribute__((ext_vector_type(4))) float f32x4;
typedef __attribute__((ext_vector_type(2))) float f32x2;
typedef __attribute__((ext_vector_type(16))) float f32x16;
typedef __attribute__((ext_vector_type(8))) __bf16 bf16x8;
typedef __attribute__((ext_vector_type(2))) __bf16 bf16x2;
typedef __attribute__((ext_vector_type(8))) _Float16 f16x8;
typedef __attribute__((ext_vector_type(2))) _Float16 f16x2;
typedef __attribute__((ext_vector_type(4))) short s16x4;
#define LAS __attribute__((address_space(3)))
#define DI __device__ __forceinline__
#define PHASE() __builtin_amdgcn_sched_barrier(0)

constexpr int SL = 2048, DM = 512, NS = 128, FF = 1792;
constexpr int CNT_OFF = 0;
constexpr int HF_OFF = CNT_OFF + 16384;
constexpr int FST_OFF = 65536;
constexpr int CS_OFF = FST_OFF + 32768;
constexpr int PST_OFF = CS_OFF + 65536;
constexpr int XC_OFF = 1 << 20;
constexpr int XM_OFF = 5 << 20;
constexpr int W1F_OFF = 9 << 20;
constexpr int W2F_OFF = 11 << 20;
constexpr int H_OFF = 13 << 20;
constexpr int H_LAYER = 32 * 8 * 2 * 14 * 1024;
constexpr int WS_BYTES = 28 << 20;
constexpr int SC1 = 16;
constexpr int SPIN_CAP = 1 << 16;

DI u32 umin(u32 a, u32 b) { return a < b ? a : b; }
DI u32 umax(u32 a, u32 b) { return a > b ? a : b; }
DI float bf2f(u32 h) { return __builtin_bit_cast(float, h << 16); }
DI float bflo(u32 p) { return __builtin_bit_cast(float, p << 16); }
DI float bfhi(u32 p) { return __builtin_bit_cast(float, p & 0xffff0000u); }
DI u32 pack_bf16(float a, float b) { bf16x2 v = {(__bf16)a, (__bf16)b}; return __builtin_bit_cast(u32, v); }
DI float bfr(float a) { return bflo(pack_bf16(a, a)); }
DI u32 pack_f16(float a, float b) { f16x2 v = {(_Float16)a, (_Float16)b}; return __builtin_bit_cast(u32, v); }
DI float dot2bf(u32 a, u32 b, float c) {
  return __builtin_amdgcn_fdot2_f32_bf16(__builtin_bit_cast(bf16x2, a), __builtin_bit_cast(bf16x2, b), c, false);
}
DI f32x16 mfma32(v4u a, v4u b, f32x16 c) {
  return __builtin_amdgcn_mfma_f32_32x32x16_bf16(__builtin_bit_cast(bf16x8, a), __builtin_bit_cast(bf16x8, b), c, 0, 0, 0);
}
DI f32x4 mfma16h(v4u a, v4u b, f32x4 c) {
  return __builtin_amdgcn_mfma_f32_16x16x32_f16(__builtin_bit_cast(f16x8, a), __builtin_bit_cast(f16x8, b), c, 0, 0, 0);
}
DI f32x4 mfma16b(v4u a, v4u b, f32x4 c) {
  return __builtin_amdgcn_mfma_f32_16x16x32_bf16(__builtin_bit_cast(bf16x8, a), __builtin_bit_cast(bf16x8, b), c, 0, 0, 0);
}
DI v4u tr_frag(const LAS char* p) {
  const s16x4 a = __builtin_amdgcn_ds_read_tr16_b64_v4i16((LAS s16x4*)p);
  const s16x4 c = __builtin_amdgcn_ds_read_tr16_b64_v4i16((LAS s16x4*)(p + 256));
  return __builtin_bit_cast(v4u, __builtin_shufflevector(a, c, 0, 1, 2, 3, 4, 5, 6, 7));
}
template <int CTRL> DI float dpp_add(float v) {
  const int o = __builtin_amdgcn_update_dpp(0, __builtin_bit_cast(int, v), CTRL, 0xf, 0xf, false);
  return v + __builtin_bit_cast(float, o);
}
DI float row_sum16(float v) { v = dpp_add<0x128>(v); v = dpp_add<0x124>(v); v = dpp_add<0x122>(v); return dpp_add<0x121>(v); }
DI float swap32_add(float a, float c) {
  const auto r = __builtin_amdgcn_permlane32_swap(__builtin_bit_cast(u32, a), __builtin_bit_cast(u32, c), false, false);
  const u32 r0 = r[0], r1 = r[1];
  return __builtin_bit_cast(float, r0) + __builtin_bit_cast(float, r1);
}
DI float swap16_add(float a, float c) {
  const auto r = __builtin_amdgcn_permlane16_swap(__builtin_bit_cast(u32, a), __builtin_bit_cast(u32, c), false, false);
  const u32 r0 = r[0], r1 = r[1];
  return __builtin_bit_cast(float, r0) + __builtin_bit_cast(float, r1);
}
DI float wave_sum(float v) { v = row_sum16(v); v = swap16_add(v, v); return swap32_add(v, v); }
DI void ln_stats(float s, float q, float& m, float& v, float& mb, float& rs) {
  m = s * (1.0f / DM);
  v = fmaxf(q * (1.0f / DM) - m * m, 0.f);
  const u32 pk = pack_bf16(m, __builtin_amdgcn_sqrtf(bfr(bfr(v) + 1e-5f)));
  mb = bflo(pk); rs = __builtin_amdgcn_rcpf(bfhi(pk));
}
DI u32 npz(float a) { return umin(__builtin_bit_cast(u32, a), 0xfffffffeu); }
#define LD128(vo, so, aux) __builtin_amdgcn_raw_buffer_load_b128(rs, (vo), (so), (aux))
#define LD64(vo, so, aux) __builtin_amdgcn_raw_buffer_load_b64(rs, (vo), (so), (aux))
#define LD32(vo, so, aux) __builtin_amdgcn_raw_buffer_load_b32(rs, (vo), (so), (aux))
#define ST128(v, vo, so, aux) __builtin_amdgcn_raw_buffer_store_b128((v), rs, (vo) + (so), 0, (aux))
#define ST64(v, vo, so, aux) __builtin_amdgcn_raw_buffer_store_b64((v), rs, (vo), (so), (aux))
#define ST32(v, vo, so, aux) __builtin_amdgcn_raw_buffer_store_b32((v), rs, (vo), (so), (aux))
#define DRAIN() asm volatile("s_waitcnt vmcnt(0)" ::: "memory")
#define CBAR() asm volatile("" ::: "memory")
#define SBAR() __builtin_amdgcn_sched_barrier(0)
DI int opaque_v(int x) { asm volatile("" : "+v"(x)); return x; }
template <typename T, int N> DI void undef_regs(T (&a)[N]) {
#pragma unroll
  for (int i = 0; i < N; ++i) asm volatile("" : "=v"(a[i]));
}
DI int opaque_s(int x) { asm volatile("" : "+s"(x)); return x; }

constexpr int KT_REL = 24576 + 16;
constexpr int KT_CH = 1312;
DI void cexp_p(float arl, float rh, float rl, float p, float& re, float& im) {
  const float e = __builtin_amdgcn_exp2f(arl * p);
  const float r = __builtin_fmaf(rl, p, __builtin_amdgcn_fractf(rh * p));
  re = e * __builtin_amdgcn_cosf(r);
  im = e * __builtin_amdgcn_sinf(r);
}
DI float k_scale(float bs, int sign) {
  const int ex = (int)((__builtin_bit_cast(u32, bs) >> 23) & 0xff) - 126;
  int e = 15 - ex; e = e > 100 ? 100 : (e < -100 ? -100 : e);
  return __builtin_bit_cast(float, (u32)(127 + sign * e) << 23);
}
constexpr int KC_CH = 8 * 260 * 16;
constexpr int L_U = 2 * KC_CH;
constexpr int U_CH = 4672;
constexpr int L_R = L_U + 2 * U_CH;
constexpr int L_XB = L_R + 32768;
constexpr int L_PB = L_XB + 256;
constexpr int L_KT = L_R + KT_REL;
constexpr int L_PRM = L_XB + 16384;
constexpr int L_BND = L_PRM + 5120;
constexpr int RB_ROW = 144;
constexpr int L_S = L_R + 8 * 64 * RB_ROW;
constexpr int L_B1 = L_S;
constexpr int L_CS = L_B1 + 896;
constexpr int L_SP = L_CS + 896;
constexpr int L_AC = L_SP + 4096;
constexpr int L_B2 = L_AC + 64;
constexpr int L_DK = L_B2 + 256;
constexpr int L_AB = L_DK + 16;
constexpr int L_END = L_AB + 512;
constexpr int MAIN_LDS = L_END;
#define MAP_T(b) ((((b) >> 1) & 3) * 8 + ((b) >> 5))
#define MAP_S(b) (((b) & 1) * 4 + (((b) >> 3) & 3))
constexpr int W2A = 12;

DI f32x2 gelu2(f32x2 x) {
  const f32x2 t = x * (x * x * -0.10294324f + -2.3022082f);
  f32x2 e; e.x = __builtin_amdgcn_exp2f(t.x); e.y = __builtin_amdgcn_exp2f(t.y);
  e = e + 1.0f;
  f32x2 r; r.x = __builtin_amdgcn_rcpf(e.x); r.y = __builtin_amdgcn_rcpf(e.y);
  return x * r;
}
DI u32 max4(v4u a) { return umax(umax(a[0], a[1]), umax(a[2], a[3])); }
DI f32x4 unpack4(v2u p) { const u32 a = p[0], c = p[1]; return f32x4{bflo(a), bfhi(a), bflo(c), bfhi(c)}; }
DI f32x4 conv_ln(LAS char* dst, v2u xp, v4u mm, v4u rr, float dk) {
  const f32x4 x = unpack4(xp);
  const f32x4 u = (x - __builtin_bit_cast(f32x4, mm)) * __builtin_bit_cast(f32x4, rr);
  *(LAS v2u*)dst = v2u{pack_f16(u[0], u[1]), pack_f16(u[2], u[3])};
  return x + u * dk;
}

constexpr u32 CODE_BYTES = 42u * 1024u;
struct Args {
  const u16 *x0, *logA, *Aim, *Cre, *Cim, *logdt, *dskip, *w1, *b1, *w2, *b2;
  char* ws;
  u16* out;
};
template <bool COMM>
DI void main_body(LAS char* sm, const Args& a, int b0, int w0) {
  unsigned long long pc;
  asm volatile("s_getpc_b64 s[90:91]\n\ts_add_u32 s90, s90, _Z6k_main4Args@rel32@lo+4\n\ts_addc_u32 s91, s91, _Z6k_main4Args@rel32@hi+12\n\ts_mov_b64 %0, s[90:91]"
               : "=s"(pc) :: "s90", "s91", "scc");
  const u32 warm = *(const __attribute__((address_space(1))) u32*)(pc + umin((u32)opaque_v(threadIdx.x) * 128u, CODE_BYTES));
  SBAR();
  const __amdgpu_buffer_rsrc_t rs = __builtin_amdgcn_make_buffer_rsrc(a.ws, 0, WS_BYTES, 0x00020000);
  u16* const out = a.out;
  PHASE();
  v4u wf[32], wa[W2A];
  f32x4 xr0, xr1;
#define LOAD_W(lo, hi) do { _Pragma("unroll") for (int ks = (lo); ks < (hi); ++ks) { \
      if constexpr (!COMM) wf[ks] = LD128(l16 + (ks & 3) * 1024, W1F_OFF + (7 * s + w) * 32768 + (ks >> 2) * 4096, SC1); \
      else if (ks < 28) wf[ks] = LD128(l16 + (ks & 3) * 1024, W2F_OFF + (s * 8 + 7) * 28672 + (ks >> 2) * 4096, SC1); } \
      SBAR(); } while (0)
#define CW(k) do { if (!COMM || ly == 0) LOAD_W(k, (k) + 1); } while (0)
  {
    const int b = opaque_s(b0), w = opaque_s(w0), tid = opaque_v(threadIdx.x), l = tid & 63, l16 = l * 16, s = MAP_S(b);
    const int cc = (tid >> 3) & 1, q8 = (tid >> 4) * 8 + (tid & 7);
    const int kk = l >> 4, i16 = l & 15, t = MAP_T(b);
    u32 rla, rai, rcr, rci, rdt, hs_c = 0;
    v4u xrow[2], xtile[2], wr[4];
    const int fr = (b < 112 ? b : b - 112) * 16 + 4 * (w & 3);
    if (!COMM && w < 4) {
      const int o = 2 * b * NS + tid;
      rla = a.logA[o]; rai = a.Aim[o]; rcr = a.Cre[o]; rci = a.Cim[o]; rdt = a.logdt[2 * b + (w >> 1)];
      undef_regs(wr); undef_regs(xrow); undef_regs(xtile);
    } else {
      asm volatile("" : "=v"(rla), "=v"(rai), "=v"(rcr), "=v"(rci), "=v"(rdt));
      if (!COMM && w == 4) hs_c = LD32(0, CNT_OFF + 8192 + b * 4, SC1);
#pragma unroll
      for (int i = 0; i < 2; ++i) {
        xrow[i] = *(const v4u*)(a.x0 + (8 * b + 2 * (w & 3) + i) * DM + 8 * l);
        xtile[i] = *(const v4u*)(a.x0 + (64 * (b >> 3) + 32 * i + (l >> 1)) * DM + 64 * (b & 7) + 16 * (w & 3) + 8 * (l & 1));
      }
      if (b < 112) {
        const u16* p = a.w1 + (16 * (fr & 31) + (l >> 2)) * FF + 32 * (fr >> 5) + 8 * (l & 3);
#pragma unroll
        for (int f = 0; f < 4; ++f) wr[f] = *(const v4u*)(p + 16 * f * FF);
      } else {
        const int r1 = (b < 224 ? fr : 0) >> 1, ks2 = r1 % 14, r2 = r1 / 14;
        const u16* p = a.w2 + (224 * (r2 & 7) + 16 * ks2 + (l >> 2)) * DM + 64 * (r2 >> 3) + 8 * (l & 3);
#pragma unroll
        for (int f = 0; f < 4; ++f) wr[f] = *(const v4u*)(p + 16 * (f >> 1) * DM + 32 * (f & 1));
      }
    }
    const float dk = bf2f(a.dskip[2 * b + cc]);
    const float bias2 = bf2f(a.b2[64 * s + l]);
    const float bb1 = bf2f(a.b1[224 * s + (tid < 224 ? tid : 0)]);
    SBAR();
    u32 hs_q = 0;
    if (COMM && b == 255) {
      const v4u d = LD128(l16, CNT_OFF, SC1);
      const u32 qo = LD32(0, CNT_OFF + 4096, SC1);
      u32 q = qo + 1u;
#pragma unroll 1
      for (int k = 0; k < 300; ++k) { if (!__any(d[0] == q || d[1] == q || d[2] == q || d[3] == q)) break; ++q; }
      hs_q = __builtin_amdgcn_readfirstlane(q);
      if (l < 8) ST32(hs_q, l * 128, CNT_OFF + 4096, SC1);
    }
    if (tid < 16) { *(LAS v4u*)(sm + (tid >> 3) * KC_CH + (tid & 7) * 4160) = v4u{0u, 0u, 0u, 0u}; *(LAS u32*)(sm + L_AC + tid * 4) = 0u; }
    if (tid >= 64 && tid < 128) *(LAS v4u*)(sm + L_U + ((tid >> 5) & 1) * U_CH + (tid & 31) * 16) = v4u{0u, 0u, 0u, 0u};
    if (tid >= 128 && tid < 130) *(LAS v4u*)(sm + L_KT - 16 + (tid - 128) * KT_CH) = v4u{0u, 0u, 0u, 0u};
    const int c = tid >> 8, x = tid & 255;
    int tix, gix[8];
    {
      const int col = x >> 4, row = x & 15;
      const int p0 = 128 * (row >> 3) + (row & 7) + 8 * col;
      const int p1 = (row < 8 ? 256 + 128 * (row >> 2) + 2 * (row & 3) : 512 + 128 * ((row - 8) >> 1) + 4 * (row & 1)) + 8 * col;
      const int c1 = row < 8 ? 128 + (p1 >> 1) : 256 + (p1 >> 2);
      const int c2 = row < 8 ? 512 + 16 * row + col : (x == 8 ? 640 : 641);
      tix = opaque_v(p0 | (c1 << 10) | (c2 << 20));
#pragma unroll
      for (int j = 0; j < 8; ++j) {
        const int la = 8 * x + 7 - 2 * j, lb = la - 1;
        const u32 hb = pack_bf16((float)la, (float)lb);
        const int ia = la < 128 ? la : (int)(hb & 0xffffu) - 17024, ib = lb < 128 ? lb : (int)(hb >> 16) - 17024;
        gix[j] = opaque_v((KT_REL + c * KT_CH + 2 * ia) | ((KT_REL + c * KT_CH + 2 * ib) << 16));
      }
    }
    const float pB = (float)(8 * i16);
    const float pA0 = (float)(128 * (i16 >> 3) + (i16 & 7));
    const float pA1 = (float)(i16 < 8 ? 256 + 128 * (i16 >> 2) + 2 * (i16 & 3) : 512 + 128 * ((i16 - 8) >> 1) + 4 * (i16 & 1));
    const float pA2 = (float)(i16 < 8 ? 1024 + 128 * i16 : 2048);
    f32x4 acc0 = {0.f, 0.f, 0.f, 0.f}, acc1 = acc0, acc2 = acc0;
    PHASE();
    if (!COMM && w < 4) {
      const float dt = bfr(expf(bf2f(rdt)));
      const float arl = -bfr(expf(bf2f(rla))) * dt * 1.4426950408889634f;
      double tt = (double)(bf2f(rai) * dt) * 0.15915494309189533577;
      tt -= __builtin_rint(tt);
      const float rh = __builtin_bit_cast(float, __builtin_bit_cast(u32, (float)tt) & 0xfffff000u);
      const float rl = (float)(tt - (double)rh);
      const float cr = bf2f(rcr) * dt * 2.0f, ci = bf2f(rci) * dt * 2.0f;
      LAS float* q = (LAS float*)(sm + L_PRM + (tid >> 2) * 80) + (tid & 3);
      q[0] = arl; q[4] = rh; q[8] = rl; q[12] = cr; q[16] = ci;
      const float bs = wave_sum(fabsf(cr) + fabsf(ci));
      if (l == 0) *(LAS float*)(sm + L_BND + w * 4) = bs;
    }
    PHASE();
    __syncthreads();
    PHASE();
    if (COMM || w >= 4) {
      __builtin_amdgcn_s_setprio(3);
      if (!COMM && w == 4 && l == 0) { ST32(hs_c + 1u, 0, CNT_OFF + 12288 + b * 4, SC1); *(LAS u32*)(sm + L_AC + 12) = hs_c + 1u; }
      {
        const u32 on = (u32)opaque_v(-1);
        const v4u on4 = {on, on, on, on};
        const int i8 = tid & 255;
#pragma unroll
        for (int k = 0; k < 2; ++k) ST128(on4, (i8 >> 3) * 32768 + (i8 & 7) * 16, XM_OFF + k * (1 << 20) + 128 * b, SC1);
        if (i8 < 64) ST128(on4, (i8 >> 2) * 512 + (i8 & 3) * 16, PST_OFF + t * 8192 + s * 64, SC1);
        else if (i8 < 68) ST128(on4, ((i8 >> 1) & 1) * 8192 + (i8 & 1) * 16, FST_OFF + 16384 + (8 * t + s) * 32, SC1);
        else if (i8 < 70) ST32(on, (i8 & 1) * 4096, HF_OFF + t * 128 + s * 4, SC1);
        SBAR();
      }
#pragma unroll
      for (int i = 0; i < 2; ++i) {
        float sx = 0.f, qx = 0.f;
#pragma unroll
        for (int j = 0; j < 4; ++j) { const u32 p = xrow[i][j]; sx = dot2bf(p, 0x3f803f80u, sx); qx = dot2bf(p, p, qx); }
        *(LAS v4u*)(sm + L_XB + (w & 3) * 4096 + i * 1024 + l16) = xtile[i];
        const float r = row_sum16(swap16_add(swap32_add(sx, qx), swap32_add(sx, qx)));
        const auto rr = __builtin_amdgcn_permlane32_swap(__builtin_bit_cast(u32, r), __builtin_bit_cast(u32, r), false, false);
        const u32 rs0 = rr[0], rs1 = rr[1];
        float mm, vv, mb, rcs;
        ln_stats(__builtin_bit_cast(float, rs0), __builtin_bit_cast(float, rs1), mm, vv, mb, rcs);
        if (l < 2) ST32(__builtin_bit_cast(u32, l ? rcs : mb), l * 16, FST_OFF + (2 * b + ((w >> 1) & 1)) * 32 + (2 * (w & 1) + i) * 4, SC1);
      }
      {
        const LAS char* tp = sm + L_XB + (w & 3) * 4096 + (16 * kk + (i16 >> 2)) * 32 + (l & 3) * 8;
        const s16x4 t0 = __builtin_amdgcn_ds_read_tr16_b64_v4i16((LAS s16x4*)tp), t1 = __builtin_amdgcn_ds_read_tr16_b64_v4i16((LAS s16x4*)(tp + 128));
        const s16x4 t2 = __builtin_amdgcn_ds_read_tr16_b64_v4i16((LAS s16x4*)(tp + 256)), t3 = __builtin_amdgcn_ds_read_tr16_b64_v4i16((LAS s16x4*)(tp + 384));
        const int so = XC_OFF + (64 * (b & 7) + 16 * (w & 3)) * 4096 + (b >> 3) * 128;
        ST128(__builtin_bit_cast(v4u, __builtin_shufflevector(t0, t1, 0, 1, 2, 3, 4, 5, 6, 7)), i16 * 4096 + kk * 32, so, SC1);
        ST128(__builtin_bit_cast(v4u, __builtin_shufflevector(t2, t3, 0, 1, 2, 3, 4, 5, 6, 7)), i16 * 4096 + kk * 32 + 16, so, SC1);
      }
      if (b < 224) {
        LAS char* wl = sm + L_XB + (w & 3) * 4096;
#pragma unroll
        for (int f = 0; f < 4; ++f) *(LAS v4u*)(wl + f * 1024 + l16) = wr[f];
        const int hs = b < 112 ? 512 : 256, ts = b < 112 ? 256 : 512;
        const LAS char* rp = wl + (l >> 5) * hs + ((l & 15) >> 2) * 64 + 32 * ((l >> 4) & 1) + 8 * (l & 3);
        float cs = 0.f;
#pragma unroll
        for (int f = 0; f < 4; ++f) {
          const s16x4 t0 = __builtin_amdgcn_ds_read_tr16_b64_v4i16((LAS s16x4*)(rp + f * 1024)), t1 = __builtin_amdgcn_ds_read_tr16_b64_v4i16((LAS s16x4*)(rp + f * 1024 + ts));
          const v4u o = __builtin_bit_cast(v4u, __builtin_shufflevector(t0, t1, 0, 1, 2, 3, 4, 5, 6, 7));
#pragma unroll
          for (int j = 0; j < 4; ++j) { const u32 p = o[j]; cs = dot2bf(p, 0x3f803f80u, cs); }
          ST128(o, l16 + f * 1024, (b < 112 ? W1F_OFF : W2F_OFF) + fr * 1024, SC1);
        }
        cs = swap32_add(cs, cs);
        if (b < 112 && l < 32) ST32(__builtin_bit_cast(u32, cs), l * 4, CS_OFF + ((b & 1) * 4 + (w & 3)) * (FF * 4) + (b >> 1) * 128, SC1);
      }
      if (!(COMM && b == 255)) __builtin_amdgcn_s_setprio(1);
    }
    PHASE();
    const f32x2 bn = *(const LAS f32x2*)(sm + L_BND + (w >> 2) * 8);
    const float sc = k_scale(bn.x + bn.y, 1);
#pragma unroll
    for (int g = 0; g < 2; ++g) {
      v4u fa0, fa1, fa2, fbh, fbl;
#pragma unroll
      for (int j = 0; j < 4; ++j) {
        const LAS float* pq = (const LAS float*)(sm + L_PRM + w * 640 + g * 320 + kk * 80) + j;
        const float arl = pq[0], rh = pq[4], rl = pq[8], cr = pq[12] * sc, ci = pq[16] * sc;
        float br, bi, er, ei;
        cexp_p(arl, rh, rl, pB, br, bi);
        bi = -bi;
        const u32 hi = pack_f16(br, bi);
        const f16x2 hv = __builtin_bit_cast(f16x2, hi);
        fbh[j] = hi;
        fbl[j] = pack_f16(br - (float)hv.x, bi - (float)hv.y);
        cexp_p(arl, rh, rl, pA0, er, ei);
        fa0[j] = pack_f16(cr * er - ci * ei, cr * ei + ci * er);
        cexp_p(arl, rh, rl, pA1, er, ei);
        fa1[j] = pack_f16(cr * er - ci * ei, cr * ei + ci * er);
        cexp_p(arl, rh, rl, pA2, er, ei);
        fa2[j] = pack_f16(cr * er - ci * ei, cr * ei + ci * er);
        if (g == 0 && j == 2 && (COMM || w >= 4)) {
          SBAR();
          DRAIN();
          u32 old = 0;
          if (l == 0) old = __hip_atomic_fetch_add((LAS u32*)(sm + L_AC), 1u, __ATOMIC_RELAXED, __HIP_MEMORY_SCOPE_WORKGROUP);
          if (__builtin_amdgcn_readfirstlane(old) == 3u) {
            const u32 q = LD32(0, CNT_OFF + 4096 + (b & 7) * 128, SC1);
            if (l == 0) { ST32(q, 0, CNT_OFF + b * 4, SC1); *(LAS u32*)(sm + L_AC + 8) = q; }
          }
          if (!(COMM && b == 255)) __builtin_amdgcn_s_setprio(0);
          PHASE();
        }
      }
      acc0 = mfma16h(fa0, fbh, acc0); acc1 = mfma16h(fa1, fbh, acc1); acc2 = mfma16h(fa2, fbh, acc2);
      acc0 = mfma16h(fa0, fbl, acc0); acc1 = mfma16h(fa1, fbl, acc1); acc2 = mfma16h(fa2, fbl, acc2);
    }
    *(LAS f32x4*)(sm + L_R + w * 3072 + i16 * 64 + kk * 16) = acc0;
    *(LAS f32x4*)(sm + L_R + w * 3072 + 1024 + i16 * 64 + kk * 16) = acc1;
    *(LAS f32x4*)(sm + L_R + w * 3072 + 2048 + i16 * 64 + kk * 16) = acc2;
    { const int o = tid < 224 ? tid * 4 : L_AC + 32 - L_B1; *(LAS float*)(sm + L_B1 + o) = bb1; }
    *(LAS float*)(sm + L_B2 + l * 4) = bias2;
    asm volatile("" :: "v"(warm));
    if (COMM && b == 255) {
#pragma unroll 1
      for (int sp = 0; sp < SPIN_CAP; ++sp) {
        CBAR();
        const v4u d = LD128(l16, CNT_OFF, SC1);
        const u32 own = __hip_atomic_load((LAS u32*)(sm + L_AC), __ATOMIC_RELAXED, __HIP_MEMORY_SCOPE_WORKGROUP);
        if (__all(d[0] == hs_q && d[1] == hs_q && d[2] == hs_q && (l == 63 ? own == 4u : d[3] == hs_q))) break;
      }
      CBAR();
      const v4u cw = LD128(l16, CNT_OFF + 12288, SC1);
      ST128(cw, l16, CNT_OFF + 8192, SC1);
      __builtin_amdgcn_s_setprio(0);
      PHASE();
    }
    PHASE();
    __syncthreads();
    {
      const LAS float* pp = (const LAS float*)(sm + L_R + c * 12288) + x;
      LAS _Float16* kt = (LAS _Float16*)(sm + L_KT + c * KT_CH);
      kt[tix & 1023] = (_Float16)((pp[0] + pp[768]) + (pp[1536] + pp[2304]));
      kt[(tix >> 10) & 1023] = (_Float16)((pp[256] + pp[1024]) + (pp[1792] + pp[2560]));
      kt[tix >> 20] = (_Float16)((pp[512] + pp[1280]) + (pp[2048] + pp[2816]));
    }
    __syncthreads();
    PHASE();
    {
      u32 wd[8];
#pragma unroll
      for (int j = 0; j < 8; ++j)
        wd[j] = (u32)*(const LAS u16*)(sm + L_R + (gix[j] & 0xffff)) | ((u32)*(const LAS u16*)(sm + L_R + ((u32)gix[j] >> 16)) << 16);
#pragma unroll
      for (int cp = 0; cp < 8; ++cp) {
        const int sg = 7 - cp;
        v4u o;
#pragma unroll
        for (int k = 0; k < 4; ++k)
          o[k] = (sg & 1) ? __builtin_amdgcn_alignbit(wd[(sg + 1) / 2 + k], wd[(sg - 1) / 2 + k], 16) : wd[sg / 2 + k];
        *(LAS v4u*)(sm + c * KC_CH + cp * 4160 + (x + 1) * 16) = o;
      }
    }
    {
      const u32 on = (u32)opaque_v(-1);
      const v4u on4 = {on, on, on, on};
      ST128(on4, (tid >> 3) * 32768 + (tid & 7) * 16, XM_OFF + (2 << 20) + 128 * b, SC1);
      ST128(on4, ((tid >> 2) & 63) * 4096 + (tid >> 8) * 2048 + (tid & 3) * 16, XC_OFF + (2 << 20) + s * (64 * 4096) + t * 64, SC1);
    }
    PHASE();
    if (!COMM && w == 0) {
      u32 qp = *(const LAS u32*)(sm + L_AC + 8);
      hs_c = *(const LAS u32*)(sm + L_AC + 12);
#pragma unroll 1
      for (int sp = 0; sp < SPIN_CAP; ++sp) {
        CBAR();
        const u32 r = LD32(0, CNT_OFF + 8192 + b * 4, SC1), q = LD32(0, CNT_OFF + 4096 + (b & 7) * 128, SC1);
        if (__builtin_amdgcn_readfirstlane(r) == __builtin_amdgcn_readfirstlane(hs_c)) break;
        if (__builtin_amdgcn_readfirstlane(q) != __builtin_amdgcn_readfirstlane(qp)) { qp = q; if (l == 0) ST32(q, 0, CNT_OFF + b * 4, SC1); }
        __builtin_amdgcn_s_sleep(2);
      }
      CBAR();
      if (l == 0) __hip_atomic_store((LAS u32*)(sm + L_AC + 4), 1u, __ATOMIC_RELAXED, __HIP_MEMORY_SCOPE_WORKGROUP);
    }
#pragma unroll 1
    for (int sp = 0; sp < (SPIN_CAP << 4); ++sp) {
      if (__hip_atomic_load((LAS u32*)(sm + L_AC + 4), __ATOMIC_RELAXED, __HIP_MEMORY_SCOPE_WORKGROUP) != 0u) break;
      __builtin_amdgcn_s_sleep(1);
    }
    CBAR();
    PHASE();
    {
      const v2u xa = LD64(cc * 4096 + q8 * 8, XC_OFF + b * 8192, SC1), xb = LD64(cc * 4096 + q8 * 8 + 2048, XC_OFF + b * 8192, SC1);
      const v4u m0 = LD128(q8 * 32, FST_OFF, SC1), r0 = LD128(q8 * 32 + 16, FST_OFF, SC1);
      const v4u m1 = LD128(q8 * 32, FST_OFF + 8192, SC1), r1 = LD128(q8 * 32 + 16, FST_OFF + 8192, SC1);
      SBAR();
      LOAD_W(0, 8);
      xr0 = conv_ln(sm + L_U + cc * U_CH + 512 + q8 * 8, xa, m0, r0, dk);
      xr1 = conv_ln(sm + L_U + cc * U_CH + 2560 + q8 * 8, xb, m1, r1, dk);
      if ((tid & ~8) == 0) { const f32x2 bc = *(const LAS f32x2*)(sm + L_BND + cc * 8); *(LAS f32x2*)(sm + L_DK + cc * 8) = f32x2{dk, k_scale(bc.x + bc.y, -1)}; }
      SBAR();
#pragma unroll
      for (int i = 8; i < (COMM ? 28 : 16); ++i) asm volatile("" : "=v"(wf[i]));
    }
  }

#pragma unroll 1
  for (int ly = 0; ly < 2; ++ly) {
    const int b = opaque_s(b0), w = opaque_s(w0);
    const int t = MAP_T(b), s = MAP_S(b);
    int tid = opaque_v(threadIdx.x), l = tid & 63, l16 = l * 16;
    const int xmo = XM_OFF + ly * (2 << 20) + t * 32768 + w * 1024;
    const int w2off = W2F_OFF + ((s * 8 + w) * 28) * 1024;
    const int hoff = H_OFF + ly * H_LAYER + (t * 8 + s) * (28 * 1024) + (2 * w) * 1024;
    v4u xs[4];
    auto x_load = [&](int half) {
#pragma unroll
      for (int k = 0; k < 4; ++k) xs[k] = LD128(l16, xmo + half * (1 << 20) + k * 8192, SC1);
    };
    auto x_ok = [&]() { return __all(umax(umax(max4(xs[0]), max4(xs[1])), umax(max4(xs[2]), max4(xs[3]))) != 0xffffffffu); };
    auto x_stage = [&](int half) {
      if (!x_ok()) {
        int sp = 0;
#pragma unroll 1
        do {
          PHASE();
          __builtin_amdgcn_s_sleep(4);
          CBAR();
          x_load(half);
        } while (!x_ok() && ++sp < SPIN_CAP);
      }
#pragma unroll
      for (int k = 0; k < 4; ++k) *(LAS v4u*)(sm + L_R + half * 32768 + k * 8192 + tid * 16) = xs[k];
    };
    v2u pg;
    bool fin0 = true;
    auto fin_load = [&](int hf) { pg = LD64(l * 8, PST_OFF + ((t * 2 + hf) * 8 + s) * 512, SC1); };
    auto fin_try = [&](int hf) -> bool {
      if (!__all(pg[1] == 1u)) return false;
      const u32 e0 = pg[0];
      float v = __builtin_bit_cast(float, e0);
      v = swap32_add(v, v); v = swap16_add(v, v); v = dpp_add<0x128>(v);
      const float o = __builtin_bit_cast(float, __builtin_amdgcn_update_dpp(0, __builtin_bit_cast(int, v), 0xb1, 0xf, 0xf, false));
      float mm, vr, mb, rcs;
      ln_stats((l & 1) ? o : v, (l & 1) ? v : o, mm, vr, mb, rcs);
      if (l < 8) ST32(npz((l & 1) ? rcs : mb), (l & 1) * 16 + (l >> 1) * 4, FST_OFF + 16384 + hf * 8192 + (8 * t + s) * 32, SC1);
      return true;
    };
    auto fin_wait = [&](int hf) {
#pragma unroll 1
      for (int sp = 0; sp < SPIN_CAP; ++sp) {
        CBAR();
        fin_load(hf);
        if (fin_try(hf)) break;
        __builtin_amdgcn_s_sleep(1);
      }
    };
    v2u ix; v4u im, ir;
    auto cin_load = [&](int hf) {
      const int cc = (tid >> 3) & 1, q8 = (tid >> 4) * 8 + (tid & 7);
      ix = LD64(cc * 4096 + q8 * 8, XC_OFF + (2 << 20) + b * 8192 + hf * 2048, SC1);
      im = LD128(q8 * 32, FST_OFF + 16384 + hf * 8192, SC1); ir = LD128(q8 * 32 + 16, FST_OFF + 16384 + hf * 8192, SC1);
    };
    auto cin_ok = [&]() { return __all(umax(umax(ix[0], ix[1]), umax(max4(im), max4(ir))) != 0xffffffffu); };
    auto cin_ln = [&](int hf) -> f32x4 {
      if (!cin_ok()) {
        int sp = 0;
#pragma unroll 1
        do {
          PHASE();
          __builtin_amdgcn_s_sleep(4);
          CBAR();
          cin_load(hf);
        } while (!cin_ok() && ++sp < SPIN_CAP);
      }
      const int cc = (tid >> 3) & 1, q8 = (tid >> 4) * 8 + (tid & 7);
      return conv_ln(sm + L_U + cc * U_CH + 512 + hf * 2048 + q8 * 8, ix, im, ir, *(const LAS float*)(sm + L_DK + cc * 8));
    };
    CW(16); CW(17); CW(18); CW(19);
    PHASE();
    __syncthreads();
    PHASE();
    {
      const int c16 = l & 15, g4 = l >> 4;
      const LAS char* pa = sm + (w >> 2) * KC_CH + (c16 & 7) * 4160 + (3 + (c16 >> 3) - g4) * 16 + (w & 3) * 128;
      const LAS char* pb = sm + L_U + (w >> 2) * U_CH + (30 + 2 * c16 + g4) * 16 - (w & 3) * 128;
      LAS char* pp = sm + L_R + w * 4096 + (w >> 2) * 128 + ((4 * c16 + g4) ^ ((c16 >> 1) & 3)) * 16;
      const int xmv = (tid >> 4) * 32768 + (tid & 15) * 8;
      const LAS char* pr = sm + L_R + ((tid >> 3) & 1) * 16512 + ((((tid >> 4) << 3) | (tid & 7)) ^ ((tid >> 4) & 3)) * 16;
      auto conv_out = [&](int half, f32x4 xr) {
        const LAS char* p = pr + (half ? L_PB - L_R : 0);
        const float ksc = *(const LAS float*)(sm + L_DK + ((tid >> 3) & 1) * 8 + 4);
        const f32x4 y = (*(const LAS f32x4*)p + *(const LAS f32x4*)(p + 4096)) + (*(const LAS f32x4*)(p + 8192) + *(const LAS f32x4*)(p + 12288));
        const f32x4 o = y * ksc + xr;
        ST64((v2u{umin(pack_bf16(o[0], o[1]), 0xfffffffeu), umin(pack_bf16(o[2], o[3]), 0xfffffffeu)}), xmv,
             XM_OFF + ly * (2 << 20) + half * (1 << 20) + 128 * b, SC1);
      };
      {
        f32x4 acc[4];
#pragma unroll
        for (int i = 0; i < 4; ++i) acc[i] = f32x4{0.f, 0.f, 0.f, 0.f};
        v4u fa[2][4], fb[2][4];
#pragma unroll
        for (int tau = 0; tau < 2; ++tau)
#pragma unroll
          for (int i = 0; i < 4; ++i) { fa[tau][i] = *(const LAS v4u*)(pa + i * 512 + tau * 64); fb[tau][i] = *(const LAS v4u*)(pb + i * 512 - tau * 64); }
#pragma unroll
        for (int tau = 0; tau < 2; ++tau)
#pragma unroll
          for (int sx = 0; sx < 4; ++sx) {
#pragma unroll
            for (int e = 0; e + sx < 4; ++e) acc[sx + e] = mfma16h(fa[tau][sx], fb[tau][e], acc[sx + e]);
            if (sx == 0) { SBAR(); CW(20 + tau); }
          }
#pragma unroll
        for (int i = 0; i < 4; ++i) *(LAS f32x4*)(pp + i * 1024) = acc[i];
      }
      v4u fa[8], fb[4];
#pragma unroll
      for (int i = 0; i < 4; ++i) { fb[i] = *(const LAS v4u*)(pb + i * 512); fa[7 - i] = *(const LAS v4u*)(pa + (7 - i) * 512); }
#pragma unroll
      for (int i = 1; i < 4; ++i) fa[i] = *(const LAS v4u*)(pa + i * 512);
      SBAR();
      CW(22); CW(23);
      PHASE();
      __syncthreads();
      conv_out(0, xr0);
      PHASE();
      if (ly == 0) {
        LOAD_W(8, 16);
        if constexpr (COMM) {
          f32x4 cs = {0.f, 0.f, 0.f, 0.f};
#pragma unroll
          for (int k = 0; k < 8; ++k) cs += __builtin_bit_cast(f32x4, LD128(l16, CS_OFF + k * (FF * 4) + s * 896, SC1));
          if (l < 56) *(LAS f32x4*)(sm + L_CS + l16) = cs;
        }
      }
      {
        f32x4 acc[4];
#pragma unroll
        for (int i = 0; i < 4; ++i) acc[i] = f32x4{0.f, 0.f, 0.f, 0.f};
#pragma unroll
        for (int e = 0; e < 4; ++e) {
#pragma unroll
          for (int i = 0; i < 4; ++i) acc[i] = mfma16h(fa[4 + i - e], fb[e], acc[i]);
          fb[e] = *(const LAS v4u*)(pb + e * 512 - 64); fa[7 - e] = *(const LAS v4u*)(pa + (7 - e) * 512 + 64);
          SBAR();
          if (e & 1) CW(24 + (e >> 1));
        }
#pragma unroll
        for (int i = 3; i >= 1; --i) fa[i] = *(const LAS v4u*)(pa + i * 512 + 64);
        if (ly) cin_load(1);
        else { asm volatile("" : "=v"(ix), "=v"(im), "=v"(ir)); }
        SBAR();
#pragma unroll
        for (int e = 0; e < 4; ++e) {
#pragma unroll
          for (int i = 0; i < 4; ++i) acc[i] = mfma16h(fa[4 + i - e], fb[e], acc[i]);
          SBAR();
          if (e & 1) CW(26 + (e >> 1));
        }
#pragma unroll
        for (int i = 0; i < 4; ++i) { fa[i] = *(const LAS v4u*)(pa + i * 512); fa[4 + i] = *(const LAS v4u*)(pa + i * 512 + 64); }
        PHASE();
        if (ly) xr1 = cin_ln(1);
        SBAR();
        CW(28); CW(29);
        PHASE();
        __syncthreads();
        v4u fc[8];
#pragma unroll
        for (int i = 0; i < 4; ++i) { fc[i] = *(const LAS v4u*)(pb + (4 + i) * 512); fc[4 + i] = *(const LAS v4u*)(pb + (4 + i) * 512 - 64); }
#pragma unroll
        for (int tau = 0; tau < 2; ++tau)
#pragma unroll
          for (int e = 0; e < 4; ++e) {
#pragma unroll
            for (int i = e; i < 4; ++i) acc[i] = mfma16h(fa[4 * tau + i - e], fc[4 * tau + e], acc[i]);
            if (e == 0) { SBAR(); CW(30 + tau); }
          }
#pragma unroll
        for (int i = 0; i < 4; ++i) *(LAS f32x4*)(pp + (L_PB - L_R) + i * 1024) = acc[i];
      }
      PHASE();
      __syncthreads();
      x_load(0);
      SBAR();
      conv_out(1, xr1);
      PHASE();
      x_stage(0);
      SBAR();
      if constexpr (!COMM) {
        const u32 on = (u32)opaque_v(-1);
#pragma unroll
        for (int k = 0; k < 4; ++k) ST128((v4u{on, on, on, on}), l16 + (k & 1) * 1024, hoff + (k >> 1) * 14336, SC1);
        SBAR();
      }
    }
    PHASE();
    __syncthreads();
    tid = opaque_v(threadIdx.x); l = tid & 63; l16 = l * 16;
    const int h = l >> 5;
    const LAS char* xb = sm + L_R + (8 * h + ((l & 15) >> 2)) * 64 + 32 * ((l >> 4) & 1) + 8 * (l & 3);
    auto stats = [&](int half) {
      const LAS char* xq = sm + L_R + half * 32768 + (8 * (l >> 4) + ((l & 15) >> 2)) * 64 + 8 * (l & 3);
      const u32 one = (u32)opaque_v(0x3f803f80);
      const v4u ones = {one, one, one, one};
      f32x4 s0 = {0.f, 0.f, 0.f, 0.f}, q0 = s0, s1 = s0, q1 = s0;
      v4u f0[4], f1[4];
#pragma unroll
      for (int k = 0; k < 4; ++k) { f0[k] = tr_frag(xq + k * 2048); f1[k] = tr_frag(xq + k * 2048 + 32); }
#pragma unroll
      for (int k = 0; k < 16; ++k) {
        s0 = mfma16b(ones, f0[k & 3], s0); q0 = mfma16b(f0[k & 3], f0[k & 3], q0);
        s1 = mfma16b(ones, f1[k & 3], s1); q1 = mfma16b(f1[k & 3], f1[k & 3], q1);
        if (k + 4 < 16) { f0[k & 3] = tr_frag(xq + (k + 4) * 2048); f1[k & 3] = tr_frag(xq + (k + 4) * 2048 + 32); }
        if (half == 0 && k == 9) x_load(1);
        SBAR();
      }
      const float d0 = (l & 2) ? ((l & 1) ? q0[3] : q0[2]) : ((l & 1) ? q0[1] : q0[0]);
      const float d1 = (l & 2) ? ((l & 1) ? q1[3] : q1[2]) : ((l & 1) ? q1[1] : q1[0]);
#pragma unroll
      for (int g = 0; g < 2; ++g) {
        float m, v, mb, r1;
        ln_stats(g ? s1[0] : s0[0], g ? d1 : d0, m, v, mb, r1);
        const u32 p2 = pack_bf16((m - mb) * r1, v * r1 * r1);
        const float r2 = __builtin_amdgcn_rcpf(bfr(__builtin_amdgcn_sqrtf(bfr(bfhi(p2) + 1e-5f))));
        if ((l >> 4) == ((l >> 2) & 3)) *(LAS f32x2*)(sm + L_AB + (half * 32 + 16 * g + (l & 15)) * 8) = f32x2{r1 * r2, (mb * r1 + bflo(p2)) * r2};
      }
    };
    auto epi_quad = [&](const f32x16& acc, int q, f32x2 ab, u32& o0, u32& o1) {
      const f32x4 c4 = *(const LAS f32x4*)(sm + L_CS + (32 * w + 8 * q + 4 * h) * 4);
      const f32x4 b4 = *(const LAS f32x4*)(sm + L_B1 + (32 * w + 8 * q + 4 * h) * 4);
      const f32x4 x = f32x4{acc[4 * q], acc[4 * q + 1], acc[4 * q + 2], acc[4 * q + 3]} * ab.x + (b4 - c4 * ab.y);
      const f32x2 g0 = gelu2(f32x2{x[0], x[1]}), g1 = gelu2(f32x2{x[2], x[3]});
      o0 = umin(pack_bf16(g0.x, g0.y), 0xfffffffeu); o1 = umin(pack_bf16(g1.x, g1.y), 0xfffffffeu);
    };
    f32x16 acc0, acc1;
    constexpr int XD = 3;
    if constexpr (!COMM) {
      v4u xf[XD];
#pragma unroll
      for (int k = 0; k < XD; ++k) xf[k] = tr_frag(xb + k * 1024);
      acc0 = f32x16{};
#pragma unroll
      for (int ks = 0; ks < 32; ++ks) {
        acc0 = mfma32(wf[ks], xf[ks % XD], acc0);
        if (ks + XD < 32) xf[ks % XD] = tr_frag(xb + (ks + XD) * 1024);
        if (ks < 2 * W2A && !(ks & 1)) { const int k = ks >> 1; wa[k] = LD128(l16 + (k & 3) * 1024, w2off + (k >> 2) * 4096, SC1); }
        if (ks == 15) x_load(1);
        SBAR();
      }
      asm volatile("" : "+v"(acc0));
    } else {
      __builtin_amdgcn_s_setprio(2);
      stats(0);
      __builtin_amdgcn_s_setprio(0);
    }
    PHASE();
    x_stage(1);
    PHASE();
    __syncthreads();
    PHASE();
    const int orow = ly ? l : 8 * w + (l >> 3), oq = ly ? w : (l & 7);
    const v2u xres0 = *(const LAS v2u*)(sm + L_R + (64 * s + orow) * 64 + 8 * oq);
    const v2u xres1 = *(const LAS v2u*)(sm + L_XB + (64 * s + orow) * 64 + 8 * oq);
    const int hfo = HF_OFF + ly * 4096 + t * 128;
    if constexpr (!COMM) {
      v4u xf[XD];
#pragma unroll
      for (int k = 0; k < XD; ++k) xf[k] = tr_frag(xb + 32768 + k * 1024);
      const f32x2 ab = *(const LAS f32x2*)(sm + L_AB + (l & 31) * 8);
      u32 o[4];
      acc1 = f32x16{};
#pragma unroll
      for (int ks = 0; ks < 32; ++ks) {
        acc1 = mfma32(wf[ks], xf[ks % XD], acc1);
        if (ks + XD < 32) xf[ks % XD] = tr_frag(xb + 32768 + (ks + XD) * 1024);
        if (ks & 1) { const int j = ks >> 1, k = W2A + j; wf[j] = LD128(l16 + (k & 3) * 1024, w2off + (k >> 2) * 4096, SC1); }
        if (ks % 6 == 1 && ks < 24) { const int q = ks / 6; epi_quad(acc0, q, ab, o[2 * (q & 1)], o[2 * (q & 1) + 1]); }
        if (ks == 11 || ks == 23) ST128((v4u{o[0], o[1], o[2], o[3]}), l16 + (ks / 12) * 1024, hoff, SC1);
        if (ks % 6 == 5 || ks >= 24) SBAR();
      }
      asm volatile("" : "+v"(acc1));
    } else {
      if (l == 0) ST32(1u, 0, hfo + s * 4, SC1);
      __builtin_amdgcn_s_setprio(2);
      stats(1);
      __builtin_amdgcn_s_setprio(0);
#pragma unroll 1
      for (int sp = 0; sp < SPIN_CAP; ++sp) {
        CBAR();
        const u32 f = LD32((l & 7) * 4, hfo, SC1);
        if (__all(f == 1u)) break;
        __builtin_amdgcn_s_sleep(1);
      }
      CBAR();
    }
    PHASE();
    __syncthreads();
    v4u hf[14];
    const int hin = H_OFF + ly * H_LAYER + (t * 8 + w) * (28 * 1024);
    auto h_load = [&](int half, int lo, int hi) {
#pragma unroll
      for (int k = lo; k < hi; ++k) { hf[k] = LD128(l16, hin + (14 * half + k) * 1024, SC1); SBAR(); }
    };
    h_load(0, 0, 7);
    PHASE();
    if constexpr (!COMM) {
      const f32x2 ab = *(const LAS f32x2*)(sm + L_AB + (32 + (l & 31)) * 8);
#pragma unroll
      for (int sp = 0; sp < 2; ++sp) {
        u32 o[4];
        epi_quad(acc1, 2 * sp, ab, o[0], o[1]);
        epi_quad(acc1, 2 * sp + 1, ab, o[2], o[3]);
        ST128((v4u{o[0], o[1], o[2], o[3]}), l16 + sp * 1024, hoff + 14336, SC1);
      }
      SBAR();
    }
    h_load(0, 7, 14);
    PHASE();
    LAS char* rbw = sm + L_R + (w * 64 + (l & 31)) * RB_ROW + h * 16;
    auto g2_out = [&](int half, v2u xres) {
      const LAS char* rp = sm + L_R + orow * RB_ROW + oq * 16;
      f32x4 o4 = unpack4(xres) + *(const LAS float*)(sm + L_B2 + orow * 4);
      f32x4 p4 = *(const LAS f32x4*)rp;
#pragma unroll
      for (int ww = 1; ww < 8; ++ww) { const f32x4 v = *(const LAS f32x4*)(rp + ww * (64 * RB_ROW)); if (ww & 1) o4 += v; else p4 += v; }
      o4 += p4;
      u32 o0 = pack_bf16(o4[0], o4[1]), o1 = pack_bf16(o4[2], o4[3]);
      if (ly) {
        u16* op = out + (1024 * half + 32 * t + 4 * w) * DM + 64 * s + l;
        op[0] = (u16)(o0 & 0xffffu); op[DM] = (u16)(o0 >> 16); op[2 * DM] = (u16)(o1 & 0xffffu); op[3 * DM] = (u16)(o1 >> 16);
      } else {
        o0 = umin(o0, 0xfffffffeu); o1 = umin(o1, 0xfffffffeu);
        ST64((v2u{o0, o1}), (tid >> 3) * 4096 + (tid & 7) * 8, XC_OFF + (2 << 20) + s * (64 * 4096) + t * 64 + half * 2048, SC1);
        const float x0 = bflo(o0), x1 = bfhi(o0), x2 = bflo(o1), x3 = bfhi(o1);
        const float a0 = swap32_add(x0, x2), a1 = swap32_add(x0 * x0, x2 * x2), a2 = swap32_add(x1, x3), a3 = swap32_add(x1 * x1, x3 * x3);
        const float sv = dpp_add<0x128>(swap16_add(a0, a2)), qv = dpp_add<0x128>(swap16_add(a1, a3));
        if ((l & 8) == 0) *(LAS f32x2*)(sm + L_SP + ((half * 8 + w) * 32 + 4 * (l & 7) + (l >> 4)) * 8) = f32x2{sv, qv};
      }
    };
    auto pst_out = [&](int half) {
      float v = 0.f;
#pragma unroll
      for (int k = 0; k < 8; ++k) v += *(const LAS float*)(sm + L_SP + (half * 8 + k) * 256 + l * 4);
      ST64((v2u{__builtin_bit_cast(u32, v), (u32)opaque_v(1)}), (l >> 3) * 512 + (l & 7) * 8, PST_OFF + (t * 2 + half) * 4096 + s * 64, SC1);
    };
#define W2FRAG(k) (COMM ? wf[k] : (k) < W2A ? wa[(k) < W2A ? (k) : 0] : wf[(k) < W2A ? 0 : (k) - W2A])
    auto gemm2 = [&](int half) {
      f32x16 a0, a1;
#pragma unroll 1
      for (int sp = 0; sp < SPIN_CAP; ++sp) {
        a0 = f32x16{}; a1 = f32x16{};
        u32 mx = 0;
#pragma unroll
        for (int k = 0; k < 14; ++k) {
          a0 = mfma32(hf[k], W2FRAG(2 * k), a0); a1 = mfma32(hf[k], W2FRAG(2 * k + 1), a1);
          mx = umax(mx, max4(hf[k]));
        }
        if (__all(mx != 0xffffffffu)) break;
        PHASE();
        __builtin_amdgcn_s_sleep(2);
        CBAR();
        h_load(half, 0, 14);
      }
      PHASE();
      if (!COMM && half == 0) h_load(1, 0, 14);
      PHASE();
#pragma unroll
      for (int q = 0; q < 4; ++q) {
        *(LAS f32x4*)(rbw + q * 32) = f32x4{a0[4 * q], a0[4 * q + 1], a0[4 * q + 2], a0[4 * q + 3]};
        *(LAS f32x4*)(rbw + 32 * RB_ROW + q * 32) = f32x4{a1[4 * q], a1[4 * q + 1], a1[4 * q + 2], a1[4 * q + 3]};
      }
      if constexpr (!COMM) {
        if (half == 1) {
          SBAR();
          if (ly == 0) {
            if (w == 0) {
#pragma unroll 1
              for (int sp = 0; sp < 3; ++sp) { CBAR(); fin_load(0); fin0 = fin_try(0); if (fin0) break; }
            }
            LOAD_W(0, 16);
          } else {
#pragma unroll
            for (int i = 0; i < 16; ++i) asm volatile("" : "=v"(wf[i]));
          }
        }
      }
    };
    gemm2(0);
    __syncthreads();
    if constexpr (COMM) h_load(1, 0, 14);
    PHASE();
    g2_out(0, xres0);
    PHASE();
    __syncthreads();
    if (COMM && ly == 0) pst_out(0);
    gemm2(1);
    __syncthreads();
    PHASE();
    g2_out(1, xres1);
    PHASE();
    if (ly == 0) {
      cin_load(0);
      __syncthreads();
      if constexpr (COMM) { pst_out(1); fin_wait(1); }
      else if (!fin0) fin_wait(0);
      PHASE();
      xr0 = cin_ln(0);
      PHASE();
    } else {
      asm volatile("" : "=v"(xr0));
    }
    asm volatile("" : "=v"(xr1));
  }
}

__global__ __launch_bounds__(512, 2) void k_main(const Args a) {
  extern __shared__ __attribute__((aligned(16))) char smem[];
  LAS char* sm = (LAS char*)smem;
  const int b0 = blockIdx.x, w0 = __builtin_amdgcn_readfirstlane(threadIdx.x >> 6);
  if (w0 == 7) main_body<true>(sm, a, b0, w0);
  else main_body<false>(sm, a, b0, w0);
}

extern "C" void kernel_launch(void* const* d_in, const int* in_sizes, int n_in, void* d_out, int out_size, void* d_ws,
                              size_t ws_size, hipStream_t stream) {
  (void)hipFuncSetAttribute((const void*)k_main, hipFuncAttributeMaxDynamicSharedMemorySize, MAIN_LDS);
  Args a;
  a.x0 = (const u16*)d_in[0]; a.logA = (const u16*)d_in[1]; a.Aim = (const u16*)d_in[2]; a.Cre = (const u16*)d_in[3];
  a.Cim = (const u16*)d_in[4]; a.logdt = (const u16*)d_in[5]; a.dskip = (const u16*)d_in[6]; a.w1 = (const u16*)d_in[8];
  a.b1 = (const u16*)d_in[9]; a.w2 = (const u16*)d_in[10]; a.b2 = (const u16*)d_in[11];
  a.ws = (char*)d_ws; a.out = (u16*)d_out;
  k_main<<<256, 512, MAIN_LDS, stream>>>(a);
}
```

```cpp
#include <hip/hip_runtime.h>
#include <hip/hip_bf16.h>

typedef unsigned int u32;
typedef unsigned short u16;
typedef __attribute__((ext_vector_type(4))) u32 v4u;
typedef __attribute__((ext_vector_type(2))) u32 v2u;
typedef __attribute__((ext_vector_type(4))) float f32x4;
typedef __attribute__((ext_vector_type(2))) float f32x2;
typedef __attribute__((ext_vector_type(16))) float f32x16;
typedef __attribute__((ext_vector_type(8))) __bf16 bf16x8;
typedef __attribute__((ext_vector_type(2))) __bf16 bf16x2;
typedef __attribute__((ext_vector_type(8))) _Float16 f16x8;
typedef __attribute__((ext_vector_type(2))) _Float16 f16x2;
typedef __attribute__((ext_vector_type(4))) short s16x4;
#define LAS __attribute__((address_space(3)))
#define DI __device__ __forceinline__
#define PHASE() __builtin_amdgcn_sched_barrier(0)

constexpr int DM = 512, NS = 128, FF = 1792;
constexpr int CNT_OFF = 0;
constexpr int HF_OFF = CNT_OFF + 16384;
constexpr int FST_OFF = 65536;
constexpr int CS_OFF = FST_OFF + 32768;
constexpr int PST_OFF = CS_OFF + 65536;
constexpr int XC_OFF = 1 << 20;
constexpr int XM_OFF = 5 << 20;
constexpr int W1F_OFF = 9 << 20;
constexpr int W2F_OFF = 11 << 20;
constexpr int H_OFF = 13 << 20;
constexpr int H_LAYER = 32 * 8 * 2 * 14 * 1024;
constexpr int WS_BYTES = 28 << 20;
constexpr int SC1 = 16;
constexpr int SPIN_CAP = 1 << 16;

DI u32 umin(u32 a, u32 b) { return a < b ? a : b; }
DI u32 umax(u32 a, u32 b) { return a > b ? a : b; }
DI float bf2f(u32 h) { return __builtin_bit_cast(float, h << 16); }
DI float bflo(u32 p) { return __builtin_bit_cast(float, p << 16); }
DI float bfhi(u32 p) { return __builtin_bit_cast(float, p & 0xffff0000u); }
DI u32 pack_bf16(float a, float b) { bf16x2 v = {(__bf16)a, (__bf16)b}; return __builtin_bit_cast(u32, v); }
DI float bfr(float a) { return bflo(pack_bf16(a, a)); }
DI u32 pack_f16(float a, float b) { f16x2 v = {(_Float16)a, (_Float16)b}; return __builtin_bit_cast(u32, v); }
DI float dot2bf(u32 a, u32 b, float c) {
  return __builtin_amdgcn_fdot2_f32_bf16(__builtin_bit_cast(bf16x2, a), __builtin_bit_cast(bf16x2, b), c, false);
}
DI f32x16 mfma32(v4u a, v4u b, f32x16 c) {
  return __builtin_amdgcn_mfma_f32_32x32x16_bf16(__builtin_bit_cast(bf16x8, a), __builtin_bit_cast(bf16x8, b), c, 0, 0, 0);
}
DI f32x4 mfma16h(v4u a, v4u b, f32x4 c) {
  return __builtin_amdgcn_mfma_f32_16x16x32_f16(__builtin_bit_cast(f16x8, a), __builtin_bit_cast(f16x8, b), c, 0, 0, 0);
}
DI f32x4 mfma16b(v4u a, v4u b, f32x4 c) {
  return __builtin_amdgcn_mfma_f32_16x16x32_bf16(__builtin_bit_cast(bf16x8, a), __builtin_bit_cast(bf16x8, b), c, 0, 0, 0);
}
DI v4u tr_frag(const LAS char* p) {
  const s16x4 a = __builtin_amdgcn_ds_read_tr16_b64_v4i16((LAS s16x4*)p);
  const s16x4 c = __builtin_amdgcn_ds_read_tr16_b64_v4i16((LAS s16x4*)(p + 256));
  return __builtin_bit_cast(v4u, __builtin_shufflevector(a, c, 0, 1, 2, 3, 4, 5, 6, 7));
}
template <int CTRL> DI float dpp_add(float v) {
  const int o = __builtin_amdgcn_update_dpp(0, __builtin_bit_cast(int, v), CTRL, 0xf, 0xf, false);
  return v + __builtin_bit_cast(float, o);
}
DI float row_sum16(float v) { v = dpp_add<0x128>(v); v = dpp_add<0x124>(v); v = dpp_add<0x122>(v); return dpp_add<0x121>(v); }
DI float swap32_add(float a, float c) {
  const auto r = __builtin_amdgcn_permlane32_swap(__builtin_bit_cast(u32, a), __builtin_bit_cast(u32, c), false, false);
  const u32 r0 = r[0], r1 = r[1];
  return __builtin_bit_cast(float, r0) + __builtin_bit_cast(float, r1);
}
DI float swap16_add(float a, float c) {
  const auto r = __builtin_amdgcn_permlane16_swap(__builtin_bit_cast(u32, a), __builtin_bit_cast(u32, c), false, false);
  const u32 r0 = r[0], r1 = r[1];
  return __builtin_bit_cast(float, r0) + __builtin_bit_cast(float, r1);
}
DI float wave_sum(float v) { v = row_sum16(v); v = swap16_add(v, v); return swap32_add(v, v); }
DI void ln_stats(float s, float q, float& m, float& v, float& mb, float& rs) {
  m = s * (1.0f / DM);
  v = fmaxf(q * (1.0f / DM) - m * m, 0.f);
  const u32 pk = pack_bf16(m, __builtin_amdgcn_sqrtf(bfr(bfr(v) + 1e-5f)));
  mb = bflo(pk); rs = __builtin_amdgcn_rcpf(bfhi(pk));
}
DI u32 npz(float a) { return umin(__builtin_bit_cast(u32, a), 0xfffffffeu); }
#define LD128(vo, so, aux) __builtin_amdgcn_raw_buffer_load_b128(rs, (vo), (so), (aux))
#define LD64(vo, so, aux) __builtin_amdgcn_raw_buffer_load_b64(rs, (vo), (so), (aux))
#define LD32(vo, so, aux) __builtin_amdgcn_raw_buffer_load_b32(rs, (vo), (so), (aux))
#define ST128(v, vo, so, aux) __builtin_amdgcn_raw_buffer_store_b128((v), rs, (vo) + (so), 0, (aux))
#define ST64(v, vo, so, aux) __builtin_amdgcn_raw_buffer_store_b64((v), rs, (vo), (so), (aux))
#define ST32(v, vo, so, aux) __builtin_amdgcn_raw_buffer_store_b32((v), rs, (vo), (so), (aux))
#define DRAIN() asm volatile("s_waitcnt vmcnt(0)" ::: "memory")
#define CBAR() asm volatile("" ::: "memory")
#define SBAR() __builtin_amdgcn_sched_barrier(0)
DI int opaque_v(int x) { asm volatile("" : "+v"(x)); return x; }
template <typename T, int N> DI void undef_regs(T (&a)[N]) {
#pragma unroll
  for (int i = 0; i < N; ++i) asm volatile("" : "=v"(a[i]));
}
DI int opaque_s(int x) { asm volatile("" : "+s"(x)); return x; }

constexpr int KT_REL = 24576 + 16;
constexpr int KT_CH = 1312;
DI void cexp_p(float arl, float rh, float rl, float p, float& re, float& im) {
  const float e = __builtin_amdgcn_exp2f(arl * p);
  const float r = __builtin_fmaf(rl, p, __builtin_amdgcn_fractf(rh * p));
  re = e * __builtin_amdgcn_cosf(r);
  im = e * __builtin_amdgcn_sinf(r);
}
DI float k_scale(float bs, int sign) {
  const int ex = (int)((__builtin_bit_cast(u32, bs) >> 23) & 0xff) - 126;
  int e = 15 - ex; e = e > 100 ? 100 : (e < -100 ? -100 : e);
  return __builtin_bit_cast(float, (u32)(127 + sign * e) << 23);
}
constexpr int KC_CH = 8 * 260 * 16;
constexpr int L_U = 2 * KC_CH;
constexpr int U_CH = 4672;
constexpr int L_R = L_U + 2 * U_CH;
constexpr int L_XB = L_R + 32768;
constexpr int L_PB = L_XB + 256;
constexpr int L_KT = L_R + KT_REL;
constexpr int L_PRM = L_XB + 16384;
constexpr int L_BND = L_PRM + 5120;
constexpr int RB_ROW = 144;
constexpr int L_S = L_R + 8 * 64 * RB_ROW;
constexpr int L_B1 = L_S;
constexpr int L_CS = L_B1 + 896;
constexpr int L_SP = L_CS + 896;
constexpr int L_AC = L_SP + 4096;
constexpr int L_B2 = L_AC + 64;
constexpr int L_DK = L_B2 + 256;
constexpr int L_AB = L_DK + 16;
constexpr int L_END = L_AB + 512;
constexpr int MAIN_LDS = L_END;
#define MAP_T(b) ((((b) >> 1) & 3) * 8 + ((b) >> 5))
#define MAP_S(b) (((b) & 1) * 4 + (((b) >> 3) & 3))
constexpr int W2A = 12;

DI f32x2 gelu2(f32x2 x) {
  const f32x2 t = x * (x * x * -0.10294324f + -2.3022082f);
  f32x2 e; e.x = __builtin_amdgcn_exp2f(t.x); e.y = __builtin_amdgcn_exp2f(t.y);
  e = e + 1.0f;
  f32x2 r; r.x = __builtin_amdgcn_rcpf(e.x); r.y = __builtin_amdgcn_rcpf(e.y);
  return x * r;
}
DI u32 max4(v4u a) { return umax(umax(a[0], a[1]), umax(a[2], a[3])); }
DI f32x4 unpack4(v2u p) { const u32 a = p[0], c = p[1]; return f32x4{bflo(a), bfhi(a), bflo(c), bfhi(c)}; }
DI f32x4 conv_ln(LAS char* dst, v2u xp, v4u mm, v4u rr, float dk) {
  const f32x4 x = unpack4(xp);
  const f32x4 u = (x - __builtin_bit_cast(f32x4, mm)) * __builtin_bit_cast(f32x4, rr);
  *(LAS v2u*)dst = v2u{pack_f16(u[0], u[1]), pack_f16(u[2], u[3])};
  return x + u * dk;
}

constexpr u32 CODE_BYTES = 42u * 1024u;
struct Args {
  const u16 *x0, *logA, *Aim, *Cre, *Cim, *logdt, *dskip, *w1, *b1, *w2, *b2;
  char* ws;
  u16* out;
};
template <bool COMM>
DI void main_body(LAS char* sm, const Args& a, int b0, int w0) {
  unsigned long long pc;
  asm volatile("s_getpc_b64 s[90:91]\n\ts_add_u32 s90, s90, _Z6k_main4Args@rel32@lo+4\n\ts_addc_u32 s91, s91, _Z6k_main4Args@rel32@hi+12\n\ts_mov_b64 %0, s[90:91]"
               : "=s"(pc) :: "s90", "s91", "scc");
  const u32 warm = *(const __attribute__((address_space(1))) u32*)(pc + umin((u32)opaque_v(threadIdx.x) * 128u, CODE_BYTES));
  SBAR();
  const __amdgpu_buffer_rsrc_t rs = __builtin_amdgcn_make_buffer_rsrc(a.ws, 0, WS_BYTES, 0x00020000);
  u16* const out = a.out;
  PHASE();
  v4u wf[32], wa[W2A];
  f32x4 xr0, xr1;
#define LOAD_W(lo, hi) do { _Pragma("unroll") for (int ks = (lo); ks < (hi); ++ks) { \
      if constexpr (!COMM) wf[ks] = LD128(l16 + (ks & 3) * 1024, W1F_OFF + (7 * s + w) * 32768 + (ks >> 2) * 4096, SC1); \
      else if (ks < 28) wf[ks] = LD128(l16 + (ks & 3) * 1024, W2F_OFF + (s * 8 + 7) * 28672 + (ks >> 2) * 4096, SC1); } \
      SBAR(); } while (0)
#define CW(k) do { if constexpr (!COMM) LOAD_W(k, (k) + 1); } while (0)
  {
    const int b = opaque_s(b0), w = opaque_s(w0), tid = opaque_v(threadIdx.x), l = tid & 63, l16 = l * 16, s = MAP_S(b);
    const int cc = (tid >> 3) & 1, q8 = (tid >> 4) * 8 + (tid & 7);
    const int kk = l >> 4, i16 = l & 15, t = MAP_T(b);
    u32 rla, rai, rcr, rci, rdt, hs_c = 0;
    v4u xrow[2], xtile[2], wr[4];
    const int fr = (b < 112 ? b : b - 112) * 16 + 4 * (w & 3);
    if (!COMM && w < 4) {
      const int o = 2 * b * NS + tid;
      rla = a.logA[o]; rai = a.Aim[o]; rcr = a.Cre[o]; rci = a.Cim[o]; rdt = a.logdt[2 * b + (w >> 1)];
      undef_regs(wr); undef_regs(xrow); undef_regs(xtile);
      SBAR();
      const u32 on = (u32)opaque_v(-1);
#pragma unroll
      for (int k = 0; k < 2; ++k) ST128((v4u{on, on, on, on}), (tid >> 3) * 32768 + (tid & 7) * 16, XM_OFF + k * (1 << 20) + 128 * b, SC1);
    } else {
      asm volatile("" : "=v"(rla), "=v"(rai), "=v"(rcr), "=v"(rci), "=v"(rdt));
      if (!COMM && w == 4) hs_c = LD32(0, CNT_OFF + 8192 + b * 4, SC1);
#pragma unroll
      for (int i = 0; i < 2; ++i) {
        xrow[i] = *(const v4u*)(a.x0 + (8 * b + 2 * (w & 3) + i) * DM + 8 * l);
        xtile[i] = *(const v4u*)(a.x0 + (64 * (b >> 3) + 32 * i + (l >> 1)) * DM + 64 * (b & 7) + 16 * (w & 3) + 8 * (l & 1));
      }
      if (b < 112) {
        const u16* p = a.w1 + (16 * (fr & 31) + (l >> 2)) * FF + 32 * (fr >> 5) + 8 * (l & 3);
#pragma unroll
        for (int f = 0; f < 4; ++f) wr[f] = *(const v4u*)(p + 16 * f * FF);
      } else {
        const int r1 = (b < 224 ? fr : 0) >> 1, ks2 = r1 % 14, r2 = r1 / 14;
        const u16* p = a.w2 + (224 * (r2 & 7) + 16 * ks2 + (l >> 2)) * DM + 64 * (r2 >> 3) + 8 * (l & 3);
#pragma unroll
        for (int f = 0; f < 4; ++f) wr[f] = *(const v4u*)(p + 16 * (f >> 1) * DM + 32 * (f & 1));
      }
    }
    const float dk = bf2f(a.dskip[2 * b + cc]);
    const float bias2 = bf2f(a.b2[64 * s + l]);
    const float bb1 = bf2f(a.b1[224 * s + (tid < 224 ? tid : 0)]);
    SBAR();
    u32 hs_q = 0;
    if (COMM && b == 255) {
      const v4u d = LD128(l16, CNT_OFF, SC1);
      const u32 qo = LD32(0, CNT_OFF + 4096, SC1);
      u32 q = qo + 1u;
#pragma unroll 1
      for (int k = 0; k < 300; ++k) { if (!__any(d[0] == q || d[1] == q || d[2] == q || d[3] == q)) break; ++q; }
      hs_q = __builtin_amdgcn_readfirstlane(q);
      if (l < 8) ST32(hs_q, l * 128, CNT_OFF + 4096, SC1);
    }
    if (tid < 16) { *(LAS v4u*)(sm + (tid >> 3) * KC_CH + (tid & 7) * 4160) = v4u{0u, 0u, 0u, 0u}; *(LAS u32*)(sm + L_AC + tid * 4) = 0u; }
    if (tid >= 64 && tid < 128) *(LAS v4u*)(sm + L_U + ((tid >> 5) & 1) * U_CH + (tid & 31) * 16) = v4u{0u, 0u, 0u, 0u};
    if (tid >= 128 && tid < 130) *(LAS v4u*)(sm + L_KT - 16 + (tid - 128) * KT_CH) = v4u{0u, 0u, 0u, 0u};
    const int c = tid >> 8, x = tid & 255;
    int tix, gix[8];
    {
      const int col = x >> 4, row = x & 15;
      const int p0 = 128 * (row >> 3) + (row & 7) + 8 * col;
      const int p1 = (row < 8 ? 256 + 128 * (row >> 2) + 2 * (row & 3) : 512 + 128 * ((row - 8) >> 1) + 4 * (row & 1)) + 8 * col;
      const int c1 = row < 8 ? 128 + (p1 >> 1) : 256 + (p1 >> 2);
      const int c2 = row < 8 ? 512 + 16 * row + col : (x == 8 ? 640 : 641);
      tix = opaque_v(p0 | (c1 << 10) | (c2 << 20));
#pragma unroll
      for (int j = 0; j < 8; ++j) {
        const int la = 8 * x + 7 - 2 * j, lb = la - 1;
        const u32 hb = pack_bf16((float)la, (float)lb);
        const int ia = la < 128 ? la : (int)(hb & 0xffffu) - 17024, ib = lb < 128 ? lb : (int)(hb >> 16) - 17024;
        gix[j] = opaque_v((KT_REL + c * KT_CH + 2 * ia) | ((KT_REL + c * KT_CH + 2 * ib) << 16));
      }
    }
    const float pB = (float)(8 * i16);
    const float pA0 = (float)(128 * (i16 >> 3) + (i16 & 7));
    const float pA1 = (float)(i16 < 8 ? 256 + 128 * (i16 >> 2) + 2 * (i16 & 3) : 512 + 128 * ((i16 - 8) >> 1) + 4 * (i16 & 1));
    const float pA2 = (float)(i16 < 8 ? 1024 + 128 * i16 : 2048);
    f32x4 acc0 = {0.f, 0.f, 0.f, 0.f}, acc1 = acc0, acc2 = acc0;
    PHASE();
    if (!COMM && w < 4) {
      const float dt = bfr(expf(bf2f(rdt)));
      const float arl = -bfr(expf(bf2f(rla))) * dt * 1.4426950408889634f;
      double tt = (double)(bf2f(rai) * dt) * 0.15915494309189533577;
      tt -= __builtin_rint(tt);
      const float rh = __builtin_bit_cast(float, __builtin_bit_cast(u32, (float)tt) & 0xfffff000u);
      const float rl = (float)(tt - (double)rh);
      const float cr = bf2f(rcr) * dt * 2.0f, ci = bf2f(rci) * dt * 2.0f;
      LAS float* q = (LAS float*)(sm + L_PRM + (tid >> 2) * 80) + (tid & 3);
      q[0] = arl; q[4] = rh; q[8] = rl; q[12] = cr; q[16] = ci;
      const float bs = wave_sum(fabsf(cr) + fabsf(ci));
      if (l == 0) *(LAS float*)(sm + L_BND + w * 4) = bs;
    }
    PHASE();
    __syncthreads();
    PHASE();
    if (COMM || w >= 4) {
      __builtin_amdgcn_s_setprio(3);
      if (!COMM && w == 4 && l == 0) { ST32(hs_c + 1u, 0, CNT_OFF + 12288 + b * 4, SC1); *(LAS u32*)(sm + L_AC + 12) = hs_c + 1u; }
      {
        const u32 on = (u32)opaque_v(-1);
        const v4u on4 = {on, on, on, on};
        const int i8 = tid & 255;
        if (i8 < 64) ST128(on4, (i8 >> 2) * 512 + (i8 & 3) * 16, PST_OFF + t * 8192 + s * 64, SC1);
        else if (i8 < 68) ST128(on4, ((i8 >> 1) & 1) * 8192 + (i8 & 1) * 16, FST_OFF + 16384 + (8 * t + s) * 32, SC1);
        else if (i8 < 70) ST32(on, (i8 & 1) * 4096, HF_OFF + t * 128 + s * 4, SC1);
        SBAR();
      }
#pragma unroll
      for (int i = 0; i < 2; ++i) {
        float sx = 0.f, qx = 0.f;
#pragma unroll
        for (int j = 0; j < 4; ++j) { const u32 p = xrow[i][j]; sx = dot2bf(p, 0x3f803f80u, sx); qx = dot2bf(p, p, qx); }
        *(LAS v4u*)(sm + L_XB + (w & 3) * 4096 + i * 1024 + l16) = xtile[i];
        const float r = row_sum16(swap16_add(swap32_add(sx, qx), swap32_add(sx, qx)));
        const auto rr = __builtin_amdgcn_permlane32_swap(__builtin_bit_cast(u32, r), __builtin_bit_cast(u32, r), false, false);
        const u32 rs0 = rr[0], rs1 = rr[1];
        float mm, vv, mb, rcs;
        ln_stats(__builtin_bit_cast(float, rs0), __builtin_bit_cast(float, rs1), mm, vv, mb, rcs);
        if (l < 2) ST32(__builtin_bit_cast(u32, l ? rcs : mb), l * 16, FST_OFF + (2 * b + ((w >> 1) & 1)) * 32 + (2 * (w & 1) + i) * 4, SC1);
      }
      {
        const LAS char* tp = sm + L_XB + (w & 3) * 4096 + (16 * kk + (i16 >> 2)) * 32 + (l & 3) * 8;
        const s16x4 t0 = __builtin_amdgcn_ds_read_tr16_b64_v4i16((LAS s16x4*)tp), t1 = __builtin_amdgcn_ds_read_tr16_b64_v4i16((LAS s16x4*)(tp + 128));
        const s16x4 t2 = __builtin_amdgcn_ds_read_tr16_b64_v4i16((LAS s16x4*)(tp + 256)), t3 = __builtin_amdgcn_ds_read_tr16_b64_v4i16((LAS s16x4*)(tp + 384));
        const int so = XC_OFF + (64 * (b & 7) + 16 * (w & 3)) * 4096 + (b >> 3) * 128;
        ST128(__builtin_bit_cast(v4u, __builtin_shufflevector(t0, t1, 0, 1, 2, 3, 4, 5, 6, 7)), i16 * 4096 + kk * 32, so, SC1);
        ST128(__builtin_bit_cast(v4u, __builtin_shufflevector(t2, t3, 0, 1, 2, 3, 4, 5, 6, 7)), i16 * 4096 + kk * 32 + 16, so, SC1);
      }
      if (b < 224) {
        LAS char* wl = sm + L_XB + (w & 3) * 4096;
#pragma unroll
        for (int f = 0; f < 4; ++f) *(LAS v4u*)(wl + f * 1024 + l16) = wr[f];
        const int hs = b < 112 ? 512 : 256, ts = b < 112 ? 256 : 512;
        const LAS char* rp = wl + (l >> 5) * hs + ((l & 15) >> 2) * 64 + 32 * ((l >> 4) & 1) + 8 * (l & 3);
        float cs = 0.f;
#pragma unroll
        for (int f = 0; f < 4; ++f) {
          const s16x4 t0 = __builtin_amdgcn_ds_read_tr16_b64_v4i16((LAS s16x4*)(rp + f * 1024)), t1 = __builtin_amdgcn_ds_read_tr16_b64_v4i16((LAS s16x4*)(rp + f * 1024 + ts));
          const v4u o = __builtin_bit_cast(v4u, __builtin_shufflevector(t0, t1, 0, 1, 2, 3, 4, 5, 6, 7));
#pragma unroll
          for (int j = 0; j < 4; ++j) { const u32 p = o[j]; cs = dot2bf(p, 0x3f803f80u, cs); }
          wr[f] = o;
          if (b < 112) ST128(o, l16 + f * 1024, W1F_OFF + fr * 1024, SC1);
        }
        cs = swap32_add(cs, cs);
        if (b < 112 && l < 32) ST32(__builtin_bit_cast(u32, cs), l * 4, CS_OFF + ((b & 1) * 4 + (w & 3)) * (FF * 4) + (b >> 1) * 128, SC1);
      }
      if (!(COMM && b == 255)) __builtin_amdgcn_s_setprio(1);
    }
    PHASE();
    const f32x2 bn = *(const LAS f32x2*)(sm + L_BND + (w >> 2) * 8);
    const float sc = k_scale(bn.x + bn.y, 1);
#pragma unroll
    for (int g = 0; g < 2; ++g) {
      v4u fa0, fa1, fa2, fbh, fbl;
#pragma unroll
      for (int j = 0; j < 4; ++j) {
        const LAS float* pq = (const LAS float*)(sm + L_PRM + w * 640 + g * 320 + kk * 80) + j;
        const float arl = pq[0], rh = pq[4], rl = pq[8], cr = pq[12] * sc, ci = pq[16] * sc;
        float br, bi, er, ei;
        cexp_p(arl, rh, rl, pB, br, bi);
        bi = -bi;
        const u32 hi = pack_f16(br, bi);
        const f16x2 hv = __builtin_bit_cast(f16x2, hi);
        fbh[j] = hi;
        fbl[j] = pack_f16(br - (float)hv.x, bi - (float)hv.y);
        cexp_p(arl, rh, rl, pA0, er, ei);
        fa0[j] = pack_f16(cr * er - ci * ei, cr * ei + ci * er);
        cexp_p(arl, rh, rl, pA1, er, ei);
        fa1[j] = pack_f16(cr * er - ci * ei, cr * ei + ci * er);
        cexp_p(arl, rh, rl, pA2, er, ei);
        fa2[j] = pack_f16(cr * er - ci * ei, cr * ei + ci * er);
        if (g == 0 && j == 2) {
          SBAR();
          DRAIN();
          u32 old = 0;
          if (l == 0) old = __hip_atomic_fetch_add((LAS u32*)(sm + L_AC), 1u, __ATOMIC_RELAXED, __HIP_MEMORY_SCOPE_WORKGROUP);
          if (__builtin_amdgcn_readfirstlane(old) == 7u) {
            const u32 q = LD32(0, CNT_OFF + 4096 + (b & 7) * 128, SC1);
            if (l == 0) { ST32(q, 0, CNT_OFF + b * 4, SC1); *(LAS u32*)(sm + L_AC + 8) = q; }
          }
          if ((COMM || w >= 4) && b >= 112 && b < 224) {
#pragma unroll
            for (int f = 0; f < 4; ++f) ST128(wr[f], l16 + f * 1024, W2F_OFF + fr * 1024, SC1);
          }
          if (!(COMM && b == 255)) __builtin_amdgcn_s_setprio(0);
          PHASE();
        }
      }
      acc0 = mfma16h(fa0, fbh, acc0); acc1 = mfma16h(fa1, fbh, acc1); acc2 = mfma16h(fa2, fbh, acc2);
      acc0 = mfma16h(fa0, fbl, acc0); acc1 = mfma16h(fa1, fbl, acc1); acc2 = mfma16h(fa2, fbl, acc2);
    }
    *(LAS f32x4*)(sm + L_R + w * 3072 + i16 * 64 + kk * 16) = acc0;
    *(LAS f32x4*)(sm + L_R + w * 3072 + 1024 + i16 * 64 + kk * 16) = acc1;
    *(LAS f32x4*)(sm + L_R + w * 3072 + 2048 + i16 * 64 + kk * 16) = acc2;
    { const int o = tid < 224 ? tid * 4 : L_AC + 32 - L_B1; *(LAS float*)(sm + L_B1 + o) = bb1; }
    *(LAS float*)(sm + L_B2 + l * 4) = bias2;
    asm volatile("" :: "v"(warm));
    if (COMM && b == 255) {
#pragma unroll 1
      for (int sp = 0; sp < SPIN_CAP; ++sp) {
        CBAR();
        const v4u d = LD128(l16, CNT_OFF, SC1);
        const u32 own = __hip_atomic_load((LAS u32*)(sm + L_AC), __ATOMIC_RELAXED, __HIP_MEMORY_SCOPE_WORKGROUP);
        if (__all(d[0] == hs_q && d[1] == hs_q && d[2] == hs_q && (l == 63 ? own == 8u : d[3] == hs_q))) break;
      }
      CBAR();
      const v4u cw = LD128(l16, CNT_OFF + 12288, SC1);
      ST128(cw, l16, CNT_OFF + 8192, SC1);
      __builtin_amdgcn_s_setprio(0);
      PHASE();
    }
    PHASE();
    __syncthreads();
    {
      const LAS float* pp = (const LAS float*)(sm + L_R + c * 12288) + x;
      LAS _Float16* kt = (LAS _Float16*)(sm + L_KT + c * KT_CH);
      kt[tix & 1023] = (_Float16)((pp[0] + pp[768]) + (pp[1536] + pp[2304]));
      kt[(tix >> 10) & 1023] = (_Float16)((pp[256] + pp[1024]) + (pp[1792] + pp[2560]));
      kt[tix >> 20] = (_Float16)((pp[512] + pp[1280]) + (pp[2048] + pp[2816]));
    }
    __syncthreads();
    PHASE();
    {
      u32 wd[8];
#pragma unroll
      for (int j = 0; j < 8; ++j)
        wd[j] = (u32)*(const LAS u16*)(sm + L_R + (gix[j] & 0xffff)) | ((u32)*(const LAS u16*)(sm + L_R + ((u32)gix[j] >> 16)) << 16);
#pragma unroll
      for (int cp = 0; cp < 8; ++cp) {
        const int sg = 7 - cp;
        v4u o;
#pragma unroll
        for (int k = 0; k < 4; ++k)
          o[k] = (sg & 1) ? __builtin_amdgcn_alignbit(wd[(sg + 1) / 2 + k], wd[(sg - 1) / 2 + k], 16) : wd[sg / 2 + k];
        *(LAS v4u*)(sm + c * KC_CH + cp * 4160 + (x + 1) * 16) = o;
      }
    }
    {
      const u32 on = (u32)opaque_v(-1);
      const v4u on4 = {on, on, on, on};
      ST128(on4, (tid >> 3) * 32768 + (tid & 7) * 16, XM_OFF + (2 << 20) + 128 * b, SC1);
      ST128(on4, ((tid >> 2) & 63) * 4096 + (tid >> 8) * 2048 + (tid & 3) * 16, XC_OFF + (2 << 20) + s * (64 * 4096) + t * 64, SC1);
    }
    PHASE();
    if (!COMM && w == 0) {
      u32 qp = *(const LAS u32*)(sm + L_AC + 8);
      hs_c = *(const LAS u32*)(sm + L_AC + 12);
#pragma unroll 1
      for (int sp = 0; sp < SPIN_CAP; ++sp) {
        CBAR();
        const u32 r = LD32(0, CNT_OFF + 8192 + b * 4, SC1), q = LD32(0, CNT_OFF + 4096 + (b & 7) * 128, SC1);
        if (__builtin_amdgcn_readfirstlane(r) == __builtin_amdgcn_readfirstlane(hs_c)) break;
        if (__builtin_amdgcn_readfirstlane(q) != __builtin_amdgcn_readfirstlane(qp)) { qp = q; if (l == 0) ST32(q, 0, CNT_OFF + b * 4, SC1); }
        __builtin_amdgcn_s_sleep(2);
      }
      CBAR();
      if (l == 0) __hip_atomic_store((LAS u32*)(sm + L_AC + 4), 1u, __ATOMIC_RELAXED, __HIP_MEMORY_SCOPE_WORKGROUP);
    }
#pragma unroll 1
    for (int sp = 0; sp < (SPIN_CAP << 4); ++sp) {
      if (__hip_atomic_load((LAS u32*)(sm + L_AC + 4), __ATOMIC_RELAXED, __HIP_MEMORY_SCOPE_WORKGROUP) != 0u) break;
      __builtin_amdgcn_s_sleep(1);
    }
    CBAR();
    PHASE();
    {
      const v2u xa = LD64(cc * 4096 + q8 * 8, XC_OFF + b * 8192, SC1), xb = LD64(cc * 4096 + q8 * 8 + 2048, XC_OFF + b * 8192, SC1);
      const v4u m0 = LD128(q8 * 32, FST_OFF, SC1), r0 = LD128(q8 * 32 + 16, FST_OFF, SC1);
      const v4u m1 = LD128(q8 * 32, FST_OFF + 8192, SC1), r1 = LD128(q8 * 32 + 16, FST_OFF + 8192, SC1);
      SBAR();
      if constexpr (!COMM) LOAD_W(0, 8);
      xr0 = conv_ln(sm + L_U + cc * U_CH + 512 + q8 * 8, xa, m0, r0, dk);
      xr1 = conv_ln(sm + L_U + cc * U_CH + 2560 + q8 * 8, xb, m1, r1, dk);
      if ((tid & ~8) == 0) { const f32x2 bc = *(const LAS f32x2*)(sm + L_BND + cc * 8); *(LAS f32x2*)(sm + L_DK + cc * 8) = f32x2{dk, k_scale(bc.x + bc.y, -1)}; }
      SBAR();
#pragma unroll
      for (int i = (COMM ? 0 : 8); i < (COMM ? 28 : 16); ++i) asm volatile("" : "=v"(wf[i]));
    }
  }

#pragma unroll 1
  for (int ly = 0; ly < 2; ++ly) {
    const int b = opaque_s(b0), w = opaque_s(w0);
    const int t = MAP_T(b), s = MAP_S(b);
    int tid = opaque_v(threadIdx.x), l = tid & 63, l16 = l * 16;
    const int xmo = XM_OFF + ly * (2 << 20) + t * 32768 + w * 1024;
    const int w2off = W2F_OFF + ((s * 8 + w) * 28) * 1024;
    const int hoff = H_OFF + ly * H_LAYER + (t * 8 + s) * (28 * 1024) + (2 * w) * 1024;
    v4u xs[4];
    auto x_load = [&](int half) {
#pragma unroll
      for (int k = 0; k < 4; ++k) xs[k] = LD128(l16, xmo + half * (1 << 20) + k * 8192, SC1);
    };
    auto x_ok = [&]() { return __all(umax(umax(max4(xs[0]), max4(xs[1])), umax(max4(xs[2]), max4(xs[3]))) != 0xffffffffu); };
    auto x_stage = [&](int half) {
      if (!x_ok()) {
        int sp = 0;
#pragma unroll 1
        do {
          PHASE();
          __builtin_amdgcn_s_sleep(4);
          CBAR();
          x_load(half);
        } while (!x_ok() && ++sp < SPIN_CAP);
      }
#pragma unroll
      for (int k = 0; k < 4; ++k) *(LAS v4u*)(sm + L_R + half * 32768 + k * 8192 + tid * 16) = xs[k];
    };
    v2u pg;
    bool fin0 = true;
    auto fin_load = [&](int hf) { pg = LD64(l * 8, PST_OFF + ((t * 2 + hf) * 8 + s) * 512, SC1); };
    auto fin_try = [&](int hf) -> bool {
      if (!__all(pg[1] == 1u)) return false;
      const u32 e0 = pg[0];
      float v = __builtin_bit_cast(float, e0);
      v = swap32_add(v, v); v = swap16_add(v, v); v = dpp_add<0x128>(v);
      const float o = __builtin_bit_cast(float, __builtin_amdgcn_update_dpp(0, __builtin_bit_cast(int, v), 0xb1, 0xf, 0xf, false));
      float mm, vr, mb, rcs;
      ln_stats((l & 1) ? o : v, (l & 1) ? v : o, mm, vr, mb, rcs);
      if (l < 8) ST32(npz((l & 1) ? rcs : mb), (l & 1) * 16 + (l >> 1) * 4, FST_OFF + 16384 + hf * 8192 + (8 * t + s) * 32, SC1);
      return true;
    };
    auto fin_wait = [&](int hf) {
#pragma unroll 1
      for (int sp = 0; sp < SPIN_CAP; ++sp) {
        CBAR();
        fin_load(hf);
        if (fin_try(hf)) break;
        __builtin_amdgcn_s_sleep(1);
      }
    };
    v2u ix; v4u im, ir;
    auto cin_load = [&](int hf) {
      const int cc = (tid >> 3) & 1, q8 = (tid >> 4) * 8 + (tid & 7);
      ix = LD64(cc * 4096 + q8 * 8, XC_OFF + (2 << 20) + b * 8192 + hf * 2048, SC1);
      im = LD128(q8 * 32, FST_OFF + 16384 + hf * 8192, SC1); ir = LD128(q8 * 32 + 16, FST_OFF + 16384 + hf * 8192, SC1);
    };
    auto cin_ok = [&]() { return __all(umax(umax(ix[0], ix[1]), umax(max4(im), max4(ir))) != 0xffffffffu); };
    auto cin_ln = [&](int hf) -> f32x4 {
      if (!cin_ok()) {
        int sp = 0;
#pragma unroll 1
        do {
          PHASE();
          __builtin_amdgcn_s_sleep(4);
          CBAR();
          cin_load(hf);
        } while (!cin_ok() && ++sp < SPIN_CAP);
      }
      const int cc = (tid >> 3) & 1, q8 = (tid >> 4) * 8 + (tid & 7);
      return conv_ln(sm + L_U + cc * U_CH + 512 + hf * 2048 + q8 * 8, ix, im, ir, *(const LAS float*)(sm + L_DK + cc * 8));
    };
    CW(16); CW(17); CW(18); CW(19);
    PHASE();
    __syncthreads();
    PHASE();
    {
      const int c16 = l & 15, g4 = l >> 4;
      const LAS char* pa = sm + (w >> 2) * KC_CH + (c16 & 7) * 4160 + (3 + (c16 >> 3) - g4) * 16 + (w & 3) * 128;
      const LAS char* pb = sm + L_U + (w >> 2) * U_CH + (30 + 2 * c16 + g4) * 16 - (w & 3) * 128;
      LAS char* pp = sm + L_R + w * 4096 + (w >> 2) * 128 + ((4 * c16 + g4) ^ ((c16 >> 1) & 3)) * 16;
      const int xmv = (tid >> 4) * 32768 + (tid & 15) * 8;
      const LAS char* pr = sm + L_R + ((tid >> 3) & 1) * 16512 + ((((tid >> 4) << 3) | (tid & 7)) ^ ((tid >> 4) & 3)) * 16;
      auto conv_out = [&](int half, f32x4 xr) {
        const LAS char* p = pr + (half ? L_PB - L_R : 0);
        const float ksc = *(const LAS float*)(sm + L_DK + ((tid >> 3) & 1) * 8 + 4);
        const f32x4 y = (*(const LAS f32x4*)p + *(const LAS f32x4*)(p + 4096)) + (*(const LAS f32x4*)(p + 8192) + *(const LAS f32x4*)(p + 12288));
        const f32x4 o = y * ksc + xr;
        ST64((v2u{umin(pack_bf16(o[0], o[1]), 0xfffffffeu), umin(pack_bf16(o[2], o[3]), 0xfffffffeu)}), xmv,
             XM_OFF + ly * (2 << 20) + half * (1 << 20) + 128 * b, SC1);
      };
      {
        f32x4 acc[4];
#pragma unroll
        for (int i = 0; i < 4; ++i) acc[i] = f32x4{0.f, 0.f, 0.f, 0.f};
        v4u fa[2][4], fb[2][4];
#pragma unroll
        for (int tau = 0; tau < 2; ++tau)
#pragma unroll
          for (int i = 0; i < 4; ++i) { fa[tau][i] = *(const LAS v4u*)(pa + i * 512 + tau * 64); fb[tau][i] = *(const LAS v4u*)(pb + i * 512 - tau * 64); }
#pragma unroll
        for (int tau = 0; tau < 2; ++tau)
#pragma unroll
          for (int sx = 0; sx < 4; ++sx) {
#pragma unroll
            for (int e = 0; e + sx < 4; ++e) acc[sx + e] = mfma16h(fa[tau][sx], fb[tau][e], acc[sx + e]);
            if (sx == 0) { SBAR(); CW(20 + tau); }
          }
#pragma unroll
        for (int i = 0; i < 4; ++i) *(LAS f32x4*)(pp + i * 1024) = acc[i];
      }
      v4u fa[8], fb[4];
#pragma unroll
      for (int i = 0; i < 4; ++i) { fb[i] = *(const LAS v4u*)(pb + i * 512); fa[7 - i] = *(const LAS v4u*)(pa + (7 - i) * 512); }
#pragma unroll
      for (int i = 1; i < 4; ++i) fa[i] = *(const LAS v4u*)(pa + i * 512);
      SBAR();
      CW(22); CW(23);
      PHASE();
      __syncthreads();
      conv_out(0, xr0);
      PHASE();
      if (ly == 0) {
        if constexpr (!COMM) LOAD_W(8, 16);
        if constexpr (COMM) {
          f32x4 cs = {0.f, 0.f, 0.f, 0.f};
#pragma unroll
          for (int k = 0; k < 8; ++k) cs += __builtin_bit_cast(f32x4, LD128(l16, CS_OFF + k * (FF * 4) + s * 896, SC1));
          if (l < 56) *(LAS f32x4*)(sm + L_CS + l16) = cs;
        }
      }
      {
        f32x4 acc[4];
#pragma unroll
        for (int i = 0; i < 4; ++i) acc[i] = f32x4{0.f, 0.f, 0.f, 0.f};
#pragma unroll
        for (int e = 0; e < 4; ++e) {
#pragma unroll
          for (int i = 0; i < 4; ++i) acc[i] = mfma16h(fa[4 + i - e], fb[e], acc[i]);
          fb[e] = *(const LAS v4u*)(pb + e * 512 - 64); fa[7 - e] = *(const LAS v4u*)(pa + (7 - e) * 512 + 64);
          SBAR();
          if (e & 1) CW(24 + (e >> 1));
        }
#pragma unroll
        for (int i = 3; i >= 1; --i) fa[i] = *(const LAS v4u*)(pa + i * 512 + 64);
        if (ly) cin_load(1);
        else { asm volatile("" : "=v"(ix), "=v"(im), "=v"(ir)); }
        SBAR();
#pragma unroll
        for (int e = 0; e < 4; ++e) {
#pragma unroll
          for (int i = 0; i < 4; ++i) acc[i] = mfma16h(fa[4 + i - e], fb[e], acc[i]);
          SBAR();
          if (e & 1) CW(26 + (e >> 1));
        }
#pragma unroll
        for (int i = 0; i < 4; ++i) { fa[i] = *(const LAS v4u*)(pa + i * 512); fa[4 + i] = *(const LAS v4u*)(pa + i * 512 + 64); }
        PHASE();
        if (ly) xr1 = cin_ln(1);
        SBAR();
        CW(28); CW(29);
        PHASE();
        __syncthreads();
        v4u fc[8];
#pragma unroll
        for (int i = 0; i < 4; ++i) { fc[i] = *(const LAS v4u*)(pb + (4 + i) * 512); fc[4 + i] = *(const LAS v4u*)(pb + (4 + i) * 512 - 64); }
#pragma unroll
        for (int tau = 0; tau < 2; ++tau)
#pragma unroll
          for (int e = 0; e < 4; ++e) {
#pragma unroll
            for (int i = e; i < 4; ++i) acc[i] = mfma16h(fa[4 * tau + i - e], fc[4 * tau + e], acc[i]);
            if (e == 0) { SBAR(); CW(30 + tau); }
          }
#pragma unroll
        for (int i = 0; i < 4; ++i) *(LAS f32x4*)(pp + (L_PB - L_R) + i * 1024) = acc[i];
      }
      PHASE();
      __syncthreads();
      x_load(0);
      SBAR();
      conv_out(1, xr1);
      PHASE();
      x_stage(0);
      SBAR();
      if constexpr (!COMM) {
        const u32 on = (u32)opaque_v(-1);
#pragma unroll
        for (int k = 0; k < 4; ++k) ST128((v4u{on, on, on, on}), l16 + (k & 1) * 1024, hoff + (k >> 1) * 14336, SC1);
        SBAR();
      }
    }
    PHASE();
    __syncthreads();
    tid = opaque_v(threadIdx.x); l = tid & 63; l16 = l * 16;
    const int h = l >> 5;
    const LAS char* xb = sm + L_R + (8 * h + ((l & 15) >> 2)) * 64 + 32 * ((l >> 4) & 1) + 8 * (l & 3);
    auto stats = [&](int half) {
      const LAS char* xq = sm + L_R + half * 32768 + (8 * (l >> 4) + ((l & 15) >> 2)) * 64 + 8 * (l & 3);
      const u32 one = (u32)opaque_v(0x3f803f80);
      const v4u ones = {one, one, one, one};
      f32x4 s0 = {0.f, 0.f, 0.f, 0.f}, q0 = s0, s1 = s0, q1 = s0;
      v4u f0[4], f1[4];
#pragma unroll
      for (int k = 0; k < 4; ++k) { f0[k] = tr_frag(xq + k * 2048); f1[k] = tr_frag(xq + k * 2048 + 32); }
#pragma unroll
      for (int k = 0; k < 16; ++k) {
        s0 = mfma16b(ones, f0[k & 3], s0); q0 = mfma16b(f0[k & 3], f0[k & 3], q0);
        s1 = mfma16b(ones, f1[k & 3], s1); q1 = mfma16b(f1[k & 3], f1[k & 3], q1);
        if (k + 4 < 16) { f0[k & 3] = tr_frag(xq + (k + 4) * 2048); f1[k & 3] = tr_frag(xq + (k + 4) * 2048 + 32); }
        if (half == 0 && k == 9) x_load(1);
        SBAR();
      }
      const float d0 = (l & 2) ? ((l & 1) ? q0[3] : q0[2]) : ((l & 1) ? q0[1] : q0[0]);
      const float d1 = (l & 2) ? ((l & 1) ? q1[3] : q1[2]) : ((l & 1) ? q1[1] : q1[0]);
#pragma unroll
      for (int g = 0; g < 2; ++g) {
        float m, v, mb, r1;
        ln_stats(g ? s1[0] : s0[0], g ? d1 : d0, m, v, mb, r1);
        const u32 p2 = pack_bf16((m - mb) * r1, v * r1 * r1);
        const float r2 = __builtin_amdgcn_rcpf(bfr(__builtin_amdgcn_sqrtf(bfr(bfhi(p2) + 1e-5f))));
        if ((l >> 4) == ((l >> 2) & 3)) *(LAS f32x2*)(sm + L_AB + (half * 32 + 16 * g + (l & 15)) * 8) = f32x2{r1 * r2, (mb * r1 + bflo(p2)) * r2};
      }
    };
    auto epi_quad = [&](const f32x16& acc, int q, f32x2 ab, u32& o0, u32& o1) {
      const f32x4 c4 = *(const LAS f32x4*)(sm + L_CS + (32 * w + 8 * q + 4 * h) * 4);
      const f32x4 b4 = *(const LAS f32x4*)(sm + L_B1 + (32 * w + 8 * q + 4 * h) * 4);
      const f32x4 x = f32x4{acc[4 * q], acc[4 * q + 1], acc[4 * q + 2], acc[4 * q + 3]} * ab.x + (b4 - c4 * ab.y);
      const f32x2 g0 = gelu2(f32x2{x[0], x[1]}), g1 = gelu2(f32x2{x[2], x[3]});
      o0 = umin(pack_bf16(g0.x, g0.y), 0xfffffffeu); o1 = umin(pack_bf16(g1.x, g1.y), 0xfffffffeu);
    };
    f32x16 acc0, acc1;
    constexpr int XD = 3;
    if constexpr (!COMM) {
      v4u xf[XD];
#pragma unroll
      for (int k = 0; k < XD; ++k) xf[k] = tr_frag(xb + k * 1024);
      acc0 = f32x16{};
#pragma unroll
      for (int ks = 0; ks < 32; ++ks) {
        acc0 = mfma32(wf[ks], xf[ks % XD], acc0);
        if (ks + XD < 32) xf[ks % XD] = tr_frag(xb + (ks + XD) * 1024);
        if (ks < 2 * W2A && !(ks & 1)) { const int k = ks >> 1; wa[k] = LD128(l16 + (k & 3) * 1024, w2off + (k >> 2) * 4096, SC1); }
        if (ks == 15) x_load(1);
        SBAR();
      }
      asm volatile("" : "+v"(acc0));
    } else {
      __builtin_amdgcn_s_setprio(2);
      stats(0);
      __builtin_amdgcn_s_setprio(0);
    }
    PHASE();
    x_stage(1);
    PHASE();
    __syncthreads();
    PHASE();
    const int orow = ly ? l : 8 * w + (l >> 3), oq = ly ? w : (l & 7);
    const v2u xres0 = *(const LAS v2u*)(sm + L_R + (64 * s + orow) * 64 + 8 * oq);
    const v2u xres1 = *(const LAS v2u*)(sm + L_XB + (64 * s + orow) * 64 + 8 * oq);
    const int hfo = HF_OFF + ly * 4096 + t * 128;
    if constexpr (!COMM) {
      v4u xf[XD];
#pragma unroll
      for (int k = 0; k < XD; ++k) xf[k] = tr_frag(xb + 32768 + k * 1024);
      const f32x2 ab = *(const LAS f32x2*)(sm + L_AB + (l & 31) * 8);
      u32 o[4];
      acc1 = f32x16{};
#pragma unroll
      for (int ks = 0; ks < 32; ++ks) {
        acc1 = mfma32(wf[ks], xf[ks % XD], acc1);
        if (ks + XD < 32) xf[ks % XD] = tr_frag(xb + 32768 + (ks + XD) * 1024);
        if (ks & 1) { const int j = ks >> 1, k = W2A + j; wf[j] = LD128(l16 + (k & 3) * 1024, w2off + (k >> 2) * 4096, SC1); }
        if (ks % 5 == 1 && ks < 20) { const int q = ks / 5; epi_quad(acc0, q, ab, o[2 * (q & 1)], o[2 * (q & 1) + 1]); }
        if (ks == 9 || ks == 19) ST128((v4u{o[0], o[1], o[2], o[3]}), l16 + (ks / 10) * 1024, hoff, SC1);
        if (ks % 5 == 4 || ks >= 20) SBAR();
      }
      asm volatile("" : "+v"(acc1));
    } else {
      if (l == 0) ST32(1u, 0, hfo + s * 4, SC1);
      if (ly == 0) LOAD_W(0, 28);
      __builtin_amdgcn_s_setprio(2);
      stats(1);
      __builtin_amdgcn_s_setprio(0);
#pragma unroll 1
      for (int sp = 0; sp < SPIN_CAP; ++sp) {
        CBAR();
        const u32 f = LD32((l & 7) * 4, hfo, SC1);
        if (__all(f == 1u)) break;
        __builtin_amdgcn_s_sleep(1);
      }
      CBAR();
    }
    PHASE();
    __syncthreads();
    v4u hf[14];
    const int hin = H_OFF + ly * H_LAYER + (t * 8 + w) * (28 * 1024);
    auto h_load = [&](int half, int lo, int hi) {
#pragma unroll
      for (int k = lo; k < hi; ++k) { hf[k] = LD128(l16, hin + (14 * half + k) * 1024, SC1); SBAR(); }
    };
    h_load(0, 0, 4);
    PHASE();
    if constexpr (!COMM) {
      const f32x2 ab = *(const LAS f32x2*)(sm + L_AB + (32 + (l & 31)) * 8);
#pragma unroll
      for (int sp = 0; sp < 2; ++sp) {
        u32 o[4];
        epi_quad(acc1, 2 * sp, ab, o[0], o[1]);
        epi_quad(acc1, 2 * sp + 1, ab, o[2], o[3]);
        ST128((v4u{o[0], o[1], o[2], o[3]}), l16 + sp * 1024, hoff + 14336, SC1);
      }
      SBAR();
    }
    h_load(0, 4, 14);
    PHASE();
    LAS char* rbw = sm + L_R + (w * 64 + (l & 31)) * RB_ROW + h * 16;
    auto g2_out = [&](int half, v2u xres) {
      const LAS char* rp = sm + L_R + orow * RB_ROW + oq * 16;
      f32x4 o4 = unpack4(xres) + *(const LAS float*)(sm + L_B2 + orow * 4);
      f32x4 p4 = *(const LAS f32x4*)rp;
#pragma unroll
      for (int ww = 1; ww < 8; ++ww) { const f32x4 v = *(const LAS f32x4*)(rp + ww * (64 * RB_ROW)); if (ww & 1) o4 += v; else p4 += v; }
      o4 += p4;
      u32 o0 = pack_bf16(o4[0], o4[1]), o1 = pack_bf16(o4[2], o4[3]);
      if (ly) {
        u16* op = out + (1024 * half + 32 * t + 4 * w) * DM + 64 * s + l;
        op[0] = (u16)(o0 & 0xffffu); op[DM] = (u16)(o0 >> 16); op[2 * DM] = (u16)(o1 & 0xffffu); op[3 * DM] = (u16)(o1 >> 16);
      } else {
        o0 = umin(o0, 0xfffffffeu); o1 = umin(o1, 0xfffffffeu);
        ST64((v2u{o0, o1}), (tid >> 3) * 4096 + (tid & 7) * 8, XC_OFF + (2 << 20) + s * (64 * 4096) + t * 64 + half * 2048, SC1);
        const float x0 = bflo(o0), x1 = bfhi(o0), x2 = bflo(o1), x3 = bfhi(o1);
        const float a0 = swap32_add(x0, x2), a1 = swap32_add(x0 * x0, x2 * x2), a2 = swap32_add(x1, x3), a3 = swap32_add(x1 * x1, x3 * x3);
        const float sv = dpp_add<0x128>(swap16_add(a0, a2)), qv = dpp_add<0x128>(swap16_add(a1, a3));
        if ((l & 8) == 0) *(LAS f32x2*)(sm + L_SP + ((half * 8 + w) * 32 + 4 * (l & 7) + (l >> 4)) * 8) = f32x2{sv, qv};
      }
    };
    auto pst_out = [&](int half) {
      float v = 0.f;
#pragma unroll
      for (int k = 0; k < 8; ++k) v += *(const LAS float*)(sm + L_SP + (half * 8 + k) * 256 + l * 4);
      ST64((v2u{__builtin_bit_cast(u32, v), (u32)opaque_v(1)}), (l >> 3) * 512 + (l & 7) * 8, PST_OFF + (t * 2 + half) * 4096 + s * 64, SC1);
    };
#define W2FRAG(k) (COMM ? wf[k] : (k) < W2A ? wa[(k) < W2A ? (k) : 0] : wf[(k) < W2A ? 0 : (k) - W2A])
    auto gemm2 = [&](int half) {
      f32x16 a0, a1;
#pragma unroll 1
      for (int sp = 0; sp < SPIN_CAP; ++sp) {
        a0 = f32x16{}; a1 = f32x16{};
        u32 mx = 0;
#pragma unroll
        for (int k = 0; k < 14; ++k) {
          a0 = mfma32(hf[k], W2FRAG(2 * k), a0); a1 = mfma32(hf[k], W2FRAG(2 * k + 1), a1);
          mx = umax(mx, max4(hf[k]));
        }
        if (__all(mx != 0xffffffffu)) break;
        PHASE();
        __builtin_amdgcn_s_sleep(2);
        CBAR();
        h_load(half, 0, 14);
      }
      PHASE();
      if (!COMM && half == 0) h_load(1, 0, 14);
      PHASE();
#pragma unroll
      for (int q = 0; q < 4; ++q) {
        *(LAS f32x4*)(rbw + q * 32) = f32x4{a0[4 * q], a0[4 * q + 1], a0[4 * q + 2], a0[4 * q + 3]};
        *(LAS f32x4*)(rbw + 32 * RB_ROW + q * 32) = f32x4{a1[4 * q], a1[4 * q + 1], a1[4 * q + 2], a1[4 * q + 3]};
      }
      if constexpr (!COMM) {
        if (half == 1) {
          SBAR();
          if (ly == 0) {
            if (w == 0) {
#pragma unroll 1
              for (int sp = 0; sp < 3; ++sp) { CBAR(); fin_load(0); fin0 = fin_try(0); if (fin0) break; }
            }
            LOAD_W(0, 16);
          } else {
#pragma unroll
            for (int i = 0; i < 16; ++i) asm volatile("" : "=v"(wf[i]));
          }
        }
      }
    };
    gemm2(0);
    __syncthreads();
    if constexpr (COMM) h_load(1, 0, 14);
    PHASE();
    g2_out(0, xres0);
    PHASE();
    __syncthreads();
    if (COMM && ly == 0) pst_out(0);
    gemm2(1);
    __syncthreads();
    PHASE();
    g2_out(1, xres1);
    PHASE();
    if (ly == 0) {
      cin_load(0);
      __syncthreads();
      if constexpr (COMM) { pst_out(1); fin_wait(1); }
      else if (!fin0) fin_wait(0);
      PHASE();
      xr0 = cin_ln(0);
      PHASE();
    } else {
      asm volatile("" : "=v"(xr0));
    }
    asm volatile("" : "=v"(xr1));
  }
}

__global__ __launch_bounds__(512, 2) void k_main(const Args a) {
  extern __shared__ __attribute__((aligned(16))) char smem[];
  LAS char* sm = (LAS char*)smem;
  const int b0 = blockIdx.x, w0 = __builtin_amdgcn_readfirstlane(threadIdx.x >> 6);
  if (w0 == 7) main_body<true>(sm, a, b0, w0);
  else main_body<false>(sm, a, b0, w0);
}

extern "C" void kernel_launch(void* const* d_in, const int* in_sizes, int n_in, void* d_out, int out_size, void* d_ws,
                              size_t ws_size, hipStream_t stream) {
  (void)hipFuncSetAttribute((const void*)k_main, hipFuncAttributeMaxDynamicSharedMemorySize, MAIN_LDS);
  Args a;
  a.x0 = (const u16*)d_in[0]; a.logA = (const u16*)d_in[1]; a.Aim = (const u16*)d_in[2]; a.Cre = (const u16*)d_in[3];
  a.Cim = (const u16*)d_in[4]; a.logdt = (const u16*)d_in[5]; a.dskip = (const u16*)d_in[6]; a.w1 = (const u16*)d_in[8];
  a.b1 = (const u16*)d_in[9]; a.w2 = (const u16*)d_in[10]; a.b2 = (const u16*)d_in[11];
  a.ws = (char*)d_ws; a.out = (u16*)d_out;
  k_main<<<256, 512, MAIN_LDS, stream>>>(a);
}
```
